```python
import torch

def _softmax(x, axis=-1):
    return torch.softmax(x, dim=axis)
def _gelu(x):
    return torch.nn.functional.gelu(x, approximate="tanh")
def _silu(x):
    return torch.nn.functional.silu(x)
def _sigmoid(x):
    return torch.sigmoid(x)
def _layernorm(x, axis=-1, eps=1e-5):
    m = x.mean(dim=axis, keepdim=True)
    v = x.var(dim=axis, keepdim=True, unbiased=False)
    return (x - m) / torch.sqrt(v + eps)
def _scatter_add(tgt, idx, src, axis=0):
    return tgt.clone().index_add_(axis, idx, src)
def _segment_sum(src, seg, n):
    out = torch.zeros((n,) + src.shape[1:], dtype=src.dtype)
    return out.index_add_(0, seg, src)
def _pad(x, pw):
    flat = tuple(v for pair in reversed(pw) for v in pair)
    return torch.nn.functional.pad(x, flat)

kernel_name = 'sequential_preLN__gqa_block_cached__swiglu_ffn__decode'

def setup_inputs():

    _w = (lambda a: (lambda t: t.to(torch.bfloat16) if t.is_floating_point() else t)(torch.as_tensor(a)))
    def _A_setup():
        B, Sq, D = 2, 1, 512
        Hq, Hkv, Sk, Dh = 32, 8, 32768, 128
        assert Hq % Hkv == 0, (Hq, Hkv)
        return {
            "x": _w((lambda *s: torch.randn(*s, dtype=torch.bfloat16) * 0.05)(B, Sq, D)),
            "k_cache": _w((lambda *s: torch.randn(*s, dtype=torch.bfloat16) * 3.2)(B, Hkv, Sk, Dh)),
            "v_cache": _w((lambda *s: torch.randn(*s, dtype=torch.bfloat16) * 0.4)(B, Hkv, Sk, Dh)),
            "wq": _w((lambda *s: torch.randn(*s, dtype=torch.bfloat16) * 3.2)(D, Hq * Dh) * (D ** -0.5)),
            "wo": _w((lambda *s: torch.randn(*s, dtype=torch.bfloat16) * 0.4)(Hq * Dh, D) * ((Hq * Dh) ** -0.5)),
        }
    _A_in = _A_setup()

    def _B_setup():
        B, S, D, F = 2, 1, 512, 1792
        return {
            "x": _w((lambda *s: torch.randn(*s, dtype=torch.bfloat16) * 0.05)(B, S, D)),
            "w_gate": _w((lambda *s: torch.randn(*s, dtype=torch.bfloat16) * 0.05)(D, F)),
            "w_up": _w((lambda *s: torch.randn(*s, dtype=torch.bfloat16) * 0.05)(D, F)),
            "w_down": _w((lambda *s: torch.randn(*s, dtype=torch.bfloat16) * 0.05)(F, D)),
        }
    _B_in = _B_setup()

    return {
        "A_x": _A_in["x"],
        "A_k_cache": _A_in["k_cache"],
        "A_v_cache": _A_in["v_cache"],
        "A_wq": _A_in["wq"],
        "A_wo": _A_in["wo"],
        "B_x": _B_in["x"],
        "B_w_gate": _B_in["w_gate"],
        "B_w_up": _B_in["w_up"],
        "B_w_down": _B_in["w_down"],
    }

def reference(A_x, A_k_cache, A_v_cache, A_wq, A_wo, B_x, B_w_gate, B_w_up, B_w_down):
    def _A_ref(x, k_cache, v_cache, wq, wo):
        B, Sq, D = x.shape
        Hq, Dh = 32, 128
        Hkv = k_cache.shape[1]
        rep = Hq // Hkv
        q = (lambda x, *d: x.permute(*d))((x @ wq).reshape(B, Sq, Hq, Dh), 0, 2, 1, 3)
        k = (lambda x, n, axis: torch.repeat_interleave(x, n, dim=axis))(k_cache, rep, axis=1)
        v = (lambda x, n, axis: torch.repeat_interleave(x, n, dim=axis))(v_cache, rep, axis=1)
        scores = torch.einsum("bhqd,bhkd->bhqk", q, k) * (Dh ** -0.5)
        p = _softmax(scores, axis=-1)
        o = torch.einsum("bhqk,bhkd->bhqd", p, v)
        return (lambda x, *d: x.permute(*d))(o, 0, 2, 1, 3).reshape(B, Sq, Hq * Dh) @ wo

    def _B_ref(x, w_gate, w_up, w_down):
        g = _silu(x @ w_gate)
        u = x @ w_up
        return (g * u) @ w_down

    x = A_x
    h = x + _A_ref(_layernorm(x, axis=-1), A_k_cache, A_v_cache, A_wq, A_wo)
    h2 = h + _B_ref(_layernorm(h, axis=-1), B_w_gate, B_w_up, B_w_down)
    return h2
```

```cpp
#include <hip/hip_runtime.h>
#include <hip/hip_bf16.h>
#include <cstdint>
#include <cstdio>

#define MIMIC 1

typedef unsigned short u16;
typedef unsigned v4u __attribute__((ext_vector_type(4)));
typedef unsigned v2u __attribute__((ext_vector_type(2)));
typedef float f32x4 __attribute__((ext_vector_type(4)));
typedef short bf16x8 __attribute__((ext_vector_type(8)));
typedef short bf16x4 __attribute__((ext_vector_type(4)));
typedef __bf16 bf16x8_t __attribute__((ext_vector_type(8)));
typedef __bf16 bf16x2_t __attribute__((ext_vector_type(2)));

constexpr int B = 2, D = 512, HQ = 32, HKV = 8, SK = 32768, DH = 128, F = 1792;
constexpr int NSPLIT = 16, KEYS_PER_WG = SK / NSPLIT;
constexpr int NW = 8;
constexpr int KEYS_PER_WAVE = KEYS_PER_WG / NW;

__device__ __forceinline__ float bf2f(u16 v) { return __uint_as_float((unsigned)v << 16); }
__device__ __forceinline__ float bflo(unsigned v) { return __uint_as_float(v << 16); }
__device__ __forceinline__ float bfhi(unsigned v) { return __uint_as_float(v & 0xffff0000u); }
__device__ __forceinline__ unsigned cvtpk(float lo, float hi) {
  typedef float f32x2 __attribute__((ext_vector_type(2)));
  return __builtin_bit_cast(unsigned, __builtin_convertvector(f32x2{lo, hi}, bf16x2_t));
}
__device__ __forceinline__ float rbf(float x) { return bflo(cvtpk(x, x)); }
__device__ __forceinline__ float mrb(float x) { return MIMIC ? rbf(x) : x; }

__device__ __forceinline__ float wave_sum(float v) {
#pragma unroll
  for (int o = 32; o >= 1; o >>= 1) v += __shfl_xor(v, o);
  return v;
}

__device__ __forceinline__ void ln512(float (&x)[8]) {
  float s = 0.f;
#pragma unroll
  for (int i = 0; i < 8; ++i) s += x[i];
  s = wave_sum(s);
  const float mean = s * (1.0f / D);
  float vs = 0.f;
#pragma unroll
  for (int i = 0; i < 8; ++i) { const float d = x[i] - mean; vs += d * d; }
  vs = wave_sum(vs);
  const float var = vs * (1.0f / D);
  const float mb = mrb(mean);
  const float sd = mrb(sqrtf(mrb(mrb(var) + 1e-5f)));
#pragma unroll
  for (int i = 0; i < 8; ++i) x[i] = mrb(mrb(x[i] - mb) / sd);
}

template <int K, int N>
__device__ __forceinline__ void gemv32(const float* xs  , const u16* W, int n0, float* red  , float (&res)[2]) {
  const int t = threadIdx.x, cg = t & 3, rg = t >> 2;
  float a0[8], a1[8];
#pragma unroll
  for (int i = 0; i < 8; ++i) a0[i] = a1[i] = 0.f;
  for (int k = rg; k < K; k += 64) {
    const v4u w = *(const v4u*)(W + (size_t)k * N + n0 + cg * 8);
    const float x0 = xs[k], x1 = xs[K + k];
#pragma unroll
    for (int i = 0; i < 4; ++i) {
      a0[2 * i] += x0 * bflo(w[i]); a0[2 * i + 1] += x0 * bfhi(w[i]);
      a1[2 * i] += x1 * bflo(w[i]); a1[2 * i + 1] += x1 * bfhi(w[i]);
    }
  }
#pragma unroll
  for (int i = 0; i < 8; ++i) { red[(rg * 2 + 0) * 32 + cg * 8 + i] = a0[i]; red[(rg * 2 + 1) * 32 + cg * 8 + i] = a1[i]; }
  __syncthreads();
  float s = 0.f;
  if (t < 64) { for (int r = 0; r < 64; ++r) s += red[r * 64 + t]; }
  res[0] = s;
  __syncthreads();
}

__global__ void __launch_bounds__(256) k_q(const u16* x, const u16* wq, u16* q) {
  __shared__ float xs[2 * D];
  __shared__ float red[64 * 64];
  const int t = threadIdx.x, w = t >> 6, lane = t & 63;
  if (w < 2) {
    float v[8];
    const v4u xv = *(const v4u*)(x + w * D + lane * 8);
#pragma unroll
    for (int i = 0; i < 4; ++i) { v[2 * i] = bflo(xv[i]); v[2 * i + 1] = bfhi(xv[i]); }
    ln512(v);
#pragma unroll
    for (int i = 0; i < 8; ++i) xs[w * D + lane * 8 + i] = v[i];
  }
  __syncthreads();
  float r[2];
  gemv32<D, HQ * DH>(xs, wq, blockIdx.x * 32, red, r);
  if (t < 64) q[(t >> 5) * (HQ * DH) + blockIdx.x * 32 + (t & 31)] = (u16)(cvtpk(r[0], r[0]) & 0xffff);
}

__device__ __forceinline__ f32x4 mfma32(v4u a, v4u b, f32x4 c) {
  return __builtin_amdgcn_mfma_f32_16x16x32_bf16(__builtin_bit_cast(bf16x8_t, a), __builtin_bit_cast(bf16x8_t, b), c, 0, 0, 0);
}
__device__ __forceinline__ f32x4 mfma16(v2u a, v2u b, f32x4 c) {
  return __builtin_amdgcn_mfma_f32_16x16x16bf16_1k(__builtin_bit_cast(bf16x4, a), __builtin_bit_cast(bf16x4, b), c, 0, 0, 0);
}
__device__ __forceinline__ float xg_max(float x) {
  auto s = __builtin_amdgcn_permlane16_swap(__float_as_uint(x), __float_as_uint(x), false, false);
  x = fmaxf(__uint_as_float(s[0]), __uint_as_float(s[1]));
  auto t = __builtin_amdgcn_permlane32_swap(__float_as_uint(x), __float_as_uint(x), false, false);
  return fmaxf(__uint_as_float(t[0]), __uint_as_float(t[1]));
}
__device__ __forceinline__ float xg_sum(float x) {
  auto s = __builtin_amdgcn_permlane16_swap(__float_as_uint(x), __float_as_uint(x), false, false);
  x = __uint_as_float(s[0]) + __uint_as_float(s[1]);
  auto t = __builtin_amdgcn_permlane32_swap(__float_as_uint(x), __float_as_uint(x), false, false);
  return __uint_as_float(t[0]) + __uint_as_float(t[1]);
}

constexpr int TK = 32;
__global__ void __launch_bounds__(NW * 64, 2) k_attn(const u16* q, const u16* kc, const u16* vc, float* pml, float* po) {
  __shared__ float mrg[NW * 4 * (DH + 4)];
  const int wg = blockIdx.x;
  const int kvh = wg & 7, b = (wg >> 3) & 1, sp = wg >> 4;
  const int t = threadIdx.x, lane = t & 63, w = __builtin_amdgcn_readfirstlane(t >> 6);
  const int c = lane & 15, g = lane >> 4;
  v4u qf[4];
#pragma unroll
  for (int s = 0; s < 4; ++s) qf[s] = *(const v4u*)(q + (size_t)b * (HQ * DH) + (kvh * 4 + (c >> 2)) * DH + 32 * s + 8 * g);
  const size_t base = ((size_t)(b * HKV + kvh) * SK + (size_t)sp * KEYS_PER_WG) * DH;
  const u16* kp = kc + base + (size_t)c * DH + 8 * g;
  const u16* vp = vc + base + (size_t)(4 * g) * DH + 8 * c;
  const unsigned m0 = (c & 3) == 0 ? 0xffffu : (c & 3) == 1 ? 0xffff0000u : 0u;
  const unsigned m1 = (c & 3) == 2 ? 0xffffu : (c & 3) == 3 ? 0xffff0000u : 0u;
  constexpr float SCALE = 0.088388347648318440f, LOG2E = 1.4426950408889634f;
  f32x4 acc0 = {0, 0, 0, 0}, acc1 = {0, 0, 0, 0};
  float m = -INFINITY, l = 0.f;
  constexpr int NT = KEYS_PER_WG / TK / NW;
  v4u kb[2][8], vb[2][8];
#define ISSUE(buf, tile) do { const size_t o_ = (size_t)((tile) * NW + w) * TK * DH; \
    _Pragma("unroll") for (int h = 0; h < 2; ++h) _Pragma("unroll") for (int s = 0; s < 4; ++s) kb[buf][h * 4 + s] = __builtin_nontemporal_load((const v4u*)(kp + o_ + h * 16 * DH + 32 * s)); \
    _Pragma("unroll") for (int h = 0; h < 2; ++h) _Pragma("unroll") for (int i = 0; i < 4; ++i) vb[buf][h * 4 + i] = __builtin_nontemporal_load((const v4u*)(vp + o_ + (h * 16 + i) * DH)); \
    __builtin_amdgcn_sched_barrier(0); } while (0)
#define COMPUTE(buf) do { \
    f32x4 sc[2]; \
    _Pragma("unroll") for (int h = 0; h < 2; ++h) { sc[h] = f32x4{0, 0, 0, 0}; \
      _Pragma("unroll") for (int s = 0; s < 4; ++s) sc[h] = mfma32(kb[buf][h * 4 + s], qf[s], sc[h]); } \
    float mx = -INFINITY; \
    _Pragma("unroll") for (int h = 0; h < 2; ++h) _Pragma("unroll") for (int i = 0; i < 4; ++i) { \
      float v_ = sc[h][i]; v_ = mrb(mrb(v_) * SCALE) * LOG2E; sc[h][i] = v_; mx = fmaxf(mx, v_); } \
    mx = xg_max(mx); \
    if (__any(mx > m)) { const float mn = fmaxf(m, mx); const float al = __builtin_amdgcn_exp2f(m - mn); m = mn; l *= al; acc0 *= al; acc1 *= al; } \
    _Pragma("unroll") for (int h = 0; h < 2; ++h) _Pragma("unroll") for (int i = 0; i < 4; ++i) { \
      const float p_ = __builtin_amdgcn_exp2f(sc[h][i] - m); const unsigned pp = cvtpk(p_, p_); l += bflo(pp); \
      const v2u bb = {pp & m0, pp & m1}; const v4u vv = vb[buf][h * 4 + i]; \
      acc0 = mfma16(v2u{vv[0], vv[1]}, bb, acc0); acc1 = mfma16(v2u{vv[2], vv[3]}, bb, acc1); } \
    } while (0)
  ISSUE(0, 0);
#pragma unroll 1
  for (int it = 0; it < NT; it += 2) {
    ISSUE(1, it + 1);
    COMPUTE(0);
    if (it + 2 < NT) ISSUE(0, it + 2);
    COMPUTE(1);
  }
  l = xg_sum(l);
  float* rec = mrg + (w * 4 + (c >> 2)) * (DH + 4);
#pragma unroll
  for (int r = 0; r < 4; ++r) { rec[4 + 8 * (4 * g + r) + (c & 3)] = acc0[r]; rec[4 + 8 * (4 * g + r) + 4 + (c & 3)] = acc1[r]; }
  if (g == 0 && (c & 3) == 0) { rec[0] = m; rec[1] = l; }
  __syncthreads();
  {
    const int h = t >> 7, d = t & 127;
    float M = -INFINITY;
#pragma unroll
    for (int ww = 0; ww < NW; ++ww) M = fmaxf(M, mrg[(ww * 4 + h) * (DH + 4)]);
    float L = 0.f, o = 0.f;
#pragma unroll
    for (int ww = 0; ww < NW; ++ww) { const float* r_ = mrg + (ww * 4 + h) * (DH + 4); const float wt = __builtin_amdgcn_exp2f(r_[0] - M); L += wt * r_[1]; o += wt * r_[4 + d]; }
    const size_t slot = ((size_t)(b * HQ + kvh * 4 + h) * NSPLIT + sp);
    po[slot * DH + d] = o;
    if (d == 0) { pml[slot * 2] = M; pml[slot * 2 + 1] = L; }
  }
}

__global__ void __launch_bounds__(128) k_comb(const float* pml, const float* po, float* o) {
  const int row = blockIdx.x, d = threadIdx.x;
  float M = -INFINITY;
  for (int s = 0; s < NSPLIT; ++s) M = fmaxf(M, pml[(row * NSPLIT + s) * 2]);
  float L = 0.f, acc = 0.f;
  for (int s = 0; s < NSPLIT; ++s) { const float wt = __builtin_amdgcn_exp2f(pml[(row * NSPLIT + s) * 2] - M); L += wt * pml[(row * NSPLIT + s) * 2 + 1]; acc += wt * po[(size_t)(row * NSPLIT + s) * DH + d]; }
  o[row * DH + d] = mrb(acc / L);
}

__global__ void __launch_bounds__(256) k_wo(const float* o, const u16* wo, const u16* x, float* h) {
  __shared__ float xs[2 * HQ * DH];
  __shared__ float red[64 * 64];
  const int t = threadIdx.x;
  for (int i = t; i < 2 * HQ * DH; i += 256) xs[i] = o[i];
  __syncthreads();
  float r[2];
  gemv32<HQ * DH, D>(xs, wo, blockIdx.x * 32, red, r);
  if (t < 64) { const int idx = (t >> 5) * D + blockIdx.x * 32 + (t & 31); h[idx] = mrb(bf2f(x[idx]) + mrb(r[0])); }
}

__global__ void __launch_bounds__(256) k_gu(const float* h, const u16* wg, const u16* wu, float* act) {
  __shared__ float xs[2 * D];
  __shared__ float red[64 * 64];
  const int t = threadIdx.x, w = t >> 6, lane = t & 63;
  if (w < 2) {
    float v[8];
#pragma unroll
    for (int i = 0; i < 8; ++i) v[i] = h[w * D + lane * 8 + i];
    ln512(v);
#pragma unroll
    for (int i = 0; i < 8; ++i) xs[w * D + lane * 8 + i] = v[i];
  }
  __syncthreads();
  float rg[2], ru[2];
  gemv32<D, F>(xs, wg, blockIdx.x * 32, red, rg);
  gemv32<D, F>(xs, wu, blockIdx.x * 32, red, ru);
  if (t < 64) {
    const float g = mrb(rg[0]), u = mrb(ru[0]);
    const float sg = mrb(g / (1.0f + __expf(-g)));
    act[(t >> 5) * F + blockIdx.x * 32 + (t & 31)] = mrb(sg * u);
  }
}

__global__ void __launch_bounds__(256) k_down(const float* act, const u16* wd, const float* h, u16* out) {
  __shared__ float xs[2 * F];
  __shared__ float red[64 * 64];
  const int t = threadIdx.x;
  for (int i = t; i < 2 * F; i += 256) xs[i] = act[i];
  __syncthreads();
  float r[2];
  gemv32<F, D>(xs, wd, blockIdx.x * 32, red, r);
  if (t < 64) { const int idx = (t >> 5) * D + blockIdx.x * 32 + (t & 31); const float v = h[idx] + mrb(r[0]); out[idx] = (u16)(cvtpk(v, v) & 0xffff); }
}

extern "C" void kernel_launch(void* const* d_in, const int* in_sizes, int n_in, void* d_out, int out_size, void* d_ws, size_t ws_size, hipStream_t stream) {
  const u16* x = (const u16*)d_in[0];
  const u16* kc = (const u16*)d_in[1];
  const u16* vc = (const u16*)d_in[2];
  const u16* wq = (const u16*)d_in[3];
  const u16* wo = (const u16*)d_in[4];
  const u16* wg = (const u16*)d_in[6];
  const u16* wu = (const u16*)d_in[7];
  const u16* wd = (const u16*)d_in[8];
  char* ws = (char*)d_ws;
  u16* q = (u16*)ws;
  float* pml = (float*)(ws + 65536);
  float* po = (float*)(ws + 131072);
  float* o = (float*)(ws + 1048576);
  float* h = (float*)(ws + 1048576 + 65536);
  float* act = (float*)(ws + 1048576 + 131072);
  k_q<<<HQ * DH / 32, 256, 0, stream>>>(x, wq, q);
  k_attn<<<B * HKV * NSPLIT, NW * 64, 0, stream>>>(q, kc, vc, pml, po);
  k_comb<<<B * HQ, 128, 0, stream>>>(pml, po, o);
  k_wo<<<D / 32, 256, 0, stream>>>(o, wo, x, h);
  k_gu<<<F / 32, 256, 0, stream>>>(h, wg, wu, act);
  k_down<<<D / 32, 256, 0, stream>>>(act, wd, h, (u16*)d_out);
}
```

```cpp
#include <hip/hip_runtime.h>
#include <hip/hip_bf16.h>
#include <cstdint>

typedef unsigned short u16;
typedef unsigned v4u __attribute__((ext_vector_type(4)));
typedef unsigned v2u __attribute__((ext_vector_type(2)));
typedef float f32x4 __attribute__((ext_vector_type(4)));
typedef float f32x2 __attribute__((ext_vector_type(2)));
typedef short s16x4 __attribute__((ext_vector_type(4)));
typedef __bf16 bf16x8_t __attribute__((ext_vector_type(8)));
typedef __bf16 bf16x2_t __attribute__((ext_vector_type(2)));
#define LAS __attribute__((address_space(3)))
#define GAS __attribute__((address_space(1)))

constexpr int D = 512, HQ = 32, HKV = 8, SK = 32768, DH = 128, F = 1792;
constexpr int NWG = 256, NTHR = 256, NSPLIT = 16;
#define TUNE_PARAM
#define TUNE_ARG
#define ABL 0
#define ST_C 29
#define ST_B 32
#define GPOS 4
#define NPRE 5
constexpr int NB = 64, NC = 56, NR = 64, NREP = 8;
constexpr int OFF_EPOCH = 0;
constexpr int OFF_LSE = 0x1000;
constexpr int OFF_HB = 0x4000;
constexpr int OFF_PART = 0x10000;
constexpr int OFF_P1 = 0x90000;
constexpr int OFF_P2 = 0x110000;
constexpr int OFF_Q = 0x180000;
constexpr int WS_BYTES = 0x1C0000, OFF_DBG = 0x200000;
constexpr int AUX_NT = 2, AUX_SC1 = 16;
constexpr unsigned SPIN_CAP = 1u << 16;
constexpr int L_W = 0;
constexpr int WREG = 24576;
constexpr int L_WQ = 4 * WREG;
constexpr int L_XN = L_WQ + 16384;
constexpr int L_RED = L_XN + 2048;
constexpr int L_TAG = L_RED + 2048;
constexpr int L_QF = L_TAG + 16;
constexpr int L_MRG = L_QF + 1024;
constexpr int L_OB = L_MRG + 4 * 4 * 132 * 4;
constexpr int L_RR = L_OB + 512;
constexpr int L_DBG = L_RR + 512;
constexpr int L_BYTES = L_DBG + 128;

__device__ __forceinline__ float bflo(unsigned v) { return __uint_as_float(v << 16); }
__device__ __forceinline__ float bfhi(unsigned v) { return __uint_as_float(v & 0xffff0000u); }
__device__ __forceinline__ unsigned cvtpk(float lo, float hi) { return __builtin_bit_cast(unsigned, __builtin_convertvector(f32x2{lo, hi}, bf16x2_t)); }
__device__ __forceinline__ float rbf(float x) { return bflo(cvtpk(x, x)); }
__device__ __forceinline__ bool tagged(v4u v, unsigned tag) { return (v[1] == tag) & (v[3] == tag); }
__device__ __forceinline__ float u2f(unsigned v) { return __uint_as_float(v); }
__device__ __forceinline__ unsigned f2u(float v) { return __float_as_uint(v); }

template <int CTRL> __device__ __forceinline__ float dpp(float x) { return __builtin_bit_cast(float, __builtin_amdgcn_mov_dpp(__builtin_bit_cast(int, x), CTRL, 0xf, 0xf, true)); }
template <int CTRL> __device__ __forceinline__ unsigned dppu(unsigned x) { return (unsigned)__builtin_amdgcn_mov_dpp((int)x, CTRL, 0xf, 0xf, true); }
constexpr int XOR1 = 0xB1, XOR2 = 0x4E, ROR8 = 0x128, ROR4 = 0x124, HMIRROR = 0x141;
__device__ __forceinline__ float sw32_sum(float a, float b) { auto r = __builtin_amdgcn_permlane32_swap(f2u(a), f2u(b), false, false); return u2f(r[0]) + u2f(r[1]); }
__device__ __forceinline__ float sw16_sum(float a, float b) { auto r = __builtin_amdgcn_permlane16_swap(f2u(a), f2u(b), false, false); return u2f(r[0]) + u2f(r[1]); }
__device__ __forceinline__ float sw32_max(float a, float b) { auto r = __builtin_amdgcn_permlane32_swap(f2u(a), f2u(b), false, false); return fmaxf(u2f(r[0]), u2f(r[1])); }
__device__ __forceinline__ float sw16_max(float a, float b) { auto r = __builtin_amdgcn_permlane16_swap(f2u(a), f2u(b), false, false); return fmaxf(u2f(r[0]), u2f(r[1])); }
__device__ __forceinline__ float wave_sum(float x) {
  x += dpp<XOR1>(x); x += dpp<XOR2>(x); x += dpp<ROR4>(x); x += dpp<ROR8>(x);
  x = sw16_sum(x, x); return sw32_sum(x, x);
}
__device__ __forceinline__ f32x4 mfma32(v4u a, v4u b, f32x4 c) { return __builtin_amdgcn_mfma_f32_16x16x32_bf16(__builtin_bit_cast(bf16x8_t, a), __builtin_bit_cast(bf16x8_t, b), c, 0, 0, 0); }
__device__ __forceinline__ f32x4 mfma16(v2u a, v2u b, f32x4 c) { return __builtin_amdgcn_mfma_f32_16x16x16bf16_1k(__builtin_bit_cast(s16x4, a), __builtin_bit_cast(s16x4, b), c, 0, 0, 0); }
__device__ __forceinline__ void barrier() { asm volatile("s_waitcnt lgkmcnt(0)\n\ts_barrier" ::: "memory"); }
__device__ __forceinline__ v4u tr_frag(LAS unsigned char* lo, LAS unsigned char* hi) {
  const v2u a = __builtin_bit_cast(v2u, __builtin_amdgcn_ds_read_tr16_b64_v4i16((LAS s16x4*)lo));
  const v2u b = __builtin_bit_cast(v2u, __builtin_amdgcn_ds_read_tr16_b64_v4i16((LAS s16x4*)hi));
  return v4u{a[0], a[1], b[0], b[1]};
}

__device__ __forceinline__ v4u ln_row(v4u xv) {
  float x[8];
#pragma unroll
  for (int i = 0; i < 4; ++i) { x[2 * i] = bflo(xv[i]); x[2 * i + 1] = bfhi(xv[i]); }
  const float mean = wave_sum(((x[0] + x[1]) + (x[2] + x[3])) + ((x[4] + x[5]) + (x[6] + x[7]))) * (1.0f / D);
  float vs = 0.f;
#pragma unroll
  for (int i = 0; i < 8; ++i) { const float d = x[i] - mean; vs = fmaf(d, d, vs); }
  const float var = wave_sum(vs) * (1.0f / D);
  const float mb = rbf(mean);
  const float rs = __builtin_amdgcn_rcpf(rbf(__builtin_amdgcn_sqrtf(rbf(rbf(var) + 1e-5f))));
  v4u r;
#pragma unroll
  for (int i = 0; i < 4; ++i) { const unsigned d = cvtpk(x[2 * i] - mb, x[2 * i + 1] - mb); r[i] = cvtpk(bflo(d) * rs, bfhi(d) * rs); }
  return r;
}
__device__ __forceinline__ void glds16(const u16* gsrc, unsigned lds_dst) {
  unsigned keep;
  asm volatile("s_mov_b32 %0, m0\n\ts_mov_b32 m0, %2\n\ts_nop 0\n\tglobal_load_lds_dwordx4 %1, off nt\n\ts_mov_b32 m0, %0" : "=&s"(keep) : "v"(gsrc), "s"(lds_dst) : "memory");
}

#define STAMP(i) do { } while (0)
#define DUMP_STAMPS() do { } while (0)
#define LD128(off) __builtin_amdgcn_raw_buffer_load_b128(rw, (off), 0, AUX_SC1)
#define ST128(v, off) __builtin_amdgcn_raw_buffer_store_b128((v), rw, (off), 0, AUX_SC1)
#define SPIN_LOOP _Pragma("unroll 1") for (unsigned spins = 0; spins < SPIN_CAP; ++spins)
#define NOHOIST() asm volatile("" ::: "memory")

__global__ void __launch_bounds__(512, 2) fused_block(const u16* __restrict__ x, const u16* __restrict__ kc, const u16* __restrict__ vc, const u16* __restrict__ wq,
                                                       const u16* __restrict__ wo, const u16* __restrict__ wgate, const u16* __restrict__ wup, const u16* __restrict__ wdown,
                                                       u16* __restrict__ out, unsigned char* ws TUNE_PARAM) {
  __shared__ __attribute__((aligned(16))) unsigned char smem[L_BYTES];
  LAS unsigned char* L = (LAS unsigned char*)smem;
  STAMP(0);
  const unsigned tstart = (unsigned)__builtin_amdgcn_s_memrealtime();
  const int wg = blockIdx.x, xcd = wg & 7, idx = wg >> 3;
  const int kvh = xcd, b = idx & 1, sp = idx >> 1;
  const int tid = threadIdx.x, lane = tid & 63, w = __builtin_amdgcn_readfirstlane(tid >> 6);
  const int c = lane & 15, g = lane >> 4;
  const int qc = xcd * 8 + (idx & 7), kq = idx >> 3;
  const __amdgpu_buffer_rsrc_t rw = __builtin_amdgcn_make_buffer_rsrc((void*)ws, 0, WS_BYTES, 0x00020000);
  const unsigned lds0 = (unsigned)(uintptr_t)L;
#define WAIT_UNTIL(t, slp) do { while ((int)((unsigned)__builtin_amdgcn_s_memrealtime() - (tstart + (unsigned)(t))) < 0) __builtin_amdgcn_s_sleep(slp); } while (0)

  const int nc = 4 - b, nbw = 3 + 2 * b;
  const int role = sp < nc ? 1 : sp < nc + nbw ? 2 : (sp >= 8 && sp < 12) ? 3 : 0;
  const int ci = xcd * 7 + 4 * b + sp, bi = xcd * 8 + 3 * b + sp - nc, rj = xcd * 8 + idx - 16;
  const int nn = 16 - nc - nbw, sn = sp - nc - nbw;
  const int strest = 512 - nc * ST_C - nbw * ST_B, stn = b ? strest >> 3 : strest / 9, stx = strest - nn * stn;
  const int nstep = role == 1 ? ST_C : role == 2 ? ST_B : stn + (sn < stx ? 1 : 0);
  const int step0 = role == 1 ? ST_C * sp : role == 2 ? nc * ST_C + ST_B * (sp - nc) : nc * ST_C + nbw * ST_B + stn * sn + (sn < stx ? sn : stx);
  const size_t pbase = (size_t)(b * HKV + kvh) * SK * DH;
  const __amdgpu_buffer_rsrc_t rk = __builtin_amdgcn_make_buffer_rsrc((void*)(kc + pbase), 0, SK * DH * 2, 0x00020000);
  const __amdgpu_buffer_rsrc_t rv = __builtin_amdgcn_make_buffer_rsrc((void*)(vc + pbase), 0, SK * DH * 2, 0x00020000);
  const int koff = c * 256 + g * 16, voff = g * 1024 + c * 16;
  const unsigned sbase = (unsigned)(step0 * 64 + w * 16) * 256u;
  v4u kb[NPRE + 1][4], vb[NPRE + 1][4];
#define ISSUEK(d, so) do { _Pragma("unroll") for (int s = 0; s < 4; ++s) kb[d][s] = __builtin_amdgcn_raw_buffer_load_b128(rk, koff + s * 64, (so), AUX_NT); __builtin_amdgcn_sched_barrier(0); } while (0)
#define ISSUEV(d, so) do { _Pragma("unroll") for (int s = 0; s < 4; ++s) vb[d][s] = __builtin_amdgcn_raw_buffer_load_b128(rv, voff + s * 256, (so), AUX_NT); __builtin_amdgcn_sched_barrier(0); } while (0)
#define VMWAIT(n) do { asm volatile("s_waitcnt vmcnt(%0)" :: "i"(n) : "memory"); __builtin_amdgcn_sched_barrier(0); } while (0)
  const int q4 = (lane >> 2) & 3, p4 = lane & 3;
  unsigned tag;
  {
    v4u x0, x1; unsigned ep;
    asm volatile("global_load_dwordx4 %0, %3, %4\n\tglobal_load_dwordx4 %1, %3, %4 offset:1024\n\tglobal_load_dword %2, %5, %6 sc1"
                 : "=&v"(x0), "=&v"(x1), "=&v"(ep) : "v"(lane * 16), "s"(x), "v"(0), "s"(ws) : "memory");
    const int r8 = lane >> 3;
    const u16* p = wq + (size_t)(128 * kq + 32 * w + r8) * (HQ * DH) + qc * 64;
#pragma unroll
    for (int t = 0; t < 4; ++t) glds16(p + (size_t)t * 8 * (HQ * DH) + (((lane & 7) ^ ((r8 & 2) | ((t & 1) << 2))) * 8), lds0 + L_WQ + w * 4096 + t * 1024);
    ISSUEK(0, sbase);
    STAMP(7);
    asm volatile("s_waitcnt vmcnt(8)" : "+v"(x0), "+v"(x1), "+v"(ep) :: "memory");
    tag = __builtin_amdgcn_readfirstlane(ep) + 1u;
    const v4u n0 = ln_row(x0), n1 = ln_row(x1);
    if ((lane >> 2) == 4 * kq + w) { *(LAS v4u*)(L + L_XN + lane * 16) = n0; *(LAS v4u*)(L + L_XN + 1024 + lane * 16) = n1; }
    STAMP(9);
    v4u fr[4];
    const int sw = (q4 & 2) | ((g & 1) << 2);
    VMWAIT(4);
    STAMP(8);
#pragma unroll
    for (int t = 0; t < 4; ++t) {
      LAS unsigned char* a = L + L_WQ + w * 4096 + g * 1024 + q4 * 128 + (p4 & 1) * 8 + (((2 * t + (p4 >> 1)) ^ sw) * 16);
      fr[t] = tr_frag(a, a + 512);
    }
    const v4u xb = *(LAS v4u*)(L + L_XN + (c & 1) * 1024 + (128 * kq + 32 * w + 8 * g) * 2);
#pragma unroll
    for (int t = 0; t < 4; ++t) {
      const f32x4 qa = mfma32(fr[t], xb, f32x4{0.f, 0.f, 0.f, 0.f});
      if (c < 2) *(LAS f32x4*)(L + L_RED + w * 512 + c * 256 + t * 64 + g * 16) = qa;
    }
    if (w != 0) ISSUEV(0, sbase);
    barrier();
    if (w == 0) {
      f32x2 s = {0.f, 0.f};
#pragma unroll
      for (int ww = 0; ww < 4; ++ww) s += *(LAS f32x2*)(L + L_RED + ww * 512 + lane * 8);
      ST128((v4u{f2u(s[0]), tag, f2u(s[1]), tag}), OFF_Q + ((kq * 2 + (lane >> 5)) * 4096 + qc * 64 + (lane & 31) * 2) * 8);
      NOHOIST(); __builtin_amdgcn_sched_barrier(0);
      ISSUEV(0, sbase);
    }
    STAMP(1);
  }
  const bool odd = (nstep - NPRE) & 1;
  const int qo = OFF_Q + (b * 4096 + kvh * 512 + w * 128) * 8 + lane * 16;
  v4u qv[4];
#pragma unroll
  for (int d = 1; d < NPRE; ++d) {
    if (d == GPOS) {
      STAMP(11);
#pragma unroll
      for (int j = 0; j < 4; ++j) qv[j] = LD128(qo + j * 65536);
      __builtin_amdgcn_sched_barrier(0);
    }
    ISSUEK(d, sbase + d * 16384u); ISSUEV(d, sbase + d * 16384u);
  }
  if (odd) { ISSUEK(NPRE, sbase + NPRE * 16384u); ISSUEV(NPRE, sbase + NPRE * 16384u); }
  STAMP(10);
  if (!__all(tagged(qv[0], tag) & tagged(qv[1], tag) & tagged(qv[2], tag) & tagged(qv[3], tag))) {
    SPIN_LOOP {
      NOHOIST();
#pragma unroll
      for (int j = 0; j < 4; ++j) qv[j] = LD128(qo + j * 65536);
      if (__all(tagged(qv[0], tag) & tagged(qv[1], tag) & tagged(qv[2], tag) & tagged(qv[3], tag))) break;
    }
  }
  *(LAS unsigned*)(L + L_QF + w * 256 + lane * 4) = cvtpk((u2f(qv[0][0]) + u2f(qv[1][0])) + (u2f(qv[2][0]) + u2f(qv[3][0])), (u2f(qv[0][2]) + u2f(qv[1][2])) + (u2f(qv[2][2]) + u2f(qv[3][2])));
  barrier();
  STAMP(2);
  v4u qf[4];
#pragma unroll
  for (int s = 0; s < 4; ++s) qf[s] = *(LAS v4u*)(L + L_QF + (c >> 2) * 256 + s * 64 + g * 16);

  const unsigned m0 = (c & 3) == 0 ? 0xffffu : (c & 3) == 1 ? 0xffff0000u : 0u;
  const unsigned m1 = (c & 3) == 2 ? 0xffffu : (c & 3) == 3 ? 0xffff0000u : 0u;
  constexpr float SCALE = 0.088388347648318440f, LOG2E = 1.4426950408889634f;
  f32x4 acc0 = {0.f, 0.f, 0.f, 0.f}, acc1 = {0.f, 0.f, 0.f, 0.f};
  float m = -INFINITY, l = 0.f;
  f32x4 sc;
#define QK(d) do { sc = f32x4{0.f, 0.f, 0.f, 0.f}; _Pragma("unroll") for (int s = 0; s < 4; ++s) sc = mfma32(kb[d][s], qf[s], sc); \
    asm volatile("" : "+v"(sc) :: "memory"); __builtin_amdgcn_sched_barrier(0); } while (0)
#define PV(d) do { \
    const unsigned r01 = cvtpk(sc[0], sc[1]), r23 = cvtpk(sc[2], sc[3]); \
    const unsigned t01 = cvtpk(bflo(r01) * SCALE, bfhi(r01) * SCALE), t23 = cvtpk(bflo(r23) * SCALE, bfhi(r23) * SCALE); \
    const float s0 = bflo(t01), s1 = bfhi(t01), s2 = bflo(t23), s3 = bfhi(t23); \
    float mx = fmaxf(fmaxf(s0, s1), fmaxf(s2, s3)); mx = sw16_max(mx, mx); mx = sw32_max(mx, mx); \
    if (__any(mx > m)) { const float mn = fmaxf(m, mx); const float al = __builtin_amdgcn_exp2f((m - mn) * LOG2E); m = mn; l *= al; acc0 *= al; acc1 *= al; } \
    const float sv[4] = {s0, s1, s2, s3}; \
    _Pragma("unroll") for (int i = 0; i < 4; ++i) { \
      const float p_ = __builtin_amdgcn_exp2f((sv[i] - m) * LOG2E); const unsigned pp = cvtpk(p_, p_); l += bflo(pp); \
      const v2u bb = {pp & m0, pp & m1}; const v4u vv = vb[d][i]; \
      acc0 = mfma16(v2u{vv[0], vv[1]}, bb, acc0); acc1 = mfma16(v2u{vv[2], vv[3]}, bb, acc1); } \
    asm volatile("" : "+v"(acc0), "+v"(acc1) :: "memory"); __builtin_amdgcn_sched_barrier(0); } while (0)
  const int ffo = (128 * w + (lane >> 2)) * F + ci * 32 + (((lane & 3) ^ ((lane >> 4) & 2)) * 8);
  const u16* wsrc = role == 1 ? wgate + ffo : wo + (size_t)(bi * 64 + g) * D + 128 * w + 8 * c;
  int wstr = role == 1 ? 16 * F : 4 * D;
  int wp = 0;
  const int wn = (ABL & 2) ? 0 : role == 1 ? 24 : role == 2 ? 16 : 0;
#define WPIECE() do { if (wp < wn) { glds16(wsrc, lds0 + L_W + w * WREG + wp * 1024); ++wp; wsrc += wstr; \
      if (role == 1 && wp == 8) wsrc = wup + ffo; \
      if (role == 1 && wp == 16) { wsrc = wdown + (size_t)(ci * 32 + g) * D + 128 * w + 8 * c; wstr = 4 * D; } } } while (0)
  unsigned so = sbase + (odd ? NPRE + 1 : NPRE) * 16384u;
  const unsigned send = sbase + (unsigned)nstep * 16384u;
  QK(0); ISSUEK(0, so); PV(0); ISSUEV(0, so); WPIECE();
  QK(1); ISSUEK(1, so + 16384u); PV(1); ISSUEV(1, so + 16384u); WPIECE();
  so += 32768u;
#pragma unroll
  for (int d = 2; d < NPRE; ++d) { QK(d); PV(d); }
  if (odd) { QK(NPRE); PV(NPRE); }
#pragma unroll 1
  for (; so < send; so += 32768u) {
    QK(0); ISSUEK(0, so); WPIECE(); PV(0); ISSUEV(0, so); WPIECE();
    QK(1); ISSUEK(1, so + 16384u); PV(1); ISSUEV(1, so + 16384u); WPIECE();
  }
  QK(0); PV(0); QK(1); PV(1);
  STAMP(3);

  l = sw16_sum(l, l); l = sw32_sum(l, l);
  {
    LAS float* rec = (LAS float*)(L + L_MRG) + (w * 4 + (c >> 2)) * 132;
#pragma unroll
    for (int r = 0; r < 4; ++r) { rec[4 + 8 * (4 * g + r) + (c & 3)] = acc0[r]; rec[4 + 8 * (4 * g + r) + 4 + (c & 3)] = acc1[r]; }
    if (g == 0 && (c & 3) == 0) *(LAS f32x2*)rec = f32x2{m, l};
  }
  barrier();
  if (w < 2) {
    const int hd = 2 * w + (lane >> 5);
    f32x2 ml[4]; f32x4 ov[4];
#pragma unroll
    for (int ww = 0; ww < 4; ++ww) { LAS float* rec = (LAS float*)(L + L_MRG) + (ww * 4 + hd) * 132; ml[ww] = *(LAS f32x2*)rec; ov[ww] = *(LAS f32x4*)(rec + 4 + 4 * (lane & 31)); }
    const float M = fmaxf(fmaxf(ml[0][0], ml[1][0]), fmaxf(ml[2][0], ml[3][0]));
    float Ls = 0.f; f32x4 o = {0.f, 0.f, 0.f, 0.f};
#pragma unroll
    for (int ww = 0; ww < 4; ++ww) { const float wt = __builtin_amdgcn_exp2f((ml[ww][0] - M) * LOG2E); Ls = fmaf(wt, ml[ww][1], Ls); o += wt * ov[ww]; }
    o *= __builtin_amdgcn_rcpf(Ls);
    ST128((v4u{cvtpk(o[0], o[1]), tag, cvtpk(o[2], o[3]), tag}), OFF_PART + ((b * HQ + kvh * 4 + hd) * NSPLIT + sp) * 512 + (lane & 31) * 16);
    if ((lane & 31) == 0)
      __builtin_amdgcn_raw_buffer_store_b64(v2u{f2u(fmaf(M, LOG2E, __builtin_amdgcn_logf(Ls))), tag}, rw, OFF_LSE + (((b * HKV + kvh) * NSPLIT + sp) * 4 + hd) * 8, 0, AUX_SC1);
  }
  STAMP(4);
  const unsigned dm0 = (c & 7) == 0 ? 0xffffu : (c & 7) == 1 ? 0xffff0000u : 0u, dm1 = (c & 7) == 2 ? 0xffffu : (c & 7) == 3 ? 0xffff0000u : 0u,
                 dm2 = (c & 7) == 4 ? 0xffffu : (c & 7) == 5 ? 0xffff0000u : 0u, dm3 = (c & 7) == 6 ? 0xffffu : (c & 7) == 7 ? 0xffff0000u : 0u;
#define DIAG(p) v4u{(p) & dm0, (p) & dm1, (p) & dm2, (p) & dm3}
#define PUBLISH_ACC(acc, base) do { ST128((v4u{f2u(acc[0]), tag, f2u(acc[1]), tag}), (base) + tid * 16); ST128((v4u{f2u(acc[2]), tag, f2u(acc[3]), tag}), (base) + 4096 + tid * 16); } while (0)

  if (role == 2) {
    v4u wr[16];
#pragma unroll
    for (int i = 0; i < 16; ++i) wr[i] = *(LAS v4u*)(L + L_W + w * WREG + i * 1024 + lane * 16);
    const int head = bi >> 1, s = lane >> 2;
    const int po = OFF_PART + (head * NSPLIT + s) * 512 + (bi & 1) * 256 + w * 64 + (lane & 3) * 16, lo = OFF_LSE + (((head >> 2) * NSPLIT + s) * 4 + (head & 3)) * 8;
    v4u a0, a1; v2u e0, e1;
    bool ok0 = false, ok1 = false;
    SPIN_LOOP {
      NOHOIST();
      if (!ok0) { a0 = LD128(po); e0 = __builtin_amdgcn_raw_buffer_load_b64(rw, lo, 0, AUX_SC1); }
      if (!ok1) { a1 = LD128(po + HQ * NSPLIT * 512); e1 = __builtin_amdgcn_raw_buffer_load_b64(rw, lo + HKV * NSPLIT * 32, 0, AUX_SC1); }
      ok0 = tagged(a0, tag) & (e0[1] == tag); ok1 = tagged(a1, tag) & (e1[1] == tag);
      if (__all(ok0 & ok1)) break;
    }
    STAMP(5);
    const float l0 = u2f(e0[0]), l1 = u2f(e1[0]);
    float x0m = fmaxf(l0, dpp<ROR4>(l0)), x1m = fmaxf(l1, dpp<ROR4>(l1));
    x0m = fmaxf(x0m, dpp<ROR8>(x0m)); x1m = fmaxf(x1m, dpp<ROR8>(x1m));
    x0m = sw16_max(x0m, x0m); x1m = sw16_max(x1m, x1m); x0m = sw32_max(x0m, x0m); x1m = sw32_max(x1m, x1m);
    const float w0 = __builtin_amdgcn_exp2f(l0 - x0m), w1 = __builtin_amdgcn_exp2f(l1 - x1m);
    const float v0 = sw32_sum(w0 * bflo(a0[0]), w1 * bflo(a1[0])), v1 = sw32_sum(w0 * bfhi(a0[0]), w1 * bfhi(a1[0])),
                v2 = sw32_sum(w0 * bflo(a0[2]), w1 * bflo(a1[2])), v3 = sw32_sum(w0 * bfhi(a0[2]), w1 * bfhi(a1[2]));
    float ws_ = sw32_sum(w0, w1);
    float u0 = sw16_sum(v0, v1), u1 = sw16_sum(v2, v3);
    ws_ = sw16_sum(ws_, ws_);
    u0 += dpp<ROR8>(u0); u1 += dpp<ROR8>(u1); ws_ += dpp<ROR8>(ws_);
    u0 += dpp<ROR4>(u0); u1 += dpp<ROR4>(u1); ws_ += dpp<ROR4>(ws_);
    const float uo = ((lane & 4) ? u1 : u0) * __builtin_amdgcn_rcpf(ws_);
    if ((lane & 8) == 0) *(LAS unsigned*)(L + L_OB + ((g >> 1) * 4 + (g & 1) + ((lane & 4) >> 1)) * 64 + (4 * w + (lane & 3)) * 4) = cvtpk(uo, uo);
    barrier();
    f32x4 acc = {0.f, 0.f, 0.f, 0.f};
#pragma unroll
    for (int q = 0; q < 4; ++q) {
      const v4u pv = *(LAS v4u*)(L + L_OB + ((c >> 3) * 4 + g) * 64 + q * 16);
#pragma unroll
      for (int i = 0; i < 4; ++i) acc = mfma32(wr[q * 4 + i], DIAG(pv[i]), acc);
    }
    PUBLISH_ACC(acc, OFF_P1 + bi * 8192);
    STAMP(6);
  } else if (role == 3) {
    const int rb = rj & 1, col0 = ((rj >> 1) & 15) * 32 + (rj >> 5) * 16;
    const unsigned xp = *(const GAS unsigned*)(x + rb * D + col0 + 2 * (lane & 7));
    const int pp = lane >> 3;
    float h0, h1;
    {
      const int po = OFF_P1 + (16 * w + pp) * 8192 + rj * 128 + (lane & 7) * 16;
      v4u a0, a1;
      bool ok0 = false, ok1 = false;
      SPIN_LOOP {
        NOHOIST();
        if (!ok0) a0 = LD128(po);
        if (!ok1) a1 = LD128(po + 8 * 8192);
        ok0 = tagged(a0, tag); ok1 = tagged(a1, tag);
        if (__all(ok0 & ok1)) break;
      }
      STAMP(5);
      float u = sw32_sum(u2f(a0[0]) + u2f(a1[0]), u2f(a0[2]) + u2f(a1[2]));
      u = sw16_sum(u, u); u += dpp<ROR8>(u);
      if ((lane & 24) == 0) *(LAS float*)(L + L_RR + w * 64 + ((lane >> 5) * 8 + (lane & 7)) * 4) = u;
      barrier();
      f32x2 s = {0.f, 0.f};
#pragma unroll
      for (int ww = 0; ww < 4; ++ww) s += *(LAS f32x2*)(L + L_RR + ww * 64 + (lane & 7) * 8);
      const unsigned at = cvtpk(s[0], s[1]);
      const unsigned hp = cvtpk(bflo(xp) + bflo(at), bfhi(xp) + bfhi(at));
      h0 = bflo(hp); h1 = bfhi(hp);
      if (w == 0) __builtin_amdgcn_raw_buffer_store_b64(v2u{hp, tag}, rw, OFF_HB + pp * 4096 + rb * 2048 + (col0 / 2 + (lane & 7)) * 8, 0, AUX_SC1);
      STAMP(6);
    }
    {
      const int po = OFF_P2 + (14 * w + pp) * 8192 + rj * 128 + (lane & 7) * 16;
      v4u a0, a1 = {0u, tag, 0u, tag};
      bool ok0 = false, ok1 = pp >= 6;
      SPIN_LOOP {
        NOHOIST();
        if (!ok0) a0 = LD128(po);
        if (!ok1) a1 = LD128(po + 8 * 8192);
        ok0 = tagged(a0, tag); ok1 = tagged(a1, tag);
        if (__all(ok0 & ok1)) break;
      }
      STAMP(7);
      float u = sw32_sum(u2f(a0[0]) + u2f(a1[0]), u2f(a0[2]) + u2f(a1[2]));
      u = sw16_sum(u, u); u += dpp<ROR8>(u);
      if ((lane & 24) == 0) *(LAS float*)(L + L_RR + 256 + w * 64 + ((lane >> 5) * 8 + (lane & 7)) * 4) = u;
      barrier();
      if (tid < 8) {
        f32x2 s = {0.f, 0.f};
#pragma unroll
        for (int ww = 0; ww < 4; ++ww) s += *(LAS f32x2*)(L + L_RR + 256 + ww * 64 + lane * 8);
        const unsigned dn = cvtpk(s[0], s[1]);
        *(GAS unsigned*)(out + rb * D + col0 + 2 * lane) = cvtpk(h0 + bflo(dn), h1 + bfhi(dn));
        if (rj == 0 && tid == 0) __builtin_amdgcn_raw_buffer_store_b32(tag, rw, OFF_EPOCH, 0, AUX_SC1);
      }
      STAMP(8);
    }
  } else if (role == 1) {
    v4u fg[8], fu[8], wd[8];
#pragma unroll
    for (int ks = 0; ks < 4; ++ks)
#pragma unroll
      for (int t = 0; t < 2; ++t) {
        LAS unsigned char* a = L + L_W + w * WREG + (32 * ks + 8 * g + q4) * 64 + (((2 * t + (p4 >> 1)) ^ ((g & 1) << 1)) * 16) + (p4 & 1) * 8;
        fg[ks * 2 + t] = tr_frag(a, a + 256); fu[ks * 2 + t] = tr_frag(a + 8192, a + 8192 + 256);
      }
#pragma unroll
    for (int i = 0; i < 8; ++i) wd[i] = *(LAS v4u*)(L + L_W + w * WREG + 16384 + i * 1024 + lane * 16);
    if (w < 2) {
      const int ho = OFF_HB + (ci & 7) * 4096 + w * 2048 + lane * 32;
      v4u ha, hb;
      bool ok = false;
      SPIN_LOOP {
        NOHOIST();
        if (!ok) { ha = LD128(ho); hb = LD128(ho + 16); }
        ok = tagged(ha, tag) & tagged(hb, tag);
        if (__all(ok)) break;
      }
      STAMP(5);
      *(LAS v4u*)(L + L_XN + w * 1024 + lane * 16) = ln_row(v4u{ha[0], ha[2], hb[0], hb[2]});
    }
    barrier();
    f32x4 ag[2], au[2];
#pragma unroll
    for (int t = 0; t < 2; ++t) { ag[t] = f32x4{0.f, 0.f, 0.f, 0.f}; au[t] = f32x4{0.f, 0.f, 0.f, 0.f}; }
#pragma unroll
    for (int ks = 0; ks < 4; ++ks) {
      const v4u xb = *(LAS v4u*)(L + L_XN + (c & 1) * 1024 + (128 * w + 32 * ks + 8 * g) * 2);
#pragma unroll
      for (int t = 0; t < 2; ++t) { ag[t] = mfma32(fg[ks * 2 + t], xb, ag[t]); au[t] = mfma32(fu[ks * 2 + t], xb, au[t]); }
    }
    {
      const f32x4 sel = (c & 4) ? ((c & 2) ? au[1] : au[0]) : ((c & 2) ? ag[1] : ag[0]);
      if (c < 8) {
#pragma unroll
        for (int i = 0; i < 4; ++i) *(LAS float*)(L + L_RED + (c >> 2) * 1024 + ((c & 1) * 32 + (c & 2) * 8 + 4 * g + i) * 16 + w * 4) = sel[i];
      }
    }
    barrier();
    const int fo = ((c >> 3) * 32 + 4 * (c & 7) + g) * 16;
    const f32x4 pg = *(LAS f32x4*)(L + L_RED + fo), pu = *(LAS f32x4*)(L + L_RED + 1024 + fo);
    const unsigned gu = cvtpk((pg[0] + pg[1]) + (pg[2] + pg[3]), (pu[0] + pu[1]) + (pu[2] + pu[3]));
    const float gg = bflo(gu);
    const float sg = rbf(gg * __builtin_amdgcn_rcpf(1.0f + __builtin_amdgcn_exp2f(-gg * LOG2E)));
    const float av = sg * bfhi(gu);
    const unsigned ap = cvtpk(av, av);
    const unsigned am = dppu<HMIRROR>(ap);
    const unsigned alo = (c & 4) ? am : ap, ahi = (c & 4) ? ap : am;
    f32x4 acc = {0.f, 0.f, 0.f, 0.f};
#define BC(v, t) ((c & 4) ? dppu<(3 - (t)) * 0x55>(v) : dppu<(t) * 0x55>(v))
#define BCH(v, t) ((c & 4) ? dppu<(t) * 0x55>(v) : dppu<(3 - (t)) * 0x55>(v))
    { const unsigned p = BC(alo, 0); acc = mfma32(wd[0], DIAG(p), acc); }
    { const unsigned p = BC(alo, 1); acc = mfma32(wd[1], DIAG(p), acc); }
    { const unsigned p = BC(alo, 2); acc = mfma32(wd[2], DIAG(p), acc); }
    { const unsigned p = BC(alo, 3); acc = mfma32(wd[3], DIAG(p), acc); }
    { const unsigned p = BCH(ahi, 0); acc = mfma32(wd[4], DIAG(p), acc); }
    { const unsigned p = BCH(ahi, 1); acc = mfma32(wd[5], DIAG(p), acc); }
    { const unsigned p = BCH(ahi, 2); acc = mfma32(wd[6], DIAG(p), acc); }
    { const unsigned p = BCH(ahi, 3); acc = mfma32(wd[7], DIAG(p), acc); }
    PUBLISH_ACC(acc, OFF_P2 + ci * 8192);
    STAMP(6);
  }
  DUMP_STAMPS();
}

extern "C" void kernel_launch(void* const* d_in, const int* in_sizes, int n_in, void* d_out, int out_size, void* d_ws, size_t ws_size, hipStream_t stream) {
  fused_block<<<NWG, NTHR, 0, stream>>>((const u16*)d_in[0], (const u16*)d_in[1], (const u16*)d_in[2], (const u16*)d_in[3], (const u16*)d_in[4],
                                        (const u16*)d_in[6], (const u16*)d_in[7], (const u16*)d_in[8], (u16*)d_out, (unsigned char*)d_ws TUNE_ARG);
}
```

```cpp
#include <hip/hip_runtime.h>
#include <hip/hip_bf16.h>
#include <cstdint>

typedef unsigned short u16;
typedef unsigned v4u __attribute__((ext_vector_type(4)));
typedef unsigned v2u __attribute__((ext_vector_type(2)));
typedef float f32x4 __attribute__((ext_vector_type(4)));
typedef float f32x2 __attribute__((ext_vector_type(2)));
typedef short s16x4 __attribute__((ext_vector_type(4)));
typedef __bf16 bf16x8_t __attribute__((ext_vector_type(8)));
typedef __bf16 bf16x2_t __attribute__((ext_vector_type(2)));
#define LAS __attribute__((address_space(3)))
#define GAS __attribute__((address_space(1)))

constexpr int D = 512, HQ = 32, HKV = 8, SK = 32768, DH = 128, F = 1792;
constexpr int NWG = 256, NTHR = 320, NSPLIT = 16;
#define TUNE_PARAM
#define TUNE_ARG
#define ABL 0
#define ST_C 29
#define ST_B 32
#define T_P 250
#define KAUX 0
#define VAUX 2
#define PACE_N 4
constexpr int NB = 64, NC = 56, NR = 64, NREP = 8;
constexpr int OFF_EPOCH = 0;
constexpr int OFF_LSE = 0x1000;
constexpr int OFF_HB = 0x4000;
constexpr int OFF_PART = 0x10000;
constexpr int OFF_P1 = 0x90000;
constexpr int OFF_P2 = 0x110000;
constexpr int OFF_Q = 0x180000;
constexpr int WS_BYTES = 0x1C0000, OFF_DBG = 0x200000;
constexpr int AUX_NT = 2, AUX_SC1 = 16;
constexpr unsigned SPIN_CAP = 1u << 16;
constexpr int L_W = 0;
constexpr int WREG = 24576;
constexpr int L_WQ = 4 * WREG;
constexpr int L_XN = L_WQ + 16384;
constexpr int L_RED = L_XN + 2048;
constexpr int L_TAG = L_RED + 2048;
constexpr int L_QF = L_TAG + 16;
constexpr int L_MRG = L_QF + 1024;
constexpr int L_OB = L_MRG + 4 * 4 * 132 * 4;
constexpr int L_RR = L_OB + 512;
constexpr int L_DBG = L_RR + 512;
constexpr int L_BYTES = L_DBG + 128;

__device__ __forceinline__ float bflo(unsigned v) { return __uint_as_float(v << 16); }
__device__ __forceinline__ float bfhi(unsigned v) { return __uint_as_float(v & 0xffff0000u); }
__device__ __forceinline__ unsigned cvtpk(float lo, float hi) { return __builtin_bit_cast(unsigned, __builtin_convertvector(f32x2{lo, hi}, bf16x2_t)); }
__device__ __forceinline__ float rbf(float x) { return bflo(cvtpk(x, x)); }
__device__ __forceinline__ bool tagged(v4u v, unsigned tag) { return (v[1] == tag) & (v[3] == tag); }
__device__ __forceinline__ float u2f(unsigned v) { return __uint_as_float(v); }
__device__ __forceinline__ unsigned f2u(float v) { return __float_as_uint(v); }

template <int CTRL> __device__ __forceinline__ float dpp(float x) { return __builtin_bit_cast(float, __builtin_amdgcn_mov_dpp(__builtin_bit_cast(int, x), CTRL, 0xf, 0xf, true)); }
template <int CTRL> __device__ __forceinline__ unsigned dppu(unsigned x) { return (unsigned)__builtin_amdgcn_mov_dpp((int)x, CTRL, 0xf, 0xf, true); }
constexpr int XOR1 = 0xB1, XOR2 = 0x4E, ROR8 = 0x128, ROR4 = 0x124, HMIRROR = 0x141;
__device__ __forceinline__ float sw32_sum(float a, float b) { auto r = __builtin_amdgcn_permlane32_swap(f2u(a), f2u(b), false, false); return u2f(r[0]) + u2f(r[1]); }
__device__ __forceinline__ float sw16_sum(float a, float b) { auto r = __builtin_amdgcn_permlane16_swap(f2u(a), f2u(b), false, false); return u2f(r[0]) + u2f(r[1]); }
__device__ __forceinline__ float sw32_max(float a, float b) { auto r = __builtin_amdgcn_permlane32_swap(f2u(a), f2u(b), false, false); return fmaxf(u2f(r[0]), u2f(r[1])); }
__device__ __forceinline__ float sw16_max(float a, float b) { auto r = __builtin_amdgcn_permlane16_swap(f2u(a), f2u(b), false, false); return fmaxf(u2f(r[0]), u2f(r[1])); }
__device__ __forceinline__ float wave_sum(float x) {
  x += dpp<XOR1>(x); x += dpp<XOR2>(x); x += dpp<ROR4>(x); x += dpp<ROR8>(x);
  x = sw16_sum(x, x); return sw32_sum(x, x);
}
__device__ __forceinline__ f32x4 mfma32(v4u a, v4u b, f32x4 c) { return __builtin_amdgcn_mfma_f32_16x16x32_bf16(__builtin_bit_cast(bf16x8_t, a), __builtin_bit_cast(bf16x8_t, b), c, 0, 0, 0); }
__device__ __forceinline__ f32x4 mfma16(v2u a, v2u b, f32x4 c) { return __builtin_amdgcn_mfma_f32_16x16x16bf16_1k(__builtin_bit_cast(s16x4, a), __builtin_bit_cast(s16x4, b), c, 0, 0, 0); }
__device__ __forceinline__ void barrier() { asm volatile("s_waitcnt lgkmcnt(0)\n\ts_barrier" ::: "memory"); }
__device__ __forceinline__ v4u tr_frag(LAS unsigned char* lo, LAS unsigned char* hi) {
  const v2u a = __builtin_bit_cast(v2u, __builtin_amdgcn_ds_read_tr16_b64_v4i16((LAS s16x4*)lo));
  const v2u b = __builtin_bit_cast(v2u, __builtin_amdgcn_ds_read_tr16_b64_v4i16((LAS s16x4*)hi));
  return v4u{a[0], a[1], b[0], b[1]};
}

__device__ __forceinline__ v4u ln_row(v4u xv) {
  float x[8];
#pragma unroll
  for (int i = 0; i < 4; ++i) { x[2 * i] = bflo(xv[i]); x[2 * i + 1] = bfhi(xv[i]); }
  const float mean = wave_sum(((x[0] + x[1]) + (x[2] + x[3])) + ((x[4] + x[5]) + (x[6] + x[7]))) * (1.0f / D);
  float vs = 0.f;
#pragma unroll
  for (int i = 0; i < 8; ++i) { const float d = x[i] - mean; vs = fmaf(d, d, vs); }
  const float var = wave_sum(vs) * (1.0f / D);
  const float mb = rbf(mean);
  const float rs = __builtin_amdgcn_rcpf(rbf(__builtin_amdgcn_sqrtf(rbf(rbf(var) + 1e-5f))));
  v4u r;
#pragma unroll
  for (int i = 0; i < 4; ++i) { const unsigned d = cvtpk(x[2 * i] - mb, x[2 * i + 1] - mb); r[i] = cvtpk(bflo(d) * rs, bfhi(d) * rs); }
  return r;
}
__device__ __forceinline__ void glds16(const u16* gsrc, unsigned lds_dst) {
  unsigned keep;
  asm volatile("s_mov_b32 %0, m0\n\ts_mov_b32 m0, %2\n\ts_nop 0\n\tglobal_load_lds_dwordx4 %1, off nt\n\ts_mov_b32 m0, %0" : "=&s"(keep) : "v"(gsrc), "s"(lds_dst) : "memory");
}

#define STAMP(i) do { } while (0)
#define DUMP_STAMPS() do { } while (0)
#define LD128(off) __builtin_amdgcn_raw_buffer_load_b128(rw, (off), 0, AUX_SC1)
#define ST128(v, off) __builtin_amdgcn_raw_buffer_store_b128((v), rw, (off), 0, AUX_SC1)
#define SPIN_LOOP _Pragma("unroll 1") for (unsigned spins = 0; spins < SPIN_CAP; ++spins)
#define NOHOIST() asm volatile("" ::: "memory")

__global__ void __launch_bounds__(NTHR, 2) fused_block(const u16* __restrict__ x, const u16* __restrict__ kc, const u16* __restrict__ vc, const u16* __restrict__ wq,
                                                       const u16* __restrict__ wo, const u16* __restrict__ wgate, const u16* __restrict__ wup, const u16* __restrict__ wdown,
                                                       u16* __restrict__ out, unsigned char* ws TUNE_PARAM) {
  __shared__ __attribute__((aligned(16))) unsigned char smem[L_BYTES];
  LAS unsigned char* L = (LAS unsigned char*)smem;
  STAMP(0);
  const unsigned tstart = (unsigned)__builtin_amdgcn_s_memrealtime();
  const int wg = blockIdx.x, xcd = wg & 7, idx = wg >> 3;
  const int kvh = xcd, b = idx & 1, sp = idx >> 1;
  const int tid = threadIdx.x, lane = tid & 63, w = __builtin_amdgcn_readfirstlane(tid >> 6);
  const int c = lane & 15, g = lane >> 4;
  const int qc = xcd * 8 + (idx & 7), kq = idx >> 3;
  const __amdgpu_buffer_rsrc_t rw = __builtin_amdgcn_make_buffer_rsrc((void*)ws, 0, WS_BYTES, 0x00020000);
  const unsigned lds0 = (unsigned)(uintptr_t)L;
#define WAIT_UNTIL(t, slp) do { while ((int)((unsigned)__builtin_amdgcn_s_memrealtime() - (tstart + (unsigned)(t))) < 0) __builtin_amdgcn_s_sleep(slp); } while (0)

  if (w == 4) {
    __builtin_amdgcn_s_setprio(3);
    const v4u x0 = *(const GAS v4u*)(x + lane * 8), x1 = *(const GAS v4u*)(x + D + lane * 8);
    const unsigned tag = __builtin_amdgcn_raw_buffer_load_b32(rw, OFF_EPOCH, 0, AUX_SC1) + 1u;
    {
      const v4u n0 = ln_row(x0), n1 = ln_row(x1);
      *(LAS v4u*)(L + L_XN + lane * 16) = n0; *(LAS v4u*)(L + L_XN + 1024 + lane * 16) = n1;
      STAMP(9);
      barrier();
      barrier();
      f32x2 s = {0.f, 0.f};
#pragma unroll
      for (int ww = 0; ww < 4; ++ww) s += *(LAS f32x2*)(L + L_RED + ww * 512 + lane * 8);
      ST128((v4u{f2u(s[0]), tag, f2u(s[1]), tag}), OFF_Q + ((kq * 2 + (lane >> 5)) * 4096 + qc * 64 + (lane & 31) * 2) * 8);
    }
    STAMP(1);
    if (lane == 0) *(LAS unsigned*)(L + L_TAG) = tag;
    {
      const int qo = OFF_Q + (b * 4096 + kvh * 512) * 8 + lane * 16;
      v4u qv[16];
      unsigned okm = 0;
      WAIT_UNTIL(T_P, 2);
      SPIN_LOOP {
        NOHOIST();
#pragma unroll
        for (int j = 0; j < 16; ++j) if (!((okm >> j) & 1)) qv[j] = LD128(qo + (j >> 2) * 65536 + (j & 3) * 1024);
        okm = 0;
#pragma unroll
        for (int j = 0; j < 16; ++j) okm |= __all(tagged(qv[j], tag)) ? (1u << j) : 0u;
        if (okm == 0xffffu) break;
      }
#pragma unroll
      for (int i = 0; i < 4; ++i) {
        const float a0 = (u2f(qv[i][0]) + u2f(qv[4 + i][0])) + (u2f(qv[8 + i][0]) + u2f(qv[12 + i][0]));
        const float a1 = (u2f(qv[i][2]) + u2f(qv[4 + i][2])) + (u2f(qv[8 + i][2]) + u2f(qv[12 + i][2]));
        *(LAS unsigned*)(L + L_QF + i * 256 + lane * 4) = cvtpk(a0, a1);
      }
    }
    STAMP(2);
    barrier();
    return;
  }

  const int nc = 4 - b, nbw = 3 + 2 * b;
  const int role = sp < nc ? 1 : sp < nc + nbw ? 2 : (sp >= 8 && sp < 12) ? 3 : 0;
  const int ci = xcd * 7 + 4 * b + sp, bi = xcd * 8 + 3 * b + sp - nc, rj = xcd * 8 + idx - 16;
  const int nn = 16 - nc - nbw, sn = sp - nc - nbw;
  const int strest = 512 - nc * ST_C - nbw * ST_B, stn = b ? strest >> 3 : strest / 9, stx = strest - nn * stn;
  const int nstep = role == 1 ? ST_C : role == 2 ? ST_B : stn + (sn < stx ? 1 : 0);
  const int step0 = role == 1 ? ST_C * sp : role == 2 ? nc * ST_C + ST_B * (sp - nc) : nc * ST_C + nbw * ST_B + stn * sn + (sn < stx ? sn : stx);
  const size_t pbase = (size_t)(b * HKV + kvh) * SK * DH;
  const __amdgpu_buffer_rsrc_t rk = __builtin_amdgcn_make_buffer_rsrc((void*)(kc + pbase), 0, SK * DH * 2, 0x00020000);
  const __amdgpu_buffer_rsrc_t rv = __builtin_amdgcn_make_buffer_rsrc((void*)(vc + pbase), 0, SK * DH * 2, 0x00020000);
  const int koff = c * 256 + g * 16, voff = g * 1024 + c * 16;
  const unsigned sbase = (unsigned)(step0 * 64 + w * 16) * 256u;
  v4u kb[5][4], vb[5][4];
#define ISSUEK(d, so) do { _Pragma("unroll") for (int s = 0; s < 4; ++s) kb[d][s] = __builtin_amdgcn_raw_buffer_load_b128(rk, koff + s * 64, (so), KAUX); __builtin_amdgcn_sched_barrier(0); } while (0)
#define ISSUEV(d, so) do { _Pragma("unroll") for (int s = 0; s < 4; ++s) vb[d][s] = __builtin_amdgcn_raw_buffer_load_b128(rv, voff + s * 256, (so), VAUX); __builtin_amdgcn_sched_barrier(0); } while (0)
#define VMWAIT(n) do { asm volatile("s_waitcnt vmcnt(%0)" :: "i"(n) : "memory"); __builtin_amdgcn_sched_barrier(0); } while (0)
  const int q4 = (lane >> 2) & 3, p4 = lane & 3;
  {
    const int r8 = lane >> 3;
    const u16* p = wq + (size_t)(128 * kq + 32 * w + r8) * (HQ * DH) + qc * 64;
#pragma unroll
    for (int t = 0; t < 4; ++t) glds16(p + (size_t)t * 8 * (HQ * DH) + (((lane & 7) ^ ((r8 & 2) | ((t & 1) << 2))) * 8), lds0 + L_WQ + w * 4096 + t * 1024);
    ISSUEK(0, sbase); ISSUEV(0, sbase);
    STAMP(7);
    v4u fr[4];
    const int sw = (q4 & 2) | ((g & 1) << 2);
    VMWAIT(8);
#pragma unroll
    for (int t = 0; t < 4; ++t) {
      LAS unsigned char* a = L + L_WQ + w * 4096 + g * 1024 + q4 * 128 + (p4 & 1) * 8 + (((2 * t + (p4 >> 1)) ^ sw) * 16);
      fr[t] = tr_frag(a, a + 512);
    }
    STAMP(8);
    barrier();
    const v4u xb = *(LAS v4u*)(L + L_XN + (c & 1) * 1024 + (128 * kq + 32 * w + 8 * g) * 2);
#pragma unroll
    for (int t = 0; t < 4; ++t) {
      const f32x4 qa = mfma32(fr[t], xb, f32x4{0.f, 0.f, 0.f, 0.f});
      if (c < 2) *(LAS f32x4*)(L + L_RED + w * 512 + c * 256 + t * 64 + g * 16) = qa;
    }
    STAMP(1);
    barrier();
  }
  const bool odd = nstep & 1;
#pragma unroll
  for (int d = 1; d < 4; ++d) { VMWAIT(PACE_N); ISSUEK(d, sbase + d * 16384u); VMWAIT(PACE_N); ISSUEV(d, sbase + d * 16384u); }
  if (odd) { VMWAIT(PACE_N); ISSUEK(4, sbase + 4 * 16384u); VMWAIT(PACE_N); ISSUEV(4, sbase + 4 * 16384u); }
  STAMP(10);
  barrier();
  STAMP(2);
  v4u qf[4];
#pragma unroll
  for (int s = 0; s < 4; ++s) qf[s] = *(LAS v4u*)(L + L_QF + (c >> 2) * 256 + s * 64 + g * 16);
  const unsigned tag = __builtin_amdgcn_readfirstlane(*(LAS unsigned*)(L + L_TAG));

  const unsigned m0 = (c & 3) == 0 ? 0xffffu : (c & 3) == 1 ? 0xffff0000u : 0u;
  const unsigned m1 = (c & 3) == 2 ? 0xffffu : (c & 3) == 3 ? 0xffff0000u : 0u;
  constexpr float SCALE = 0.088388347648318440f, LOG2E = 1.4426950408889634f;
  f32x4 acc0 = {0.f, 0.f, 0.f, 0.f}, acc1 = {0.f, 0.f, 0.f, 0.f};
  float m = -INFINITY, l = 0.f;
  f32x4 sc;
#define QK(d) do { sc = f32x4{0.f, 0.f, 0.f, 0.f}; _Pragma("unroll") for (int s = 0; s < 4; ++s) sc = mfma32(kb[d][s], qf[s], sc); \
    asm volatile("" : "+v"(sc) :: "memory"); __builtin_amdgcn_sched_barrier(0); } while (0)
#define PV(d) do { \
    const unsigned r01 = cvtpk(sc[0], sc[1]), r23 = cvtpk(sc[2], sc[3]); \
    const unsigned t01 = cvtpk(bflo(r01) * SCALE, bfhi(r01) * SCALE), t23 = cvtpk(bflo(r23) * SCALE, bfhi(r23) * SCALE); \
    const float s0 = bflo(t01), s1 = bfhi(t01), s2 = bflo(t23), s3 = bfhi(t23); \
    float mx = fmaxf(fmaxf(s0, s1), fmaxf(s2, s3)); mx = sw16_max(mx, mx); mx = sw32_max(mx, mx); \
    if (__any(mx > m)) { const float mn = fmaxf(m, mx); const float al = __builtin_amdgcn_exp2f((m - mn) * LOG2E); m = mn; l *= al; acc0 *= al; acc1 *= al; } \
    const float sv[4] = {s0, s1, s2, s3}; \
    _Pragma("unroll") for (int i = 0; i < 4; ++i) { \
      const float p_ = __builtin_amdgcn_exp2f((sv[i] - m) * LOG2E); const unsigned pp = cvtpk(p_, p_); l += bflo(pp); \
      const v2u bb = {pp & m0, pp & m1}; const v4u vv = vb[d][i]; \
      acc0 = mfma16(v2u{vv[0], vv[1]}, bb, acc0); acc1 = mfma16(v2u{vv[2], vv[3]}, bb, acc1); } \
    asm volatile("" : "+v"(acc0), "+v"(acc1) :: "memory"); __builtin_amdgcn_sched_barrier(0); } while (0)
  const int ffo = (128 * w + (lane >> 2)) * F + ci * 32 + (((lane & 3) ^ ((lane >> 4) & 2)) * 8);
  const u16* wsrc = role == 1 ? wgate + ffo : wo + (size_t)(bi * 64 + g) * D + 128 * w + 8 * c;
  int wstr = role == 1 ? 16 * F : 4 * D;
  int wp = 0;
  const int wn = (ABL & 2) ? 0 : role == 1 ? 24 : role == 2 ? 16 : 0;
#define WPIECE() do { if (wp < wn) { glds16(wsrc, lds0 + L_W + w * WREG + wp * 1024); ++wp; wsrc += wstr; \
      if (role == 1 && wp == 8) wsrc = wup + ffo; \
      if (role == 1 && wp == 16) { wsrc = wdown + (size_t)(ci * 32 + g) * D + 128 * w + 8 * c; wstr = 4 * D; } } } while (0)
  unsigned so = sbase + (odd ? 5u : 4u) * 16384u;
  const unsigned send = sbase + (unsigned)nstep * 16384u;
  QK(0); ISSUEK(0, so); PV(0); ISSUEV(0, so); WPIECE();
  QK(1); ISSUEK(1, so + 16384u); PV(1); ISSUEV(1, so + 16384u); WPIECE();
  so += 32768u;
  QK(2); PV(2); QK(3); PV(3);
  if (odd) { QK(4); PV(4); }
#pragma unroll 1
  for (; so < send; so += 32768u) {
    QK(0); ISSUEK(0, so); WPIECE(); PV(0); ISSUEV(0, so); WPIECE();
    QK(1); ISSUEK(1, so + 16384u); PV(1); ISSUEV(1, so + 16384u); WPIECE();
  }
  QK(0); PV(0); QK(1); PV(1);
  STAMP(3);

  l = sw16_sum(l, l); l = sw32_sum(l, l);
  {
    LAS float* rec = (LAS float*)(L + L_MRG) + (w * 4 + (c >> 2)) * 132;
#pragma unroll
    for (int r = 0; r < 4; ++r) { rec[4 + 8 * (4 * g + r) + (c & 3)] = acc0[r]; rec[4 + 8 * (4 * g + r) + 4 + (c & 3)] = acc1[r]; }
    if (g == 0 && (c & 3) == 0) *(LAS f32x2*)rec = f32x2{m, l};
  }
  barrier();
  if (ABL & 4) WAIT_UNTIL(4600, 16);
  if (w < 2) {
    const int hd = 2 * w + (lane >> 5);
    f32x2 ml[4]; f32x4 ov[4];
#pragma unroll
    for (int ww = 0; ww < 4; ++ww) { LAS float* rec = (LAS float*)(L + L_MRG) + (ww * 4 + hd) * 132; ml[ww] = *(LAS f32x2*)rec; ov[ww] = *(LAS f32x4*)(rec + 4 + 4 * (lane & 31)); }
    const float M = fmaxf(fmaxf(ml[0][0], ml[1][0]), fmaxf(ml[2][0], ml[3][0]));
    float Ls = 0.f; f32x4 o = {0.f, 0.f, 0.f, 0.f};
#pragma unroll
    for (int ww = 0; ww < 4; ++ww) { const float wt = __builtin_amdgcn_exp2f((ml[ww][0] - M) * LOG2E); Ls = fmaf(wt, ml[ww][1], Ls); o += wt * ov[ww]; }
    o *= __builtin_amdgcn_rcpf(Ls);
    ST128((v4u{cvtpk(o[0], o[1]), tag, cvtpk(o[2], o[3]), tag}), OFF_PART + ((b * HQ + kvh * 4 + hd) * NSPLIT + sp) * 512 + (lane & 31) * 16);
    if ((lane & 31) == 0)
      __builtin_amdgcn_raw_buffer_store_b64(v2u{f2u(fmaf(M, LOG2E, __builtin_amdgcn_logf(Ls))), tag}, rw, OFF_LSE + (((b * HKV + kvh) * NSPLIT + sp) * 4 + hd) * 8, 0, AUX_SC1);
  }
  STAMP(4);
  if (ABL & 8) WAIT_UNTIL(4600, 16);
  if ((ABL & 16) && role != 2) WAIT_UNTIL(4600, 16);
  const unsigned dm0 = (c & 7) == 0 ? 0xffffu : (c & 7) == 1 ? 0xffff0000u : 0u, dm1 = (c & 7) == 2 ? 0xffffu : (c & 7) == 3 ? 0xffff0000u : 0u,
                 dm2 = (c & 7) == 4 ? 0xffffu : (c & 7) == 5 ? 0xffff0000u : 0u, dm3 = (c & 7) == 6 ? 0xffffu : (c & 7) == 7 ? 0xffff0000u : 0u;
#define DIAG(p) v4u{(p) & dm0, (p) & dm1, (p) & dm2, (p) & dm3}
#define PUBLISH_ACC(acc, base) do { ST128((v4u{f2u(acc[0]), tag, f2u(acc[1]), tag}), (base) + tid * 16); ST128((v4u{f2u(acc[2]), tag, f2u(acc[3]), tag}), (base) + 4096 + tid * 16); } while (0)

  if (role == 2) {
    v4u wr[16];
#pragma unroll
    for (int i = 0; i < 16; ++i) wr[i] = *(LAS v4u*)(L + L_W + w * WREG + i * 1024 + lane * 16);
    const int head = bi >> 1, s = lane >> 2;
    const int po = OFF_PART + (head * NSPLIT + s) * 512 + (bi & 1) * 256 + w * 64 + (lane & 3) * 16, lo = OFF_LSE + (((head >> 2) * NSPLIT + s) * 4 + (head & 3)) * 8;
    v4u a0, a1; v2u e0, e1;
    bool ok0 = false, ok1 = false;
    SPIN_LOOP {
      NOHOIST();
      if (!ok0) { a0 = LD128(po); e0 = __builtin_amdgcn_raw_buffer_load_b64(rw, lo, 0, AUX_SC1); }
      if (!ok1) { a1 = LD128(po + HQ * NSPLIT * 512); e1 = __builtin_amdgcn_raw_buffer_load_b64(rw, lo + HKV * NSPLIT * 32, 0, AUX_SC1); }
      ok0 = tagged(a0, tag) & (e0[1] == tag); ok1 = tagged(a1, tag) & (e1[1] == tag);
      if (__all(ok0 & ok1)) break;
    }
    STAMP(5);
    const float l0 = u2f(e0[0]), l1 = u2f(e1[0]);
    float x0m = fmaxf(l0, dpp<ROR4>(l0)), x1m = fmaxf(l1, dpp<ROR4>(l1));
    x0m = fmaxf(x0m, dpp<ROR8>(x0m)); x1m = fmaxf(x1m, dpp<ROR8>(x1m));
    x0m = sw16_max(x0m, x0m); x1m = sw16_max(x1m, x1m); x0m = sw32_max(x0m, x0m); x1m = sw32_max(x1m, x1m);
    const float w0 = __builtin_amdgcn_exp2f(l0 - x0m), w1 = __builtin_amdgcn_exp2f(l1 - x1m);
    const float v0 = sw32_sum(w0 * bflo(a0[0]), w1 * bflo(a1[0])), v1 = sw32_sum(w0 * bfhi(a0[0]), w1 * bfhi(a1[0])),
                v2 = sw32_sum(w0 * bflo(a0[2]), w1 * bflo(a1[2])), v3 = sw32_sum(w0 * bfhi(a0[2]), w1 * bfhi(a1[2]));
    float ws_ = sw32_sum(w0, w1);
    float u0 = sw16_sum(v0, v1), u1 = sw16_sum(v2, v3);
    ws_ = sw16_sum(ws_, ws_);
    u0 += dpp<ROR8>(u0); u1 += dpp<ROR8>(u1); ws_ += dpp<ROR8>(ws_);
    u0 += dpp<ROR4>(u0); u1 += dpp<ROR4>(u1); ws_ += dpp<ROR4>(ws_);
    const float uo = ((lane & 4) ? u1 : u0) * __builtin_amdgcn_rcpf(ws_);
    if ((lane & 8) == 0) *(LAS unsigned*)(L + L_OB + ((g >> 1) * 4 + (g & 1) + ((lane & 4) >> 1)) * 64 + (4 * w + (lane & 3)) * 4) = cvtpk(uo, uo);
    barrier();
    f32x4 acc = {0.f, 0.f, 0.f, 0.f};
#pragma unroll
    for (int q = 0; q < 4; ++q) {
      const v4u pv = *(LAS v4u*)(L + L_OB + ((c >> 3) * 4 + g) * 64 + q * 16);
#pragma unroll
      for (int i = 0; i < 4; ++i) acc = mfma32(wr[q * 4 + i], DIAG(pv[i]), acc);
    }
    PUBLISH_ACC(acc, OFF_P1 + bi * 8192);
    STAMP(6);
  } else if (role == 3) {
    const int rb = rj & 1, col0 = ((rj >> 1) & 15) * 32 + (rj >> 5) * 16;
    const unsigned xp = *(const GAS unsigned*)(x + rb * D + col0 + 2 * (lane & 7));
    const int pp = lane >> 3;
    float h0, h1;
    {
      const int po = OFF_P1 + (16 * w + pp) * 8192 + rj * 128 + (lane & 7) * 16;
      v4u a0, a1;
      bool ok0 = false, ok1 = false;
      SPIN_LOOP {
        NOHOIST();
        if (!ok0) a0 = LD128(po);
        if (!ok1) a1 = LD128(po + 8 * 8192);
        ok0 = tagged(a0, tag); ok1 = tagged(a1, tag);
        if (__all(ok0 & ok1)) break;
      }
      STAMP(5);
      float u = sw32_sum(u2f(a0[0]) + u2f(a1[0]), u2f(a0[2]) + u2f(a1[2]));
      u = sw16_sum(u, u); u += dpp<ROR8>(u);
      if ((lane & 24) == 0) *(LAS float*)(L + L_RR + w * 64 + ((lane >> 5) * 8 + (lane & 7)) * 4) = u;
      barrier();
      f32x2 s = {0.f, 0.f};
#pragma unroll
      for (int ww = 0; ww < 4; ++ww) s += *(LAS f32x2*)(L + L_RR + ww * 64 + (lane & 7) * 8);
      const unsigned at = cvtpk(s[0], s[1]);
      const unsigned hp = cvtpk(bflo(xp) + bflo(at), bfhi(xp) + bfhi(at));
      h0 = bflo(hp); h1 = bfhi(hp);
      if (w == 0) __builtin_amdgcn_raw_buffer_store_b64(v2u{hp, tag}, rw, OFF_HB + pp * 4096 + rb * 2048 + (col0 / 2 + (lane & 7)) * 8, 0, AUX_SC1);
      STAMP(6);
    }
    {
      const int po = OFF_P2 + (14 * w + pp) * 8192 + rj * 128 + (lane & 7) * 16;
      v4u a0, a1 = {0u, tag, 0u, tag};
      bool ok0 = false, ok1 = pp >= 6;
      SPIN_LOOP {
        NOHOIST();
        if (!ok0) a0 = LD128(po);
        if (!ok1) a1 = LD128(po + 8 * 8192);
        ok0 = tagged(a0, tag); ok1 = tagged(a1, tag);
        if (__all(ok0 & ok1)) break;
      }
      STAMP(7);
      float u = sw32_sum(u2f(a0[0]) + u2f(a1[0]), u2f(a0[2]) + u2f(a1[2]));
      u = sw16_sum(u, u); u += dpp<ROR8>(u);
      if ((lane & 24) == 0) *(LAS float*)(L + L_RR + 256 + w * 64 + ((lane >> 5) * 8 + (lane & 7)) * 4) = u;
      barrier();
      if (tid < 8) {
        f32x2 s = {0.f, 0.f};
#pragma unroll
        for (int ww = 0; ww < 4; ++ww) s += *(LAS f32x2*)(L + L_RR + 256 + ww * 64 + lane * 8);
        const unsigned dn = cvtpk(s[0], s[1]);
        *(GAS unsigned*)(out + rb * D + col0 + 2 * lane) = cvtpk(h0 + bflo(dn), h1 + bfhi(dn));
        if (rj == 0 && tid == 0) __builtin_amdgcn_raw_buffer_store_b32(tag, rw, OFF_EPOCH, 0, AUX_SC1);
      }
      STAMP(8);
    }
  } else if (role == 1) {
    v4u fg[8], fu[8], wd[8];
#pragma unroll
    for (int ks = 0; ks < 4; ++ks)
#pragma unroll
      for (int t = 0; t < 2; ++t) {
        LAS unsigned char* a = L + L_W + w * WREG + (32 * ks + 8 * g + q4) * 64 + (((2 * t + (p4 >> 1)) ^ ((g & 1) << 1)) * 16) + (p4 & 1) * 8;
        fg[ks * 2 + t] = tr_frag(a, a + 256); fu[ks * 2 + t] = tr_frag(a + 8192, a + 8192 + 256);
      }
#pragma unroll
    for (int i = 0; i < 8; ++i) wd[i] = *(LAS v4u*)(L + L_W + w * WREG + 16384 + i * 1024 + lane * 16);
    if (w < 2) {
      const int ho = OFF_HB + (ci & 7) * 4096 + w * 2048 + lane * 32;
      v4u ha, hb;
      bool ok = false;
      SPIN_LOOP {
        NOHOIST();
        if (!ok) { ha = LD128(ho); hb = LD128(ho + 16); }
        ok = tagged(ha, tag) & tagged(hb, tag);
        if (__all(ok)) break;
      }
      STAMP(5);
      *(LAS v4u*)(L + L_XN + w * 1024 + lane * 16) = ln_row(v4u{ha[0], ha[2], hb[0], hb[2]});
    }
    barrier();
    f32x4 ag[2], au[2];
#pragma unroll
    for (int t = 0; t < 2; ++t) { ag[t] = f32x4{0.f, 0.f, 0.f, 0.f}; au[t] = f32x4{0.f, 0.f, 0.f, 0.f}; }
#pragma unroll
    for (int ks = 0; ks < 4; ++ks) {
      const v4u xb = *(LAS v4u*)(L + L_XN + (c & 1) * 1024 + (128 * w + 32 * ks + 8 * g) * 2);
#pragma unroll
      for (int t = 0; t < 2; ++t) { ag[t] = mfma32(fg[ks * 2 + t], xb, ag[t]); au[t] = mfma32(fu[ks * 2 + t], xb, au[t]); }
    }
    {
      const f32x4 sel = (c & 4) ? ((c & 2) ? au[1] : au[0]) : ((c & 2) ? ag[1] : ag[0]);
      if (c < 8) {
#pragma unroll
        for (int i = 0; i < 4; ++i) *(LAS float*)(L + L_RED + (c >> 2) * 1024 + ((c & 1) * 32 + (c & 2) * 8 + 4 * g + i) * 16 + w * 4) = sel[i];
      }
    }
    barrier();
    const int fo = ((c >> 3) * 32 + 4 * (c & 7) + g) * 16;
    const f32x4 pg = *(LAS f32x4*)(L + L_RED + fo), pu = *(LAS f32x4*)(L + L_RED + 1024 + fo);
    const unsigned gu = cvtpk((pg[0] + pg[1]) + (pg[2] + pg[3]), (pu[0] + pu[1]) + (pu[2] + pu[3]));
    const float gg = bflo(gu);
    const float sg = rbf(gg * __builtin_amdgcn_rcpf(1.0f + __builtin_amdgcn_exp2f(-gg * LOG2E)));
    const float av = sg * bfhi(gu);
    const unsigned ap = cvtpk(av, av);
    const unsigned am = dppu<HMIRROR>(ap);
    const unsigned alo = (c & 4) ? am : ap, ahi = (c & 4) ? ap : am;
    f32x4 acc = {0.f, 0.f, 0.f, 0.f};
#define BC(v, t) ((c & 4) ? dppu<(3 - (t)) * 0x55>(v) : dppu<(t) * 0x55>(v))
#define BCH(v, t) ((c & 4) ? dppu<(t) * 0x55>(v) : dppu<(3 - (t)) * 0x55>(v))
    { const unsigned p = BC(alo, 0); acc = mfma32(wd[0], DIAG(p), acc); }
    { const unsigned p = BC(alo, 1); acc = mfma32(wd[1], DIAG(p), acc); }
    { const unsigned p = BC(alo, 2); acc = mfma32(wd[2], DIAG(p), acc); }
    { const unsigned p = BC(alo, 3); acc = mfma32(wd[3], DIAG(p), acc); }
    { const unsigned p = BCH(ahi, 0); acc = mfma32(wd[4], DIAG(p), acc); }
    { const unsigned p = BCH(ahi, 1); acc = mfma32(wd[5], DIAG(p), acc); }
    { const unsigned p = BCH(ahi, 2); acc = mfma32(wd[6], DIAG(p), acc); }
    { const unsigned p = BCH(ahi, 3); acc = mfma32(wd[7], DIAG(p), acc); }
    PUBLISH_ACC(acc, OFF_P2 + ci * 8192);
    STAMP(6);
  }
  DUMP_STAMPS();
}

extern "C" void kernel_launch(void* const* d_in, const int* in_sizes, int n_in, void* d_out, int out_size, void* d_ws, size_t ws_size, hipStream_t stream) {
  fused_block<<<NWG, NTHR, 0, stream>>>((const u16*)d_in[0], (const u16*)d_in[1], (const u16*)d_in[2], (const u16*)d_in[3], (const u16*)d_in[4],
                                        (const u16*)d_in[6], (const u16*)d_in[7], (const u16*)d_in[8], (u16*)d_out, (unsigned char*)d_ws TUNE_ARG);
}
```

```cpp
#include <hip/hip_runtime.h>
#include <hip/hip_bf16.h>
#include <cstdint>

typedef unsigned short u16;
typedef unsigned v4u __attribute__((ext_vector_type(4)));
typedef unsigned v2u __attribute__((ext_vector_type(2)));
typedef float f32x4 __attribute__((ext_vector_type(4)));
typedef float f32x2 __attribute__((ext_vector_type(2)));
typedef short s16x4 __attribute__((ext_vector_type(4)));
typedef __bf16 bf16x8_t __attribute__((ext_vector_type(8)));
typedef __bf16 bf16x2_t __attribute__((ext_vector_type(2)));
#define LAS __attribute__((address_space(3)))
#define GAS __attribute__((address_space(1)))

constexpr int D = 512, HQ = 32, HKV = 8, SK = 32768, DH = 128, F = 1792;
constexpr int NWG = 256, NTHR = 320, NSPLIT = 16;
#define TUNE_PARAM
#define TUNE_ARG
#define ABL 0
#define ST_C 29
#define ST_B 32
#define T_P 250
#define KAUX 2
#define VAUX 2
#define PACE_N 4
constexpr int NB = 64, NC = 56, NR = 64, NREP = 8;
constexpr int OFF_EPOCH = 0;
constexpr int OFF_LSE = 0x1000;
constexpr int OFF_HB = 0x4000;
constexpr int OFF_PART = 0x10000;
constexpr int OFF_P1 = 0x90000;
constexpr int OFF_P2 = 0x110000;
constexpr int OFF_Q = 0x180000;
constexpr int WS_BYTES = 0x1C0000, OFF_DBG = 0x200000;
constexpr int AUX_NT = 2, AUX_SC1 = 16;
constexpr unsigned SPIN_CAP = 1u << 16;
constexpr int L_W = 0;
constexpr int WREG = 24576;
constexpr int L_WQ = 4 * WREG;
constexpr int L_XN = L_WQ + 16384;
constexpr int L_RED = L_XN + 2048;
constexpr int L_TAG = L_RED + 2048;
constexpr int L_QF = L_TAG + 16;
constexpr int L_MRG = L_QF + 1024;
constexpr int L_OB = L_MRG + 4 * 4 * 132 * 4;
constexpr int L_RR = L_OB + 512;
constexpr int L_DBG = L_RR + 512;
constexpr int L_BYTES = L_DBG + 128;

__device__ __forceinline__ float bflo(unsigned v) { return __uint_as_float(v << 16); }
__device__ __forceinline__ float bfhi(unsigned v) { return __uint_as_float(v & 0xffff0000u); }
__device__ __forceinline__ unsigned cvtpk(float lo, float hi) { return __builtin_bit_cast(unsigned, __builtin_convertvector(f32x2{lo, hi}, bf16x2_t)); }
__device__ __forceinline__ float rbf(float x) { return bflo(cvtpk(x, x)); }
__device__ __forceinline__ bool tagged(v4u v, unsigned tag) { return (v[1] == tag) & (v[3] == tag); }
__device__ __forceinline__ float u2f(unsigned v) { return __uint_as_float(v); }
__device__ __forceinline__ unsigned f2u(float v) { return __float_as_uint(v); }

template <int CTRL> __device__ __forceinline__ float dpp(float x) { return __builtin_bit_cast(float, __builtin_amdgcn_mov_dpp(__builtin_bit_cast(int, x), CTRL, 0xf, 0xf, true)); }
template <int CTRL> __device__ __forceinline__ unsigned dppu(unsigned x) { return (unsigned)__builtin_amdgcn_mov_dpp((int)x, CTRL, 0xf, 0xf, true); }
constexpr int XOR1 = 0xB1, XOR2 = 0x4E, ROR8 = 0x128, ROR4 = 0x124, HMIRROR = 0x141;
__device__ __forceinline__ float sw32_sum(float a, float b) { auto r = __builtin_amdgcn_permlane32_swap(f2u(a), f2u(b), false, false); return u2f(r[0]) + u2f(r[1]); }
__device__ __forceinline__ float sw16_sum(float a, float b) { auto r = __builtin_amdgcn_permlane16_swap(f2u(a), f2u(b), false, false); return u2f(r[0]) + u2f(r[1]); }
__device__ __forceinline__ float sw32_max(float a, float b) { auto r = __builtin_amdgcn_permlane32_swap(f2u(a), f2u(b), false, false); return fmaxf(u2f(r[0]), u2f(r[1])); }
__device__ __forceinline__ float sw16_max(float a, float b) { auto r = __builtin_amdgcn_permlane16_swap(f2u(a), f2u(b), false, false); return fmaxf(u2f(r[0]), u2f(r[1])); }
__device__ __forceinline__ float wave_sum(float x) {
  x += dpp<XOR1>(x); x += dpp<XOR2>(x); x += dpp<ROR4>(x); x += dpp<ROR8>(x);
  x = sw16_sum(x, x); return sw32_sum(x, x);
}
__device__ __forceinline__ f32x4 mfma32(v4u a, v4u b, f32x4 c) { return __builtin_amdgcn_mfma_f32_16x16x32_bf16(__builtin_bit_cast(bf16x8_t, a), __builtin_bit_cast(bf16x8_t, b), c, 0, 0, 0); }
__device__ __forceinline__ f32x4 mfma16(v2u a, v2u b, f32x4 c) { return __builtin_amdgcn_mfma_f32_16x16x16bf16_1k(__builtin_bit_cast(s16x4, a), __builtin_bit_cast(s16x4, b), c, 0, 0, 0); }
__device__ __forceinline__ void barrier() { asm volatile("s_waitcnt lgkmcnt(0)\n\ts_barrier" ::: "memory"); }
__device__ __forceinline__ v4u tr_frag(LAS unsigned char* lo, LAS unsigned char* hi) {
  const v2u a = __builtin_bit_cast(v2u, __builtin_amdgcn_ds_read_tr16_b64_v4i16((LAS s16x4*)lo));
  const v2u b = __builtin_bit_cast(v2u, __builtin_amdgcn_ds_read_tr16_b64_v4i16((LAS s16x4*)hi));
  return v4u{a[0], a[1], b[0], b[1]};
}

__device__ __forceinline__ v4u ln_row(v4u xv) {
  float x[8];
#pragma unroll
  for (int i = 0; i < 4; ++i) { x[2 * i] = bflo(xv[i]); x[2 * i + 1] = bfhi(xv[i]); }
  const float mean = wave_sum(((x[0] + x[1]) + (x[2] + x[3])) + ((x[4] + x[5]) + (x[6] + x[7]))) * (1.0f / D);
  float vs = 0.f;
#pragma unroll
  for (int i = 0; i < 8; ++i) { const float d = x[i] - mean; vs = fmaf(d, d, vs); }
  const float var = wave_sum(vs) * (1.0f / D);
  const float mb = rbf(mean);
  const float rs = __builtin_amdgcn_rcpf(rbf(__builtin_amdgcn_sqrtf(rbf(rbf(var) + 1e-5f))));
  v4u r;
#pragma unroll
  for (int i = 0; i < 4; ++i) { const unsigned d = cvtpk(x[2 * i] - mb, x[2 * i + 1] - mb); r[i] = cvtpk(bflo(d) * rs, bfhi(d) * rs); }
  return r;
}
__device__ __forceinline__ void glds16(const u16* gsrc, unsigned lds_dst) {
  unsigned keep;
  asm volatile("s_mov_b32 %0, m0\n\ts_mov_b32 m0, %2\n\ts_nop 0\n\tglobal_load_lds_dwordx4 %1, off nt\n\ts_mov_b32 m0, %0" : "=&s"(keep) : "v"(gsrc), "s"(lds_dst) : "memory");
}

#define STAMP(i) do { } while (0)
#define DUMP_STAMPS() do { } while (0)
#define LD128(off) __builtin_amdgcn_raw_buffer_load_b128(rw, (off), 0, AUX_SC1)
#define ST128(v, off) __builtin_amdgcn_raw_buffer_store_b128((v), rw, (off), 0, AUX_SC1)
#define SPIN_LOOP _Pragma("unroll 1") for (unsigned spins = 0; spins < SPIN_CAP; ++spins)
#define NOHOIST() asm volatile("" ::: "memory")

__global__ void __launch_bounds__(NTHR, 2) fused_block(const u16* __restrict__ x, const u16* __restrict__ kc, const u16* __restrict__ vc, const u16* __restrict__ wq,
                                                       const u16* __restrict__ wo, const u16* __restrict__ wgate, const u16* __restrict__ wup, const u16* __restrict__ wdown,
                                                       u16* __restrict__ out, unsigned char* ws TUNE_PARAM) {
  __shared__ __attribute__((aligned(16))) unsigned char smem[L_BYTES];
  LAS unsigned char* L = (LAS unsigned char*)smem;
  STAMP(0);
  const unsigned tstart = (unsigned)__builtin_amdgcn_s_memrealtime();
  const int wg = blockIdx.x, xcd = wg & 7, idx = wg >> 3;
  const int kvh = xcd, b = idx & 1, sp = idx >> 1;
  const int tid = threadIdx.x, lane = tid & 63, w = __builtin_amdgcn_readfirstlane(tid >> 6);
  const int c = lane & 15, g = lane >> 4;
  const int qc = xcd * 8 + (idx & 7), kq = idx >> 3;
  const __amdgpu_buffer_rsrc_t rw = __builtin_amdgcn_make_buffer_rsrc((void*)ws, 0, WS_BYTES, 0x00020000);
  const unsigned lds0 = (unsigned)(uintptr_t)L;
#define WAIT_UNTIL(t, slp) do { while ((int)((unsigned)__builtin_amdgcn_s_memrealtime() - (tstart + (unsigned)(t))) < 0) __builtin_amdgcn_s_sleep(slp); } while (0)

  if (w == 4) {
    __builtin_amdgcn_s_setprio(3);
    const v4u x0 = *(const GAS v4u*)(x + lane * 8), x1 = *(const GAS v4u*)(x + D + lane * 8);
    const unsigned tag = __builtin_amdgcn_raw_buffer_load_b32(rw, OFF_EPOCH, 0, AUX_SC1) + 1u;
    {
      const v4u n0 = ln_row(x0), n1 = ln_row(x1);
      *(LAS v4u*)(L + L_XN + lane * 16) = n0; *(LAS v4u*)(L + L_XN + 1024 + lane * 16) = n1;
      STAMP(9);
      barrier();
      barrier();
      f32x2 s = {0.f, 0.f};
#pragma unroll
      for (int ww = 0; ww < 4; ++ww) s += *(LAS f32x2*)(L + L_RED + ww * 512 + lane * 8);
      ST128((v4u{f2u(s[0]), tag, f2u(s[1]), tag}), OFF_Q + ((kq * 2 + (lane >> 5)) * 4096 + qc * 64 + (lane & 31) * 2) * 8);
    }
    STAMP(1);
    if (lane == 0) __builtin_amdgcn_raw_buffer_store_b32(tstart, rw, 0x800 + wg * 4, 0, AUX_SC1);
    if (lane == 0) *(LAS unsigned*)(L + L_TAG) = tag;
    {
      const int qo = OFF_Q + (b * 4096 + kvh * 512) * 8 + lane * 16;
      v4u qv[16];
      unsigned okm = 0;
      WAIT_UNTIL(T_P, 2);
      SPIN_LOOP {
        NOHOIST();
#pragma unroll
        for (int j = 0; j < 16; ++j) if (!((okm >> j) & 1)) qv[j] = LD128(qo + (j >> 2) * 65536 + (j & 3) * 1024);
        okm = 0;
#pragma unroll
        for (int j = 0; j < 16; ++j) okm |= __all(tagged(qv[j], tag)) ? (1u << j) : 0u;
        if (okm == 0xffffu) break;
      }
#pragma unroll
      for (int i = 0; i < 4; ++i) {
        const float a0 = (u2f(qv[i][0]) + u2f(qv[4 + i][0])) + (u2f(qv[8 + i][0]) + u2f(qv[12 + i][0]));
        const float a1 = (u2f(qv[i][2]) + u2f(qv[4 + i][2])) + (u2f(qv[8 + i][2]) + u2f(qv[12 + i][2]));
        *(LAS unsigned*)(L + L_QF + i * 256 + lane * 4) = cvtpk(a0, a1);
      }
    }
    STAMP(2);
    barrier();
    return;
  }

  const int nc = 4 - b, nbw = 3 + 2 * b;
  const int role = sp < nc ? 1 : sp < nc + nbw ? 2 : (sp >= 8 && sp < 12) ? 3 : 0;
  const int ci = xcd * 7 + 4 * b + sp, bi = xcd * 8 + 3 * b + sp - nc, rj = xcd * 8 + idx - 16;
  const int nn = 16 - nc - nbw, sn = sp - nc - nbw;
  const int strest = 512 - nc * ST_C - nbw * ST_B, stn = b ? strest >> 3 : strest / 9, stx = strest - nn * stn;
  const int nstep = role == 1 ? ST_C : role == 2 ? ST_B : stn + (sn < stx ? 1 : 0);
  const int step0 = role == 1 ? ST_C * sp : role == 2 ? nc * ST_C + ST_B * (sp - nc) : nc * ST_C + nbw * ST_B + stn * sn + (sn < stx ? sn : stx);
  const size_t pbase = (size_t)(b * HKV + kvh) * SK * DH;
  const __amdgpu_buffer_rsrc_t rk = __builtin_amdgcn_make_buffer_rsrc((void*)(kc + pbase), 0, SK * DH * 2, 0x00020000);
  const __amdgpu_buffer_rsrc_t rv = __builtin_amdgcn_make_buffer_rsrc((void*)(vc + pbase), 0, SK * DH * 2, 0x00020000);
  const int koff = c * 256 + g * 16, voff = g * 1024 + c * 16;
  const unsigned sbase = (unsigned)(step0 * 64 + w * 16) * 256u;
  v4u kb[5][4], vb[5][4];
#define ISSUEK(d, so) do { _Pragma("unroll") for (int s = 0; s < 4; ++s) kb[d][s] = __builtin_amdgcn_raw_buffer_load_b128(rk, koff + s * 64, (so), KAUX); __builtin_amdgcn_sched_barrier(0); } while (0)
#define ISSUEV(d, so) do { _Pragma("unroll") for (int s = 0; s < 4; ++s) vb[d][s] = __builtin_amdgcn_raw_buffer_load_b128(rv, voff + s * 256, (so), VAUX); __builtin_amdgcn_sched_barrier(0); } while (0)
#define VMWAIT(n) do { asm volatile("s_waitcnt vmcnt(%0)" :: "i"(n) : "memory"); __builtin_amdgcn_sched_barrier(0); } while (0)
  const int q4 = (lane >> 2) & 3, p4 = lane & 3;
  {
    const int r8 = lane >> 3;
    const u16* p = wq + (size_t)(128 * kq + 32 * w + r8) * (HQ * DH) + qc * 64;
#pragma unroll
    for (int t = 0; t < 4; ++t) glds16(p + (size_t)t * 8 * (HQ * DH) + (((lane & 7) ^ ((r8 & 2) | ((t & 1) << 2))) * 8), lds0 + L_WQ + w * 4096 + t * 1024);
    ISSUEK(0, sbase); ISSUEV(0, sbase);
    STAMP(7);
    v4u fr[4];
    const int sw = (q4 & 2) | ((g & 1) << 2);
    VMWAIT(8);
#pragma unroll
    for (int t = 0; t < 4; ++t) {
      LAS unsigned char* a = L + L_WQ + w * 4096 + g * 1024 + q4 * 128 + (p4 & 1) * 8 + (((2 * t + (p4 >> 1)) ^ sw) * 16);
      fr[t] = tr_frag(a, a + 512);
    }
    STAMP(8);
    barrier();
    const v4u xb = *(LAS v4u*)(L + L_XN + (c & 1) * 1024 + (128 * kq + 32 * w + 8 * g) * 2);
#pragma unroll
    for (int t = 0; t < 4; ++t) {
      const f32x4 qa = mfma32(fr[t], xb, f32x4{0.f, 0.f, 0.f, 0.f});
      if (c < 2) *(LAS f32x4*)(L + L_RED + w * 512 + c * 256 + t * 64 + g * 16) = qa;
    }
    STAMP(1);
    barrier();
  }
  const bool odd = nstep & 1;
#pragma unroll
  for (int d = 1; d < 4; ++d) { VMWAIT(PACE_N); ISSUEK(d, sbase + d * 16384u); VMWAIT(PACE_N); ISSUEV(d, sbase + d * 16384u); }
  if (odd) { VMWAIT(PACE_N); ISSUEK(4, sbase + 4 * 16384u); VMWAIT(PACE_N); ISSUEV(4, sbase + 4 * 16384u); }
  STAMP(10);
  barrier();
  STAMP(2);
  v4u qf[4];
#pragma unroll
  for (int s = 0; s < 4; ++s) qf[s] = *(LAS v4u*)(L + L_QF + (c >> 2) * 256 + s * 64 + g * 16);
  const unsigned tag = __builtin_amdgcn_readfirstlane(*(LAS unsigned*)(L + L_TAG));

  const unsigned m0 = (c & 3) == 0 ? 0xffffu : (c & 3) == 1 ? 0xffff0000u : 0u;
  const unsigned m1 = (c & 3) == 2 ? 0xffffu : (c & 3) == 3 ? 0xffff0000u : 0u;
  constexpr float SCALE = 0.088388347648318440f, LOG2E = 1.4426950408889634f;
  f32x4 acc0 = {0.f, 0.f, 0.f, 0.f}, acc1 = {0.f, 0.f, 0.f, 0.f};
  float m = -INFINITY, l = 0.f;
  f32x4 sc;
#define QK(d) do { sc = f32x4{0.f, 0.f, 0.f, 0.f}; _Pragma("unroll") for (int s = 0; s < 4; ++s) sc = mfma32(kb[d][s], qf[s], sc); \
    asm volatile("" : "+v"(sc) :: "memory"); __builtin_amdgcn_sched_barrier(0); } while (0)
#define PV(d) do { \
    const unsigned r01 = cvtpk(sc[0], sc[1]), r23 = cvtpk(sc[2], sc[3]); \
    const unsigned t01 = cvtpk(bflo(r01) * SCALE, bfhi(r01) * SCALE), t23 = cvtpk(bflo(r23) * SCALE, bfhi(r23) * SCALE); \
    const float s0 = bflo(t01), s1 = bfhi(t01), s2 = bflo(t23), s3 = bfhi(t23); \
    float mx = fmaxf(fmaxf(s0, s1), fmaxf(s2, s3)); mx = sw16_max(mx, mx); mx = sw32_max(mx, mx); \
    if (__any(mx > m)) { const float mn = fmaxf(m, mx); const float al = __builtin_amdgcn_exp2f((m - mn) * LOG2E); m = mn; l *= al; acc0 *= al; acc1 *= al; } \
    const float sv[4] = {s0, s1, s2, s3}; \
    _Pragma("unroll") for (int i = 0; i < 4; ++i) { \
      const float p_ = __builtin_amdgcn_exp2f((sv[i] - m) * LOG2E); const unsigned pp = cvtpk(p_, p_); l += bflo(pp); \
      const v2u bb = {pp & m0, pp & m1}; const v4u vv = vb[d][i]; \
      acc0 = mfma16(v2u{vv[0], vv[1]}, bb, acc0); acc1 = mfma16(v2u{vv[2], vv[3]}, bb, acc1); } \
    asm volatile("" : "+v"(acc0), "+v"(acc1) :: "memory"); __builtin_amdgcn_sched_barrier(0); } while (0)
  const int ffo = (128 * w + (lane >> 2)) * F + ci * 32 + (((lane & 3) ^ ((lane >> 4) & 2)) * 8);
  const u16* wsrc = role == 1 ? wgate + ffo : wo + (size_t)(bi * 64 + g) * D + 128 * w + 8 * c;
  int wstr = role == 1 ? 16 * F : 4 * D;
  int wp = 0;
  const int wn = (ABL & 2) ? 0 : role == 1 ? 24 : role == 2 ? 16 : 0;
#define WPIECE() do { if (wp < wn) { glds16(wsrc, lds0 + L_W + w * WREG + wp * 1024); ++wp; wsrc += wstr; \
      if (role == 1 && wp == 8) wsrc = wup + ffo; \
      if (role == 1 && wp == 16) { wsrc = wdown + (size_t)(ci * 32 + g) * D + 128 * w + 8 * c; wstr = 4 * D; } } } while (0)
  unsigned so = sbase + (odd ? 5u : 4u) * 16384u;
  const unsigned send = sbase + (unsigned)nstep * 16384u;
  QK(0); ISSUEK(0, so); PV(0); ISSUEV(0, so); WPIECE();
  QK(1); ISSUEK(1, so + 16384u); PV(1); ISSUEV(1, so + 16384u); WPIECE();
  so += 32768u;
  QK(2); PV(2); QK(3); PV(3);
  if (odd) { QK(4); PV(4); }
#pragma unroll 1
  for (; so < send; so += 32768u) {
    QK(0); ISSUEK(0, so); WPIECE(); PV(0); ISSUEV(0, so); WPIECE();
    QK(1); ISSUEK(1, so + 16384u); PV(1); ISSUEV(1, so + 16384u); WPIECE();
  }
  QK(0); PV(0); QK(1); PV(1);
  STAMP(3);

  l = sw16_sum(l, l); l = sw32_sum(l, l);
  {
    LAS float* rec = (LAS float*)(L + L_MRG) + (w * 4 + (c >> 2)) * 132;
#pragma unroll
    for (int r = 0; r < 4; ++r) { rec[4 + 8 * (4 * g + r) + (c & 3)] = acc0[r]; rec[4 + 8 * (4 * g + r) + 4 + (c & 3)] = acc1[r]; }
    if (g == 0 && (c & 3) == 0) *(LAS f32x2*)rec = f32x2{m, l};
  }
  barrier();
  if (ABL & 4) WAIT_UNTIL(4600, 16);
  if (w < 2) {
    const int hd = 2 * w + (lane >> 5);
    f32x2 ml[4]; f32x4 ov[4];
#pragma unroll
    for (int ww = 0; ww < 4; ++ww) { LAS float* rec = (LAS float*)(L + L_MRG) + (ww * 4 + hd) * 132; ml[ww] = *(LAS f32x2*)rec; ov[ww] = *(LAS f32x4*)(rec + 4 + 4 * (lane & 31)); }
    const float M = fmaxf(fmaxf(ml[0][0], ml[1][0]), fmaxf(ml[2][0], ml[3][0]));
    float Ls = 0.f; f32x4 o = {0.f, 0.f, 0.f, 0.f};
#pragma unroll
    for (int ww = 0; ww < 4; ++ww) { const float wt = __builtin_amdgcn_exp2f((ml[ww][0] - M) * LOG2E); Ls = fmaf(wt, ml[ww][1], Ls); o += wt * ov[ww]; }
    o *= __builtin_amdgcn_rcpf(Ls);
    ST128((v4u{cvtpk(o[0], o[1]), tag, cvtpk(o[2], o[3]), tag}), OFF_PART + ((b * HQ + kvh * 4 + hd) * NSPLIT + sp) * 512 + (lane & 31) * 16);
    if ((lane & 31) == 0)
      __builtin_amdgcn_raw_buffer_store_b64(v2u{f2u(fmaf(M, LOG2E, __builtin_amdgcn_logf(Ls))), tag}, rw, OFF_LSE + (((b * HKV + kvh) * NSPLIT + sp) * 4 + hd) * 8, 0, AUX_SC1);
  }
  STAMP(4);
  if (ABL & 8) WAIT_UNTIL(4600, 16);
  if ((ABL & 16) && role != 2) WAIT_UNTIL(4600, 16);
  const unsigned dm0 = (c & 7) == 0 ? 0xffffu : (c & 7) == 1 ? 0xffff0000u : 0u, dm1 = (c & 7) == 2 ? 0xffffu : (c & 7) == 3 ? 0xffff0000u : 0u,
                 dm2 = (c & 7) == 4 ? 0xffffu : (c & 7) == 5 ? 0xffff0000u : 0u, dm3 = (c & 7) == 6 ? 0xffffu : (c & 7) == 7 ? 0xffff0000u : 0u;
#define DIAG(p) v4u{(p) & dm0, (p) & dm1, (p) & dm2, (p) & dm3}
#define PUBLISH_ACC(acc, base) do { ST128((v4u{f2u(acc[0]), tag, f2u(acc[1]), tag}), (base) + tid * 16); ST128((v4u{f2u(acc[2]), tag, f2u(acc[3]), tag}), (base) + 4096 + tid * 16); } while (0)

  if (role == 2) {
    v4u wr[16];
#pragma unroll
    for (int i = 0; i < 16; ++i) wr[i] = *(LAS v4u*)(L + L_W + w * WREG + i * 1024 + lane * 16);
    const int head = bi >> 1, s = lane >> 2;
    const int po = OFF_PART + (head * NSPLIT + s) * 512 + (bi & 1) * 256 + w * 64 + (lane & 3) * 16, lo = OFF_LSE + (((head >> 2) * NSPLIT + s) * 4 + (head & 3)) * 8;
    v4u a0, a1; v2u e0, e1;
    bool ok0 = false, ok1 = false;
    SPIN_LOOP {
      NOHOIST();
      if (!ok0) { a0 = LD128(po); e0 = __builtin_amdgcn_raw_buffer_load_b64(rw, lo, 0, AUX_SC1); }
      if (!ok1) { a1 = LD128(po + HQ * NSPLIT * 512); e1 = __builtin_amdgcn_raw_buffer_load_b64(rw, lo + HKV * NSPLIT * 32, 0, AUX_SC1); }
      ok0 = tagged(a0, tag) & (e0[1] == tag); ok1 = tagged(a1, tag) & (e1[1] == tag);
      if (__all(ok0 & ok1)) break;
    }
    STAMP(5);
    const float l0 = u2f(e0[0]), l1 = u2f(e1[0]);
    float x0m = fmaxf(l0, dpp<ROR4>(l0)), x1m = fmaxf(l1, dpp<ROR4>(l1));
    x0m = fmaxf(x0m, dpp<ROR8>(x0m)); x1m = fmaxf(x1m, dpp<ROR8>(x1m));
    x0m = sw16_max(x0m, x0m); x1m = sw16_max(x1m, x1m); x0m = sw32_max(x0m, x0m); x1m = sw32_max(x1m, x1m);
    const float w0 = __builtin_amdgcn_exp2f(l0 - x0m), w1 = __builtin_amdgcn_exp2f(l1 - x1m);
    const float v0 = sw32_sum(w0 * bflo(a0[0]), w1 * bflo(a1[0])), v1 = sw32_sum(w0 * bfhi(a0[0]), w1 * bfhi(a1[0])),
                v2 = sw32_sum(w0 * bflo(a0[2]), w1 * bflo(a1[2])), v3 = sw32_sum(w0 * bfhi(a0[2]), w1 * bfhi(a1[2]));
    float ws_ = sw32_sum(w0, w1);
    float u0 = sw16_sum(v0, v1), u1 = sw16_sum(v2, v3);
    ws_ = sw16_sum(ws_, ws_);
    u0 += dpp<ROR8>(u0); u1 += dpp<ROR8>(u1); ws_ += dpp<ROR8>(ws_);
    u0 += dpp<ROR4>(u0); u1 += dpp<ROR4>(u1); ws_ += dpp<ROR4>(ws_);
    const float uo = ((lane & 4) ? u1 : u0) * __builtin_amdgcn_rcpf(ws_);
    if ((lane & 8) == 0) *(LAS unsigned*)(L + L_OB + ((g >> 1) * 4 + (g & 1) + ((lane & 4) >> 1)) * 64 + (4 * w + (lane & 3)) * 4) = cvtpk(uo, uo);
    barrier();
    f32x4 acc = {0.f, 0.f, 0.f, 0.f};
#pragma unroll
    for (int q = 0; q < 4; ++q) {
      const v4u pv = *(LAS v4u*)(L + L_OB + ((c >> 3) * 4 + g) * 64 + q * 16);
#pragma unroll
      for (int i = 0; i < 4; ++i) acc = mfma32(wr[q * 4 + i], DIAG(pv[i]), acc);
    }
    PUBLISH_ACC(acc, OFF_P1 + bi * 8192);
    STAMP(6);
  } else if (role == 3) {
    const int rb = rj & 1, col0 = ((rj >> 1) & 15) * 32 + (rj >> 5) * 16;
    const unsigned xp = *(const GAS unsigned*)(x + rb * D + col0 + 2 * (lane & 7));
    const int pp = lane >> 3;
    float h0, h1;
    {
      const int po = OFF_P1 + (16 * w + pp) * 8192 + rj * 128 + (lane & 7) * 16;
      v4u a0, a1;
      bool ok0 = false, ok1 = false;
      SPIN_LOOP {
        NOHOIST();
        if (!ok0) a0 = LD128(po);
        if (!ok1) a1 = LD128(po + 8 * 8192);
        ok0 = tagged(a0, tag); ok1 = tagged(a1, tag);
        if (__all(ok0 & ok1)) break;
      }
      STAMP(5);
      float u = sw32_sum(u2f(a0[0]) + u2f(a1[0]), u2f(a0[2]) + u2f(a1[2]));
      u = sw16_sum(u, u); u += dpp<ROR8>(u);
      if ((lane & 24) == 0) *(LAS float*)(L + L_RR + w * 64 + ((lane >> 5) * 8 + (lane & 7)) * 4) = u;
      barrier();
      f32x2 s = {0.f, 0.f};
#pragma unroll
      for (int ww = 0; ww < 4; ++ww) s += *(LAS f32x2*)(L + L_RR + ww * 64 + (lane & 7) * 8);
      const unsigned at = cvtpk(s[0], s[1]);
      const unsigned hp = cvtpk(bflo(xp) + bflo(at), bfhi(xp) + bfhi(at));
      h0 = bflo(hp); h1 = bfhi(hp);
      if (w == 0) __builtin_amdgcn_raw_buffer_store_b64(v2u{hp, tag}, rw, OFF_HB + pp * 4096 + rb * 2048 + (col0 / 2 + (lane & 7)) * 8, 0, AUX_SC1);
      STAMP(6);
    }
    {
      const int po = OFF_P2 + (14 * w + pp) * 8192 + rj * 128 + (lane & 7) * 16;
      v4u a0, a1 = {0u, tag, 0u, tag};
      bool ok0 = false, ok1 = pp >= 6;
      SPIN_LOOP {
        NOHOIST();
        if (!ok0) a0 = LD128(po);
        if (!ok1) a1 = LD128(po + 8 * 8192);
        ok0 = tagged(a0, tag); ok1 = tagged(a1, tag);
        if (__all(ok0 & ok1)) break;
      }
      STAMP(7);
      float u = sw32_sum(u2f(a0[0]) + u2f(a1[0]), u2f(a0[2]) + u2f(a1[2]));
      u = sw16_sum(u, u); u += dpp<ROR8>(u);
      if ((lane & 24) == 0) *(LAS float*)(L + L_RR + 256 + w * 64 + ((lane >> 5) * 8 + (lane & 7)) * 4) = u;
      barrier();
      if (tid < 8) {
        f32x2 s = {0.f, 0.f};
#pragma unroll
        for (int ww = 0; ww < 4; ++ww) s += *(LAS f32x2*)(L + L_RR + 256 + ww * 64 + lane * 8);
        const unsigned dn = cvtpk(s[0], s[1]);
        *(GAS unsigned*)(out + rb * D + col0 + 2 * lane) = cvtpk(h0 + bflo(dn), h1 + bfhi(dn));
        if (rj == 0 && tid == 0) __builtin_amdgcn_raw_buffer_store_b32(tag, rw, OFF_EPOCH, 0, AUX_SC1);
      }
      STAMP(8);
    }
  } else if (role == 1) {
    v4u fg[8], fu[8], wd[8];
#pragma unroll
    for (int ks = 0; ks < 4; ++ks)
#pragma unroll
      for (int t = 0; t < 2; ++t) {
        LAS unsigned char* a = L + L_W + w * WREG + (32 * ks + 8 * g + q4) * 64 + (((2 * t + (p4 >> 1)) ^ ((g & 1) << 1)) * 16) + (p4 & 1) * 8;
        fg[ks * 2 + t] = tr_frag(a, a + 256); fu[ks * 2 + t] = tr_frag(a + 8192, a + 8192 + 256);
      }
#pragma unroll
    for (int i = 0; i < 8; ++i) wd[i] = *(LAS v4u*)(L + L_W + w * WREG + 16384 + i * 1024 + lane * 16);
    if (w < 2) {
      const int ho = OFF_HB + (ci & 7) * 4096 + w * 2048 + lane * 32;
      v4u ha, hb;
      bool ok = false;
      SPIN_LOOP {
        NOHOIST();
        if (!ok) { ha = LD128(ho); hb = LD128(ho + 16); }
        ok = tagged(ha, tag) & tagged(hb, tag);
        if (__all(ok)) break;
      }
      STAMP(5);
      *(LAS v4u*)(L + L_XN + w * 1024 + lane * 16) = ln_row(v4u{ha[0], ha[2], hb[0], hb[2]});
    }
    barrier();
    f32x4 ag[2], au[2];
#pragma unroll
    for (int t = 0; t < 2; ++t) { ag[t] = f32x4{0.f, 0.f, 0.f, 0.f}; au[t] = f32x4{0.f, 0.f, 0.f, 0.f}; }
#pragma unroll
    for (int ks = 0; ks < 4; ++ks) {
      const v4u xb = *(LAS v4u*)(L + L_XN + (c & 1) * 1024 + (128 * w + 32 * ks + 8 * g) * 2);
#pragma unroll
      for (int t = 0; t < 2; ++t) { ag[t] = mfma32(fg[ks * 2 + t], xb, ag[t]); au[t] = mfma32(fu[ks * 2 + t], xb, au[t]); }
    }
    {
      const f32x4 sel = (c & 4) ? ((c & 2) ? au[1] : au[0]) : ((c & 2) ? ag[1] : ag[0]);
      if (c < 8) {
#pragma unroll
        for (int i = 0; i < 4; ++i) *(LAS float*)(L + L_RED + (c >> 2) * 1024 + ((c & 1) * 32 + (c & 2) * 8 + 4 * g + i) * 16 + w * 4) = sel[i];
      }
    }
    barrier();
    const int fo = ((c >> 3) * 32 + 4 * (c & 7) + g) * 16;
    const f32x4 pg = *(LAS f32x4*)(L + L_RED + fo), pu = *(LAS f32x4*)(L + L_RED + 1024 + fo);
    const unsigned gu = cvtpk((pg[0] + pg[1]) + (pg[2] + pg[3]), (pu[0] + pu[1]) + (pu[2] + pu[3]));
    const float gg = bflo(gu);
    const float sg = rbf(gg * __builtin_amdgcn_rcpf(1.0f + __builtin_amdgcn_exp2f(-gg * LOG2E)));
    const float av = sg * bfhi(gu);
    const unsigned ap = cvtpk(av, av);
    const unsigned am = dppu<HMIRROR>(ap);
    const unsigned alo = (c & 4) ? am : ap, ahi = (c & 4) ? ap : am;
    f32x4 acc = {0.f, 0.f, 0.f, 0.f};
#define BC(v, t) ((c & 4) ? dppu<(3 - (t)) * 0x55>(v) : dppu<(t) * 0x55>(v))
#define BCH(v, t) ((c & 4) ? dppu<(t) * 0x55>(v) : dppu<(3 - (t)) * 0x55>(v))
    { const unsigned p = BC(alo, 0); acc = mfma32(wd[0], DIAG(p), acc); }
    { const unsigned p = BC(alo, 1); acc = mfma32(wd[1], DIAG(p), acc); }
    { const unsigned p = BC(alo, 2); acc = mfma32(wd[2], DIAG(p), acc); }
    { const unsigned p = BC(alo, 3); acc = mfma32(wd[3], DIAG(p), acc); }
    { const unsigned p = BCH(ahi, 0); acc = mfma32(wd[4], DIAG(p), acc); }
    { const unsigned p = BCH(ahi, 1); acc = mfma32(wd[5], DIAG(p), acc); }
    { const unsigned p = BCH(ahi, 2); acc = mfma32(wd[6], DIAG(p), acc); }
    { const unsigned p = BCH(ahi, 3); acc = mfma32(wd[7], DIAG(p), acc); }
    PUBLISH_ACC(acc, OFF_P2 + ci * 8192);
    STAMP(6);
  }
  if (role == 3 && rj == 0 && w == 0) {
    const v4u ts = LD128(0x800 + lane * 16);
    int mn = 0x7fffffff, mx = -0x7fffffff;
    for (int i = 0; i < 4; ++i) { const int d = (int)(ts[i] - tstart); mn = d < mn ? d : mn; mx = d > mx ? d : mx; }
    for (int o = 32; o >= 1; o >>= 1) { const int a = __shfl_xor(mn, o), bb = __shfl_xor(mx, o); mn = a < mn ? a : mn; mx = bb > mx ? bb : mx; }
    mn = __builtin_amdgcn_readfirstlane(mn); mx = __builtin_amdgcn_readfirstlane(mx);
    const int now = (int)((unsigned)__builtin_amdgcn_s_memrealtime() - tstart);
    WAIT_UNTIL(7000 + mn, 1);
  }
}

extern "C" void kernel_launch(void* const* d_in, const int* in_sizes, int n_in, void* d_out, int out_size, void* d_ws, size_t ws_size, hipStream_t stream) {
  fused_block<<<NWG, NTHR, 0, stream>>>((const u16*)d_in[0], (const u16*)d_in[1], (const u16*)d_in[2], (const u16*)d_in[3], (const u16*)d_in[4],
                                        (const u16*)d_in[6], (const u16*)d_in[7], (const u16*)d_in[8], (u16*)d_out, (unsigned char*)d_ws TUNE_ARG);
}
```

```cpp
#include <hip/hip_runtime.h>
#include <hip/hip_bf16.h>
#include <cstdint>

typedef unsigned short u16;
typedef unsigned v4u __attribute__((ext_vector_type(4)));
typedef unsigned v2u __attribute__((ext_vector_type(2)));
typedef float f32x4 __attribute__((ext_vector_type(4)));
typedef float f32x2 __attribute__((ext_vector_type(2)));
typedef short s16x4 __attribute__((ext_vector_type(4)));
typedef __bf16 bf16x8_t __attribute__((ext_vector_type(8)));
typedef __bf16 bf16x2_t __attribute__((ext_vector_type(2)));
#define LAS __attribute__((address_space(3)))
#define GAS __attribute__((address_space(1)))

constexpr int D = 512, HQ = 32, HKV = 8, SK = 32768, DH = 128, F = 1792;
constexpr int NWG = 256, NTHR = 320, NSPLIT = 16;
#define TUNE_PARAM
#define TUNE_ARG
#define ABL 0
#define ST_C 29
#define ST_B 32
#define T_P 250
#define KAUX 2
#define VAUX 2
#define PACE_N 4
constexpr int NB = 64, NC = 56, NR = 64, NREP = 8;
constexpr int OFF_EPOCH = 0;
constexpr int OFF_LSE = 0x1000;
constexpr int OFF_HB = 0x4000;
constexpr int OFF_PART = 0x10000;
constexpr int OFF_P1 = 0x90000;
constexpr int OFF_P2 = 0x110000;
constexpr int OFF_Q = 0x180000;
constexpr int WS_BYTES = 0x1C0000, OFF_DBG = 0x200000;
constexpr int AUX_NT = 2, AUX_SC1 = 16;
constexpr unsigned SPIN_CAP = 1u << 16;
constexpr int L_W = 0;
constexpr int WREG = 24576;
constexpr int L_WQ = 4 * WREG;
constexpr int L_XN = L_WQ + 16384;
constexpr int L_RED = L_XN + 2048;
constexpr int L_TAG = L_RED + 2048;
constexpr int L_QF = L_TAG + 16;
constexpr int L_MRG = L_QF + 1024;
constexpr int L_OB = L_MRG + 4 * 4 * 132 * 4;
constexpr int L_RR = L_OB + 512;
constexpr int L_DBG = L_RR + 512;
constexpr int L_BYTES = L_DBG + 128;

__device__ __forceinline__ float bflo(unsigned v) { return __uint_as_float(v << 16); }
__device__ __forceinline__ float bfhi(unsigned v) { return __uint_as_float(v & 0xffff0000u); }
__device__ __forceinline__ unsigned cvtpk(float lo, float hi) { return __builtin_bit_cast(unsigned, __builtin_convertvector(f32x2{lo, hi}, bf16x2_t)); }
__device__ __forceinline__ float rbf(float x) { return bflo(cvtpk(x, x)); }
__device__ __forceinline__ bool tagged(v4u v, unsigned tag) { return (v[1] == tag) & (v[3] == tag); }
__device__ __forceinline__ float u2f(unsigned v) { return __uint_as_float(v); }
__device__ __forceinline__ unsigned f2u(float v) { return __float_as_uint(v); }

template <int CTRL> __device__ __forceinline__ float dpp(float x) { return __builtin_bit_cast(float, __builtin_amdgcn_mov_dpp(__builtin_bit_cast(int, x), CTRL, 0xf, 0xf, true)); }
template <int CTRL> __device__ __forceinline__ unsigned dppu(unsigned x) { return (unsigned)__builtin_amdgcn_mov_dpp((int)x, CTRL, 0xf, 0xf, true); }
constexpr int XOR1 = 0xB1, XOR2 = 0x4E, ROR8 = 0x128, ROR4 = 0x124, HMIRROR = 0x141;
__device__ __forceinline__ float sw32_sum(float a, float b) { auto r = __builtin_amdgcn_permlane32_swap(f2u(a), f2u(b), false, false); return u2f(r[0]) + u2f(r[1]); }
__device__ __forceinline__ float sw16_sum(float a, float b) { auto r = __builtin_amdgcn_permlane16_swap(f2u(a), f2u(b), false, false); return u2f(r[0]) + u2f(r[1]); }
__device__ __forceinline__ float sw32_max(float a, float b) { auto r = __builtin_amdgcn_permlane32_swap(f2u(a), f2u(b), false, false); return fmaxf(u2f(r[0]), u2f(r[1])); }
__device__ __forceinline__ float sw16_max(float a, float b) { auto r = __builtin_amdgcn_permlane16_swap(f2u(a), f2u(b), false, false); return fmaxf(u2f(r[0]), u2f(r[1])); }
__device__ __forceinline__ float wave_sum(float x) {
  x += dpp<XOR1>(x); x += dpp<XOR2>(x); x += dpp<ROR4>(x); x += dpp<ROR8>(x);
  x = sw16_sum(x, x); return sw32_sum(x, x);
}
__device__ __forceinline__ f32x4 mfma32(v4u a, v4u b, f32x4 c) { return __builtin_amdgcn_mfma_f32_16x16x32_bf16(__builtin_bit_cast(bf16x8_t, a), __builtin_bit_cast(bf16x8_t, b), c, 0, 0, 0); }
__device__ __forceinline__ f32x4 mfma16(v2u a, v2u b, f32x4 c) { return __builtin_amdgcn_mfma_f32_16x16x16bf16_1k(__builtin_bit_cast(s16x4, a), __builtin_bit_cast(s16x4, b), c, 0, 0, 0); }
__device__ __forceinline__ void barrier() { asm volatile("s_waitcnt lgkmcnt(0)\n\ts_barrier" ::: "memory"); }
__device__ __forceinline__ v4u tr_frag(LAS unsigned char* lo, LAS unsigned char* hi) {
  const v2u a = __builtin_bit_cast(v2u, __builtin_amdgcn_ds_read_tr16_b64_v4i16((LAS s16x4*)lo));
  const v2u b = __builtin_bit_cast(v2u, __builtin_amdgcn_ds_read_tr16_b64_v4i16((LAS s16x4*)hi));
  return v4u{a[0], a[1], b[0], b[1]};
}

__device__ __forceinline__ v4u ln_row(v4u xv) {
  float x[8];
#pragma unroll
  for (int i = 0; i < 4; ++i) { x[2 * i] = bflo(xv[i]); x[2 * i + 1] = bfhi(xv[i]); }
  const float mean = wave_sum(((x[0] + x[1]) + (x[2] + x[3])) + ((x[4] + x[5]) + (x[6] + x[7]))) * (1.0f / D);
  float vs = 0.f;
#pragma unroll
  for (int i = 0; i < 8; ++i) { const float d = x[i] - mean; vs = fmaf(d, d, vs); }
  const float var = wave_sum(vs) * (1.0f / D);
  const float mb = rbf(mean);
  const float rs = __builtin_amdgcn_rcpf(rbf(__builtin_amdgcn_sqrtf(rbf(rbf(var) + 1e-5f))));
  v4u r;
#pragma unroll
  for (int i = 0; i < 4; ++i) { const unsigned d = cvtpk(x[2 * i] - mb, x[2 * i + 1] - mb); r[i] = cvtpk(bflo(d) * rs, bfhi(d) * rs); }
  return r;
}
__device__ __forceinline__ void glds16(const u16* gsrc, unsigned lds_dst) {
  unsigned keep;
  asm volatile("s_mov_b32 %0, m0\n\ts_mov_b32 m0, %2\n\ts_nop 0\n\tglobal_load_lds_dwordx4 %1, off nt\n\ts_mov_b32 m0, %0" : "=&s"(keep) : "v"(gsrc), "s"(lds_dst) : "memory");
}

#define STAMP(i) do { } while (0)
#define DUMP_STAMPS() do { } while (0)
#define LD128(off) __builtin_amdgcn_raw_buffer_load_b128(rw, (off), 0, AUX_SC1)
#define ST128(v, off) __builtin_amdgcn_raw_buffer_store_b128((v), rw, (off), 0, AUX_SC1)
#define SPIN_LOOP _Pragma("unroll 1") for (unsigned spins = 0; spins < SPIN_CAP; ++spins)
#define NOHOIST() asm volatile("" ::: "memory")

__global__ void __launch_bounds__(NTHR, 2) fused_block(const u16* x_, const u16* kc_, const u16* vc_, const u16* wq_,
                                                       const u16* wo_, const u16* wgate_, const u16* wup_, const u16* wdown_,
                                                       u16* out_, unsigned char* ws_ TUNE_PARAM) {
  __shared__ __attribute__((aligned(16))) unsigned char smem[L_BYTES];
  LAS unsigned char* L = (LAS unsigned char*)smem;
  STAMP(0);
  unsigned long long ts64;
  asm volatile("s_memrealtime %0\n\ts_waitcnt lgkmcnt(0)" : "=s"(ts64) :: "memory");
  __builtin_amdgcn_sched_barrier(0);
  const unsigned tstart = (unsigned)ts64;
  typedef const __attribute__((address_space(4))) unsigned long long* kptr;
  kptr ka = (kptr)__builtin_amdgcn_kernarg_segment_ptr();
  asm volatile("" : "+s"(ka));
  const u16* x = (const u16*)ka[0]; const u16* kc = (const u16*)ka[1]; const u16* vc = (const u16*)ka[2]; const u16* wq = (const u16*)ka[3];
  const u16* wo = (const u16*)ka[4]; const u16* wgate = (const u16*)ka[5]; const u16* wup = (const u16*)ka[6]; const u16* wdown = (const u16*)ka[7];
  u16* out = (u16*)ka[8]; unsigned char* ws = (unsigned char*)ka[9];
  const int wg = blockIdx.x, xcd = wg & 7, idx = wg >> 3;
  const int kvh = xcd, b = idx & 1, sp = idx >> 1;
  const int tid = threadIdx.x, lane = tid & 63, w = __builtin_amdgcn_readfirstlane(tid >> 6);
  const int c = lane & 15, g = lane >> 4;
  const int qc = xcd * 8 + (idx & 7), kq = idx >> 3;
  const __amdgpu_buffer_rsrc_t rw = __builtin_amdgcn_make_buffer_rsrc((void*)ws, 0, WS_BYTES, 0x00020000);
  const unsigned lds0 = (unsigned)(uintptr_t)L;
#define WAIT_UNTIL(t, slp) do { while ((int)((unsigned)__builtin_amdgcn_s_memrealtime() - (tstart + (unsigned)(t))) < 0) __builtin_amdgcn_s_sleep(slp); } while (0)

  if (w == 4) {
    __builtin_amdgcn_s_setprio(3);
    const v4u x0 = *(const GAS v4u*)(x + lane * 8), x1 = *(const GAS v4u*)(x + D + lane * 8);
    const unsigned tag = __builtin_amdgcn_raw_buffer_load_b32(rw, OFF_EPOCH, 0, AUX_SC1) + 1u;
    {
      const v4u n0 = ln_row(x0), n1 = ln_row(x1);
      *(LAS v4u*)(L + L_XN + lane * 16) = n0; *(LAS v4u*)(L + L_XN + 1024 + lane * 16) = n1;
      STAMP(9);
      barrier();
      barrier();
      f32x2 s = {0.f, 0.f};
#pragma unroll
      for (int ww = 0; ww < 4; ++ww) s += *(LAS f32x2*)(L + L_RED + ww * 512 + lane * 8);
      ST128((v4u{f2u(s[0]), tag, f2u(s[1]), tag}), OFF_Q + ((kq * 2 + (lane >> 5)) * 4096 + qc * 64 + (lane & 31) * 2) * 8);
    }
    STAMP(1);
    if (lane == 0) __builtin_amdgcn_raw_buffer_store_b32(tstart, rw, 0x800 + wg * 4, 0, AUX_SC1);
    if (lane == 0) *(LAS unsigned*)(L + L_TAG) = tag;
    {
      const int qo = OFF_Q + (b * 4096 + kvh * 512) * 8 + lane * 16;
      v4u qv[16];
      unsigned okm = 0;
      WAIT_UNTIL(T_P, 2);
      SPIN_LOOP {
        NOHOIST();
#pragma unroll
        for (int j = 0; j < 16; ++j) if (!((okm >> j) & 1)) qv[j] = LD128(qo + (j >> 2) * 65536 + (j & 3) * 1024);
        okm = 0;
#pragma unroll
        for (int j = 0; j < 16; ++j) okm |= __all(tagged(qv[j], tag)) ? (1u << j) : 0u;
        if (okm == 0xffffu) break;
      }
#pragma unroll
      for (int i = 0; i < 4; ++i) {
        const float a0 = (u2f(qv[i][0]) + u2f(qv[4 + i][0])) + (u2f(qv[8 + i][0]) + u2f(qv[12 + i][0]));
        const float a1 = (u2f(qv[i][2]) + u2f(qv[4 + i][2])) + (u2f(qv[8 + i][2]) + u2f(qv[12 + i][2]));
        *(LAS unsigned*)(L + L_QF + i * 256 + lane * 4) = cvtpk(a0, a1);
      }
    }
    STAMP(2);
    barrier();
    return;
  }

  const int nc = 4 - b, nbw = 3 + 2 * b;
  const int role = sp < nc ? 1 : sp < nc + nbw ? 2 : (sp >= 8 && sp < 12) ? 3 : 0;
  const int ci = xcd * 7 + 4 * b + sp, bi = xcd * 8 + 3 * b + sp - nc, rj = xcd * 8 + idx - 16;
  const int nn = 16 - nc - nbw, sn = sp - nc - nbw;
  const int strest = 512 - nc * ST_C - nbw * ST_B, stn = b ? strest >> 3 : strest / 9, stx = strest - nn * stn;
  const int nstep = role == 1 ? ST_C : role == 2 ? ST_B : stn + (sn < stx ? 1 : 0);
  const int step0 = role == 1 ? ST_C * sp : role == 2 ? nc * ST_C + ST_B * (sp - nc) : nc * ST_C + nbw * ST_B + stn * sn + (sn < stx ? sn : stx);
  const size_t pbase = (size_t)(b * HKV + kvh) * SK * DH;
  const __amdgpu_buffer_rsrc_t rk = __builtin_amdgcn_make_buffer_rsrc((void*)(kc + pbase), 0, SK * DH * 2, 0x00020000);
  const __amdgpu_buffer_rsrc_t rv = __builtin_amdgcn_make_buffer_rsrc((void*)(vc + pbase), 0, SK * DH * 2, 0x00020000);
  const int koff = c * 256 + g * 16, voff = g * 1024 + c * 16;
  const unsigned sbase = (unsigned)(step0 * 64 + w * 16) * 256u;
  v4u kb[5][4], vb[5][4];
#define ISSUEK(d, so) do { _Pragma("unroll") for (int s = 0; s < 4; ++s) kb[d][s] = __builtin_amdgcn_raw_buffer_load_b128(rk, koff + s * 64, (so), KAUX); __builtin_amdgcn_sched_barrier(0); } while (0)
#define ISSUEV(d, so) do { _Pragma("unroll") for (int s = 0; s < 4; ++s) vb[d][s] = __builtin_amdgcn_raw_buffer_load_b128(rv, voff + s * 256, (so), VAUX); __builtin_amdgcn_sched_barrier(0); } while (0)
#define VMWAIT(n) do { asm volatile("s_waitcnt vmcnt(%0)" :: "i"(n) : "memory"); __builtin_amdgcn_sched_barrier(0); } while (0)
  const int q4 = (lane >> 2) & 3, p4 = lane & 3;
  {
    const int r8 = lane >> 3;
    const u16* p = wq + (size_t)(128 * kq + 32 * w + r8) * (HQ * DH) + qc * 64;
#pragma unroll
    for (int t = 0; t < 4; ++t) glds16(p + (size_t)t * 8 * (HQ * DH) + (((lane & 7) ^ ((r8 & 2) | ((t & 1) << 2))) * 8), lds0 + L_WQ + w * 4096 + t * 1024);
    ISSUEK(0, sbase); ISSUEV(0, sbase);
    STAMP(7);
    v4u fr[4];
    const int sw = (q4 & 2) | ((g & 1) << 2);
    VMWAIT(8);
#pragma unroll
    for (int t = 0; t < 4; ++t) {
      LAS unsigned char* a = L + L_WQ + w * 4096 + g * 1024 + q4 * 128 + (p4 & 1) * 8 + (((2 * t + (p4 >> 1)) ^ sw) * 16);
      fr[t] = tr_frag(a, a + 512);
    }
    STAMP(8);
    barrier();
    const v4u xb = *(LAS v4u*)(L + L_XN + (c & 1) * 1024 + (128 * kq + 32 * w + 8 * g) * 2);
#pragma unroll
    for (int t = 0; t < 4; ++t) {
      const f32x4 qa = mfma32(fr[t], xb, f32x4{0.f, 0.f, 0.f, 0.f});
      if (c < 2) *(LAS f32x4*)(L + L_RED + w * 512 + c * 256 + t * 64 + g * 16) = qa;
    }
    STAMP(1);
    barrier();
  }
  const bool odd = nstep & 1;
#pragma unroll
  for (int d = 1; d < 4; ++d) { VMWAIT(PACE_N); ISSUEK(d, sbase + d * 16384u); VMWAIT(PACE_N); ISSUEV(d, sbase + d * 16384u); }
  if (odd) { VMWAIT(PACE_N); ISSUEK(4, sbase + 4 * 16384u); VMWAIT(PACE_N); ISSUEV(4, sbase + 4 * 16384u); }
  STAMP(10);
  barrier();
  STAMP(2);
  v4u qf[4];
#pragma unroll
  for (int s = 0; s < 4; ++s) qf[s] = *(LAS v4u*)(L + L_QF + (c >> 2) * 256 + s * 64 + g * 16);
  const unsigned tag = __builtin_amdgcn_readfirstlane(*(LAS unsigned*)(L + L_TAG));

  const unsigned m0 = (c & 3) == 0 ? 0xffffu : (c & 3) == 1 ? 0xffff0000u : 0u;
  const unsigned m1 = (c & 3) == 2 ? 0xffffu : (c & 3) == 3 ? 0xffff0000u : 0u;
  constexpr float SCALE = 0.088388347648318440f, LOG2E = 1.4426950408889634f;
  f32x4 acc0 = {0.f, 0.f, 0.f, 0.f}, acc1 = {0.f, 0.f, 0.f, 0.f};
  float m = -INFINITY, l = 0.f;
  f32x4 sc;
#define QK(d) do { sc = f32x4{0.f, 0.f, 0.f, 0.f}; _Pragma("unroll") for (int s = 0; s < 4; ++s) sc = mfma32(kb[d][s], qf[s], sc); \
    asm volatile("" : "+v"(sc) :: "memory"); __builtin_amdgcn_sched_barrier(0); } while (0)
#define PV(d) do { \
    const unsigned r01 = cvtpk(sc[0], sc[1]), r23 = cvtpk(sc[2], sc[3]); \
    const unsigned t01 = cvtpk(bflo(r01) * SCALE, bfhi(r01) * SCALE), t23 = cvtpk(bflo(r23) * SCALE, bfhi(r23) * SCALE); \
    const float s0 = bflo(t01), s1 = bfhi(t01), s2 = bflo(t23), s3 = bfhi(t23); \
    float mx = fmaxf(fmaxf(s0, s1), fmaxf(s2, s3)); mx = sw16_max(mx, mx); mx = sw32_max(mx, mx); \
    if (__any(mx > m)) { const float mn = fmaxf(m, mx); const float al = __builtin_amdgcn_exp2f((m - mn) * LOG2E); m = mn; l *= al; acc0 *= al; acc1 *= al; } \
    const float sv[4] = {s0, s1, s2, s3}; \
    _Pragma("unroll") for (int i = 0; i < 4; ++i) { \
      const float p_ = __builtin_amdgcn_exp2f((sv[i] - m) * LOG2E); const unsigned pp = cvtpk(p_, p_); l += bflo(pp); \
      const v2u bb = {pp & m0, pp & m1}; const v4u vv = vb[d][i]; \
      acc0 = mfma16(v2u{vv[0], vv[1]}, bb, acc0); acc1 = mfma16(v2u{vv[2], vv[3]}, bb, acc1); } \
    asm volatile("" : "+v"(acc0), "+v"(acc1) :: "memory"); __builtin_amdgcn_sched_barrier(0); } while (0)
  const int ffo = (128 * w + (lane >> 2)) * F + ci * 32 + (((lane & 3) ^ ((lane >> 4) & 2)) * 8);
  const u16* wsrc = role == 1 ? wgate + ffo : wo + (size_t)(bi * 64 + g) * D + 128 * w + 8 * c;
  int wstr = role == 1 ? 16 * F : 4 * D;
  int wp = 0;
  const int wn = (ABL & 2) ? 0 : role == 1 ? 24 : role == 2 ? 16 : 0;
#define WPIECE() do { if (wp < wn) { glds16(wsrc, lds0 + L_W + w * WREG + wp * 1024); ++wp; wsrc += wstr; \
      if (role == 1 && wp == 8) wsrc = wup + ffo; \
      if (role == 1 && wp == 16) { wsrc = wdown + (size_t)(ci * 32 + g) * D + 128 * w + 8 * c; wstr = 4 * D; } } } while (0)
  unsigned so = sbase + (odd ? 5u : 4u) * 16384u;
  const unsigned send = sbase + (unsigned)nstep * 16384u;
  QK(0); ISSUEK(0, so); PV(0); ISSUEV(0, so); WPIECE();
  QK(1); ISSUEK(1, so + 16384u); PV(1); ISSUEV(1, so + 16384u); WPIECE();
  so += 32768u;
  QK(2); PV(2); QK(3); PV(3);
  if (odd) { QK(4); PV(4); }
#pragma unroll 1
  for (; so < send; so += 32768u) {
    QK(0); ISSUEK(0, so); WPIECE(); PV(0); ISSUEV(0, so); WPIECE();
    QK(1); ISSUEK(1, so + 16384u); PV(1); ISSUEV(1, so + 16384u); WPIECE();
  }
  QK(0); PV(0); QK(1); PV(1);
  STAMP(3);

  l = sw16_sum(l, l); l = sw32_sum(l, l);
  {
    LAS float* rec = (LAS float*)(L + L_MRG) + (w * 4 + (c >> 2)) * 132;
#pragma unroll
    for (int r = 0; r < 4; ++r) { rec[4 + 8 * (4 * g + r) + (c & 3)] = acc0[r]; rec[4 + 8 * (4 * g + r) + 4 + (c & 3)] = acc1[r]; }
    if (g == 0 && (c & 3) == 0) *(LAS f32x2*)rec = f32x2{m, l};
  }
  barrier();
  if (ABL & 4) WAIT_UNTIL(4600, 16);
  if (w < 2) {
    const int hd = 2 * w + (lane >> 5);
    f32x2 ml[4]; f32x4 ov[4];
#pragma unroll
    for (int ww = 0; ww < 4; ++ww) { LAS float* rec = (LAS float*)(L + L_MRG) + (ww * 4 + hd) * 132; ml[ww] = *(LAS f32x2*)rec; ov[ww] = *(LAS f32x4*)(rec + 4 + 4 * (lane & 31)); }
    const float M = fmaxf(fmaxf(ml[0][0], ml[1][0]), fmaxf(ml[2][0], ml[3][0]));
    float Ls = 0.f; f32x4 o = {0.f, 0.f, 0.f, 0.f};
#pragma unroll
    for (int ww = 0; ww < 4; ++ww) { const float wt = __builtin_amdgcn_exp2f((ml[ww][0] - M) * LOG2E); Ls = fmaf(wt, ml[ww][1], Ls); o += wt * ov[ww]; }
    o *= __builtin_amdgcn_rcpf(Ls);
    ST128((v4u{cvtpk(o[0], o[1]), tag, cvtpk(o[2], o[3]), tag}), OFF_PART + ((b * HQ + kvh * 4 + hd) * NSPLIT + sp) * 512 + (lane & 31) * 16);
    if ((lane & 31) == 0)
      __builtin_amdgcn_raw_buffer_store_b64(v2u{f2u(fmaf(M, LOG2E, __builtin_amdgcn_logf(Ls))), tag}, rw, OFF_LSE + (((b * HKV + kvh) * NSPLIT + sp) * 4 + hd) * 8, 0, AUX_SC1);
  }
  STAMP(4);
  if (ABL & 8) WAIT_UNTIL(4600, 16);
  if ((ABL & 16) && role != 2) WAIT_UNTIL(4600, 16);
  const unsigned dm0 = (c & 7) == 0 ? 0xffffu : (c & 7) == 1 ? 0xffff0000u : 0u, dm1 = (c & 7) == 2 ? 0xffffu : (c & 7) == 3 ? 0xffff0000u : 0u,
                 dm2 = (c & 7) == 4 ? 0xffffu : (c & 7) == 5 ? 0xffff0000u : 0u, dm3 = (c & 7) == 6 ? 0xffffu : (c & 7) == 7 ? 0xffff0000u : 0u;
#define DIAG(p) v4u{(p) & dm0, (p) & dm1, (p) & dm2, (p) & dm3}
#define PUBLISH_ACC(acc, base) do { ST128((v4u{f2u(acc[0]), tag, f2u(acc[1]), tag}), (base) + tid * 16); ST128((v4u{f2u(acc[2]), tag, f2u(acc[3]), tag}), (base) + 4096 + tid * 16); } while (0)

  if (role == 2) {
    v4u wr[16];
#pragma unroll
    for (int i = 0; i < 16; ++i) wr[i] = *(LAS v4u*)(L + L_W + w * WREG + i * 1024 + lane * 16);
    const int head = bi >> 1, s = lane >> 2;
    const int po = OFF_PART + (head * NSPLIT + s) * 512 + (bi & 1) * 256 + w * 64 + (lane & 3) * 16, lo = OFF_LSE + (((head >> 2) * NSPLIT + s) * 4 + (head & 3)) * 8;
    v4u a0, a1; v2u e0, e1;
    bool ok0 = false, ok1 = false;
    SPIN_LOOP {
      NOHOIST();
      if (!ok0) { a0 = LD128(po); e0 = __builtin_amdgcn_raw_buffer_load_b64(rw, lo, 0, AUX_SC1); }
      if (!ok1) { a1 = LD128(po + HQ * NSPLIT * 512); e1 = __builtin_amdgcn_raw_buffer_load_b64(rw, lo + HKV * NSPLIT * 32, 0, AUX_SC1); }
      ok0 = tagged(a0, tag) & (e0[1] == tag); ok1 = tagged(a1, tag) & (e1[1] == tag);
      if (__all(ok0 & ok1)) break;
    }
    STAMP(5);
    const float l0 = u2f(e0[0]), l1 = u2f(e1[0]);
    float x0m = fmaxf(l0, dpp<ROR4>(l0)), x1m = fmaxf(l1, dpp<ROR4>(l1));
    x0m = fmaxf(x0m, dpp<ROR8>(x0m)); x1m = fmaxf(x1m, dpp<ROR8>(x1m));
    x0m = sw16_max(x0m, x0m); x1m = sw16_max(x1m, x1m); x0m = sw32_max(x0m, x0m); x1m = sw32_max(x1m, x1m);
    const float w0 = __builtin_amdgcn_exp2f(l0 - x0m), w1 = __builtin_amdgcn_exp2f(l1 - x1m);
    const float v0 = sw32_sum(w0 * bflo(a0[0]), w1 * bflo(a1[0])), v1 = sw32_sum(w0 * bfhi(a0[0]), w1 * bfhi(a1[0])),
                v2 = sw32_sum(w0 * bflo(a0[2]), w1 * bflo(a1[2])), v3 = sw32_sum(w0 * bfhi(a0[2]), w1 * bfhi(a1[2]));
    float ws_ = sw32_sum(w0, w1);
    float u0 = sw16_sum(v0, v1), u1 = sw16_sum(v2, v3);
    ws_ = sw16_sum(ws_, ws_);
    u0 += dpp<ROR8>(u0); u1 += dpp<ROR8>(u1); ws_ += dpp<ROR8>(ws_);
    u0 += dpp<ROR4>(u0); u1 += dpp<ROR4>(u1); ws_ += dpp<ROR4>(ws_);
    const float uo = ((lane & 4) ? u1 : u0) * __builtin_amdgcn_rcpf(ws_);
    if ((lane & 8) == 0) *(LAS unsigned*)(L + L_OB + ((g >> 1) * 4 + (g & 1) + ((lane & 4) >> 1)) * 64 + (4 * w + (lane & 3)) * 4) = cvtpk(uo, uo);
    barrier();
    f32x4 acc = {0.f, 0.f, 0.f, 0.f};
#pragma unroll
    for (int q = 0; q < 4; ++q) {
      const v4u pv = *(LAS v4u*)(L + L_OB + ((c >> 3) * 4 + g) * 64 + q * 16);
#pragma unroll
      for (int i = 0; i < 4; ++i) acc = mfma32(wr[q * 4 + i], DIAG(pv[i]), acc);
    }
    PUBLISH_ACC(acc, OFF_P1 + bi * 8192);
    STAMP(6);
  } else if (role == 3) {
    const int rb = rj & 1, col0 = ((rj >> 1) & 15) * 32 + (rj >> 5) * 16;
    const unsigned xp = *(const GAS unsigned*)(x + rb * D + col0 + 2 * (lane & 7));
    const int pp = lane >> 3;
    float h0, h1;
    {
      const int po = OFF_P1 + (16 * w + pp) * 8192 + rj * 128 + (lane & 7) * 16;
      v4u a0, a1;
      bool ok0 = false, ok1 = false;
      SPIN_LOOP {
        NOHOIST();
        if (!ok0) a0 = LD128(po);
        if (!ok1) a1 = LD128(po + 8 * 8192);
        ok0 = tagged(a0, tag); ok1 = tagged(a1, tag);
        if (__all(ok0 & ok1)) break;
      }
      STAMP(5);
      float u = sw32_sum(u2f(a0[0]) + u2f(a1[0]), u2f(a0[2]) + u2f(a1[2]));
      u = sw16_sum(u, u); u += dpp<ROR8>(u);
      if ((lane & 24) == 0) *(LAS float*)(L + L_RR + w * 64 + ((lane >> 5) * 8 + (lane & 7)) * 4) = u;
      barrier();
      f32x2 s = {0.f, 0.f};
#pragma unroll
      for (int ww = 0; ww < 4; ++ww) s += *(LAS f32x2*)(L + L_RR + ww * 64 + (lane & 7) * 8);
      const unsigned at = cvtpk(s[0], s[1]);
      const unsigned hp = cvtpk(bflo(xp) + bflo(at), bfhi(xp) + bfhi(at));
      h0 = bflo(hp); h1 = bfhi(hp);
      if (w == 0) __builtin_amdgcn_raw_buffer_store_b64(v2u{hp, tag}, rw, OFF_HB + pp * 4096 + rb * 2048 + (col0 / 2 + (lane & 7)) * 8, 0, AUX_SC1);
      STAMP(6);
    }
    {
      const int po = OFF_P2 + (14 * w + pp) * 8192 + rj * 128 + (lane & 7) * 16;
      v4u a0, a1 = {0u, tag, 0u, tag};
      bool ok0 = false, ok1 = pp >= 6;
      SPIN_LOOP {
        NOHOIST();
        if (!ok0) a0 = LD128(po);
        if (!ok1) a1 = LD128(po + 8 * 8192);
        ok0 = tagged(a0, tag); ok1 = tagged(a1, tag);
        if (__all(ok0 & ok1)) break;
      }
      STAMP(7);
      float u = sw32_sum(u2f(a0[0]) + u2f(a1[0]), u2f(a0[2]) + u2f(a1[2]));
      u = sw16_sum(u, u); u += dpp<ROR8>(u);
      if ((lane & 24) == 0) *(LAS float*)(L + L_RR + 256 + w * 64 + ((lane >> 5) * 8 + (lane & 7)) * 4) = u;
      barrier();
      if (tid < 8) {
        f32x2 s = {0.f, 0.f};
#pragma unroll
        for (int ww = 0; ww < 4; ++ww) s += *(LAS f32x2*)(L + L_RR + 256 + ww * 64 + lane * 8);
        const unsigned dn = cvtpk(s[0], s[1]);
        *(GAS unsigned*)(out + rb * D + col0 + 2 * lane) = cvtpk(h0 + bflo(dn), h1 + bfhi(dn));
        if (rj == 0 && tid == 0) __builtin_amdgcn_raw_buffer_store_b32(tag, rw, OFF_EPOCH, 0, AUX_SC1);
      }
      STAMP(8);
    }
  } else if (role == 1) {
    v4u fg[8], fu[8], wd[8];
#pragma unroll
    for (int ks = 0; ks < 4; ++ks)
#pragma unroll
      for (int t = 0; t < 2; ++t) {
        LAS unsigned char* a = L + L_W + w * WREG + (32 * ks + 8 * g + q4) * 64 + (((2 * t + (p4 >> 1)) ^ ((g & 1) << 1)) * 16) + (p4 & 1) * 8;
        fg[ks * 2 + t] = tr_frag(a, a + 256); fu[ks * 2 + t] = tr_frag(a + 8192, a + 8192 + 256);
      }
#pragma unroll
    for (int i = 0; i < 8; ++i) wd[i] = *(LAS v4u*)(L + L_W + w * WREG + 16384 + i * 1024 + lane * 16);
    if (w < 2) {
      const int ho = OFF_HB + (ci & 7) * 4096 + w * 2048 + lane * 32;
      v4u ha, hb;
      bool ok = false;
      SPIN_LOOP {
        NOHOIST();
        if (!ok) { ha = LD128(ho); hb = LD128(ho + 16); }
        ok = tagged(ha, tag) & tagged(hb, tag);
        if (__all(ok)) break;
      }
      STAMP(5);
      *(LAS v4u*)(L + L_XN + w * 1024 + lane * 16) = ln_row(v4u{ha[0], ha[2], hb[0], hb[2]});
    }
    barrier();
    f32x4 ag[2], au[2];
#pragma unroll
    for (int t = 0; t < 2; ++t) { ag[t] = f32x4{0.f, 0.f, 0.f, 0.f}; au[t] = f32x4{0.f, 0.f, 0.f, 0.f}; }
#pragma unroll
    for (int ks = 0; ks < 4; ++ks) {
      const v4u xb = *(LAS v4u*)(L + L_XN + (c & 1) * 1024 + (128 * w + 32 * ks + 8 * g) * 2);
#pragma unroll
      for (int t = 0; t < 2; ++t) { ag[t] = mfma32(fg[ks * 2 + t], xb, ag[t]); au[t] = mfma32(fu[ks * 2 + t], xb, au[t]); }
    }
    {
      const f32x4 sel = (c & 4) ? ((c & 2) ? au[1] : au[0]) : ((c & 2) ? ag[1] : ag[0]);
      if (c < 8) {
#pragma unroll
        for (int i = 0; i < 4; ++i) *(LAS float*)(L + L_RED + (c >> 2) * 1024 + ((c & 1) * 32 + (c & 2) * 8 + 4 * g + i) * 16 + w * 4) = sel[i];
      }
    }
    barrier();
    const int fo = ((c >> 3) * 32 + 4 * (c & 7) + g) * 16;
    const f32x4 pg = *(LAS f32x4*)(L + L_RED + fo), pu = *(LAS f32x4*)(L + L_RED + 1024 + fo);
    const unsigned gu = cvtpk((pg[0] + pg[1]) + (pg[2] + pg[3]), (pu[0] + pu[1]) + (pu[2] + pu[3]));
    const float gg = bflo(gu);
    const float sg = rbf(gg * __builtin_amdgcn_rcpf(1.0f + __builtin_amdgcn_exp2f(-gg * LOG2E)));
    const float av = sg * bfhi(gu);
    const unsigned ap = cvtpk(av, av);
    const unsigned am = dppu<HMIRROR>(ap);
    const unsigned alo = (c & 4) ? am : ap, ahi = (c & 4) ? ap : am;
    f32x4 acc = {0.f, 0.f, 0.f, 0.f};
#define BC(v, t) ((c & 4) ? dppu<(3 - (t)) * 0x55>(v) : dppu<(t) * 0x55>(v))
#define BCH(v, t) ((c & 4) ? dppu<(t) * 0x55>(v) : dppu<(3 - (t)) * 0x55>(v))
    { const unsigned p = BC(alo, 0); acc = mfma32(wd[0], DIAG(p), acc); }
    { const unsigned p = BC(alo, 1); acc = mfma32(wd[1], DIAG(p), acc); }
    { const unsigned p = BC(alo, 2); acc = mfma32(wd[2], DIAG(p), acc); }
    { const unsigned p = BC(alo, 3); acc = mfma32(wd[3], DIAG(p), acc); }
    { const unsigned p = BCH(ahi, 0); acc = mfma32(wd[4], DIAG(p), acc); }
    { const unsigned p = BCH(ahi, 1); acc = mfma32(wd[5], DIAG(p), acc); }
    { const unsigned p = BCH(ahi, 2); acc = mfma32(wd[6], DIAG(p), acc); }
    { const unsigned p = BCH(ahi, 3); acc = mfma32(wd[7], DIAG(p), acc); }
    PUBLISH_ACC(acc, OFF_P2 + ci * 8192);
    STAMP(6);
  }
  if (role == 3 && rj == 0 && w == 0) {
    const v4u ts = LD128(0x800 + lane * 16);
    int mn = 0x7fffffff, mx = -0x7fffffff;
    for (int i = 0; i < 4; ++i) { const int d = (int)(ts[i] - tstart); mn = d < mn ? d : mn; mx = d > mx ? d : mx; }
    for (int o = 32; o >= 1; o >>= 1) { const int a = __shfl_xor(mn, o), bb = __shfl_xor(mx, o); mn = a < mn ? a : mn; mx = bb > mx ? bb : mx; }
    mn = __builtin_amdgcn_readfirstlane(mn); mx = __builtin_amdgcn_readfirstlane(mx);
    const int now = (int)((unsigned)__builtin_amdgcn_s_memrealtime() - tstart);
    WAIT_UNTIL(7000 + mn, 1);
  }
}

extern "C" void kernel_launch(void* const* d_in, const int* in_sizes, int n_in, void* d_out, int out_size, void* d_ws, size_t ws_size, hipStream_t stream) {
  fused_block<<<NWG, NTHR, 0, stream>>>((const u16*)d_in[0], (const u16*)d_in[1], (const u16*)d_in[2], (const u16*)d_in[3], (const u16*)d_in[4],
                                        (const u16*)d_in[6], (const u16*)d_in[7], (const u16*)d_in[8], (u16*)d_out, (unsigned char*)d_ws TUNE_ARG);
}
```

```cpp
#include <hip/hip_runtime.h>
#include <hip/hip_bf16.h>
#include <cstdint>

typedef unsigned short u16;
typedef unsigned v4u __attribute__((ext_vector_type(4)));
typedef unsigned v2u __attribute__((ext_vector_type(2)));
typedef float f32x4 __attribute__((ext_vector_type(4)));
typedef float f32x2 __attribute__((ext_vector_type(2)));
typedef short s16x4 __attribute__((ext_vector_type(4)));
typedef __bf16 bf16x8_t __attribute__((ext_vector_type(8)));
typedef __bf16 bf16x2_t __attribute__((ext_vector_type(2)));
#define LAS __attribute__((address_space(3)))
#define GAS __attribute__((address_space(1)))

constexpr int D = 512, HQ = 32, HKV = 8, SK = 32768, DH = 128, F = 1792;
constexpr int NWG = 256, NTHR = 320;
constexpr int NSPLIT = 16, KEYS_PER_WG = SK / NSPLIT;
constexpr int NTILE = KEYS_PER_WG / 16 / 4;
#define RING 2
constexpr int NB = 64, NC = 112, NR = 32, NREP = 8;
constexpr int OFF_EPOCH = 0, OFF_FLAGS = 1024, OFF_Q = 4096, OFF_LSE = 0xA000, OFF_HB = 0xB000, OFF_PART = 0x10000,
              OFF_P1 = 0x90000, OFF_P2 = 0xD0000, WS_BYTES = 0x140000, OFF_DBG = 0x200000;
constexpr unsigned SENT = 0xFFFFFFFFu;
constexpr int AUX_NT = 2, AUX_SC1 = 16;
constexpr unsigned SPIN_CAP = 1u << 16;
constexpr int L_TR = 0;
constexpr int L_XN = 32768;
constexpr int L_RED = L_XN + 2048;
constexpr int L_QF = L_RED + 1024;
constexpr int L_MRG = L_QF + 1024;
constexpr int L_OB = L_MRG + 4 * 4 * 132 * 4;
constexpr int L_RR = L_OB + 512;
constexpr int L_BYTES = L_RR + 1024;

__device__ __forceinline__ float bflo(unsigned v) { return __uint_as_float(v << 16); }
__device__ __forceinline__ float bfhi(unsigned v) { return __uint_as_float(v & 0xffff0000u); }
__device__ __forceinline__ unsigned cvtpk(float lo, float hi) { return __builtin_bit_cast(unsigned, __builtin_convertvector(f32x2{lo, hi}, bf16x2_t)); }
__device__ __forceinline__ float rbf(float x) { return bflo(cvtpk(x, x)); }
__device__ __forceinline__ unsigned nosent(unsigned x) { return x < 0xFFFFFFFEu ? x : 0xFFFFFFFEu; }
__device__ __forceinline__ v4u nosent4(f32x4 v) { return v4u{nosent(__float_as_uint(v[0])), nosent(__float_as_uint(v[1])), nosent(__float_as_uint(v[2])), nosent(__float_as_uint(v[3]))}; }
__device__ __forceinline__ bool ok4(v4u v) { return (v[0] != SENT) & (v[1] != SENT) & (v[2] != SENT) & (v[3] != SENT); }

template <int CTRL> __device__ __forceinline__ float dpp(float x) { return __builtin_bit_cast(float, __builtin_amdgcn_mov_dpp(__builtin_bit_cast(int, x), CTRL, 0xf, 0xf, true)); }
template <int CTRL> __device__ __forceinline__ unsigned dppu(unsigned x) { return (unsigned)__builtin_amdgcn_mov_dpp((int)x, CTRL, 0xf, 0xf, true); }
constexpr int XOR1 = 0xB1, XOR2 = 0x4E, ROR8 = 0x128, ROR4 = 0x124;
__device__ __forceinline__ float sw32_sum(float a, float b) { auto r = __builtin_amdgcn_permlane32_swap(__float_as_uint(a), __float_as_uint(b), false, false); return __uint_as_float(r[0]) + __uint_as_float(r[1]); }
__device__ __forceinline__ float sw16_sum(float a, float b) { auto r = __builtin_amdgcn_permlane16_swap(__float_as_uint(a), __float_as_uint(b), false, false); return __uint_as_float(r[0]) + __uint_as_float(r[1]); }
__device__ __forceinline__ float sw32_max(float a, float b) { auto r = __builtin_amdgcn_permlane32_swap(__float_as_uint(a), __float_as_uint(b), false, false); return fmaxf(__uint_as_float(r[0]), __uint_as_float(r[1])); }
__device__ __forceinline__ float sw16_max(float a, float b) { auto r = __builtin_amdgcn_permlane16_swap(__float_as_uint(a), __float_as_uint(b), false, false); return fmaxf(__uint_as_float(r[0]), __uint_as_float(r[1])); }
__device__ __forceinline__ float wave_sum(float x) {
  x += dpp<XOR1>(x); x += dpp<XOR2>(x); x += dpp<ROR4>(x); x += dpp<ROR8>(x);
  x = sw16_sum(x, x); return sw32_sum(x, x);
}
__device__ __forceinline__ f32x4 mfma32(v4u a, v4u b, f32x4 c) { return __builtin_amdgcn_mfma_f32_16x16x32_bf16(__builtin_bit_cast(bf16x8_t, a), __builtin_bit_cast(bf16x8_t, b), c, 0, 0, 0); }
__device__ __forceinline__ f32x4 mfma16(v2u a, v2u b, f32x4 c) { return __builtin_amdgcn_mfma_f32_16x16x16bf16_1k(__builtin_bit_cast(s16x4, a), __builtin_bit_cast(s16x4, b), c, 0, 0, 0); }
__device__ __forceinline__ void barrier() { asm volatile("s_waitcnt lgkmcnt(0)\n\ts_barrier" ::: "memory"); }

__device__ __forceinline__ v4u ln_row(v4u xv) {
  float x[8];
#pragma unroll
  for (int i = 0; i < 4; ++i) { x[2 * i] = bflo(xv[i]); x[2 * i + 1] = bfhi(xv[i]); }
  const float mean = wave_sum(((x[0] + x[1]) + (x[2] + x[3])) + ((x[4] + x[5]) + (x[6] + x[7]))) * (1.0f / D);
  float vs = 0.f;
#pragma unroll
  for (int i = 0; i < 8; ++i) { const float d = x[i] - mean; vs = fmaf(d, d, vs); }
  const float var = wave_sum(vs) * (1.0f / D);
  const float mb = rbf(mean);
  const float rs = __builtin_amdgcn_rcpf(rbf(__builtin_amdgcn_sqrtf(rbf(rbf(var) + 1e-5f))));
  v4u r;
#pragma unroll
  for (int i = 0; i < 4; ++i) { const unsigned d = cvtpk(x[2 * i] - mb, x[2 * i + 1] - mb); r[i] = cvtpk(bflo(d) * rs, bfhi(d) * rs); }
  return r;
}
__device__ __forceinline__ void transpose_tile(LAS unsigned char* scr, int lane, const v4u* raw, v4u* frag) {
#pragma unroll
  for (int i = 0; i < 4; ++i) *(LAS v4u*)(scr + i * 1024 + lane * 16) = raw[i];
  const int base = (8 * (lane >> 4) + ((lane >> 2) & 3)) * 32 + 8 * (lane & 3);
#pragma unroll
  for (int i = 0; i < 4; ++i) {
    const v2u lo = __builtin_bit_cast(v2u, __builtin_amdgcn_ds_read_tr16_b64_v4i16((LAS s16x4*)(scr + i * 1024 + base)));
    const v2u hi = __builtin_bit_cast(v2u, __builtin_amdgcn_ds_read_tr16_b64_v4i16((LAS s16x4*)(scr + i * 1024 + base + 128)));
    frag[i] = v4u{lo[0], lo[1], hi[0], hi[1]};
  }
}

#define STAMP(i) do { } while (0)

__global__ void __launch_bounds__(NTHR, 2) fused_block(const u16* __restrict__ x, const u16* __restrict__ kc, const u16* __restrict__ vc, const u16* __restrict__ wq,
                                                       const u16* __restrict__ wo, const u16* __restrict__ wgate, const u16* __restrict__ wup, const u16* __restrict__ wdown,
                                                       u16* __restrict__ out, unsigned char* ws) {
  __shared__ __attribute__((aligned(16))) unsigned char smem[L_BYTES];
  LAS unsigned char* L = (LAS unsigned char*)smem;
  const unsigned tstart = (unsigned)__builtin_amdgcn_s_memrealtime();
  const int wg = blockIdx.x, xcd = wg & 7, idx = wg >> 3;
  const int kvh = xcd, b = idx & 1, sp = idx >> 1;
  const int tid = threadIdx.x, lane = tid & 63, w = __builtin_amdgcn_readfirstlane(tid >> 6);
  const int c = lane & 15, g = lane >> 4;
  const int role = idx < 14 ? 1 : idx < 22 ? 2 : idx < 26 ? 3 : 0;
  const int ci = xcd * 14 + idx, bi = xcd * 8 + idx - 14, rj = xcd * 4 + idx - 22;
  const int qs = xcd * 32 + idx;
  const __amdgpu_buffer_rsrc_t rw = __builtin_amdgcn_make_buffer_rsrc((void*)ws, 0, WS_BYTES, 0x00020000);

  if (w == 4) {
    const unsigned tag = __builtin_amdgcn_raw_buffer_load_b32(rw, OFF_EPOCH, 0, AUX_SC1) + 1u;
    const v4u s4 = {SENT, SENT, SENT, SENT};
    {
      const int po = OFF_PART + (((b * HQ + kvh * 4 + (lane >> 5)) * NSPLIT + sp) * DH) * 4 + (lane & 31) * 16;
      __builtin_amdgcn_raw_buffer_store_b128(s4, rw, po, 0, AUX_SC1);
      __builtin_amdgcn_raw_buffer_store_b128(s4, rw, po + 2 * NSPLIT * DH * 4, 0, AUX_SC1);
      if (lane < 4) __builtin_amdgcn_raw_buffer_store_b32(SENT, rw, OFF_LSE + ((b * HQ + kvh * 4 + lane) * NSPLIT + sp) * 4, 0, AUX_SC1);
      if (role == 1 || role == 2) {
        const int ro = (role == 1 ? OFF_P2 + ci * 4096 : OFF_P1 + bi * 4096) + lane * 16;
#pragma unroll
        for (int i = 0; i < 4; ++i) __builtin_amdgcn_raw_buffer_store_b128(s4, rw, ro + i * 1024, 0, AUX_SC1);
      } else if (role == 3) {
        if (lane < 4 * NREP) __builtin_amdgcn_raw_buffer_store_b128(s4, rw, OFF_HB + (lane >> 2) * 2048 + (rj & 1) * 1024 + (rj >> 1) * 64 + (lane & 3) * 16, 0, AUX_SC1);
      }
    }
    asm volatile("s_waitcnt vmcnt(0)" ::: "memory");
    if (lane == 0) __builtin_amdgcn_raw_buffer_store_b32(tag, rw, OFF_FLAGS + wg * 4, 0, AUX_SC1);
    barrier();
    if (lane < 16) {
      f32x2 s = {0.f, 0.f};
#pragma unroll
      for (int ww = 0; ww < 4; ++ww) s += *(LAS f32x2*)(L + L_RED + ww * 128 + lane * 8);
      __builtin_amdgcn_raw_buffer_store_b64(v2u{cvtpk(s[0], s[1]), tag}, rw, OFF_Q + ((lane >> 3) * 2048 + qs * 8 + (lane & 7)) * 8, 0, AUX_SC1);
    }
    STAMP(1);
    {
      const int qo = OFF_Q + (b * 2048 + kvh * 256) * 8 + lane * 16;
      v4u q0, q1;
      for (unsigned spins = 0; spins < SPIN_CAP; ++spins) {
        asm volatile("" ::: "memory");
        q0 = __builtin_amdgcn_raw_buffer_load_b128(rw, qo, 0, AUX_SC1);
        q1 = __builtin_amdgcn_raw_buffer_load_b128(rw, qo + 1024, 0, AUX_SC1);
        if (__all((q0[1] == tag) & (q0[3] == tag) & (q1[1] == tag) & (q1[3] == tag))) break;
      }
      *(LAS v2u*)(L + L_QF + lane * 8) = v2u{q0[0], q0[2]};
      *(LAS v2u*)(L + L_QF + 512 + lane * 8) = v2u{q1[0], q1[2]};
    }
    STAMP(2);
    barrier();
    for (unsigned spins = 0; spins < SPIN_CAP; ++spins) {
      asm volatile("" ::: "memory");
      const v4u f = __builtin_amdgcn_raw_buffer_load_b128(rw, OFF_FLAGS + lane * 16, 0, AUX_SC1);
      if (__all((f[0] == tag) & (f[1] == tag) & (f[2] == tag) & (f[3] == tag))) break;
      __builtin_amdgcn_s_sleep(8);
    }
    if (lane == 0) *(LAS unsigned*)(L + L_RED + 1020) = tag;
    barrier();
    return;
  }

  const v4u x0 = *(const GAS v4u*)(x + lane * 8), x1 = *(const GAS v4u*)(x + D + lane * 8);
  v4u wqr[4];
  {
    const u16* p = wq + (size_t)(128 * w + (lane >> 1)) * (HQ * DH) + qs * 16 + (lane & 1) * 8;
#pragma unroll
    for (int i = 0; i < 4; ++i) wqr[i] = __builtin_nontemporal_load((const GAS v4u*)(p + (size_t)i * 32 * (HQ * DH)));
  }
  v4u wr[16];
  {
    const u16* p[4]; int st[4];
    if (role == 1) {
      const size_t o = (size_t)(128 * w + (lane >> 1)) * F + ci * 16 + (lane & 1) * 8;
      p[0] = wgate + o; p[1] = wup + o; st[0] = st[1] = 32 * F;
      p[2] = wdown + (size_t)(ci * 16 + g) * D + 128 * w + 8 * c; st[2] = 4 * D;
      p[3] = x; st[3] = 0;
    } else if (role == 2) {
#pragma unroll
      for (int q = 0; q < 4; ++q) { p[q] = wo + (size_t)(bi * 64 + 16 * q + g) * D + 128 * w + 8 * c; st[q] = 4 * D; }
    } else {
#pragma unroll
      for (int q = 0; q < 4; ++q) { p[q] = x; st[q] = 0; }
    }
#pragma unroll
    for (int q = 0; q < 4; ++q)
#pragma unroll
      for (int i = 0; i < 4; ++i) wr[q * 4 + i] = __builtin_nontemporal_load((const GAS v4u*)(p[q] + i * st[q]));
  }
  __builtin_amdgcn_sched_barrier(0);
  const size_t pbase = (size_t)(b * HKV + kvh) * SK * DH;
  const __amdgpu_buffer_rsrc_t rk = __builtin_amdgcn_make_buffer_rsrc((void*)(kc + pbase), 0, SK * DH * 2, 0x00020000);
  const __amdgpu_buffer_rsrc_t rv = __builtin_amdgcn_make_buffer_rsrc((void*)(vc + pbase), 0, SK * DH * 2, 0x00020000);
  const int koff = c * 256 + g * 16, voff = g * 1024 + c * 16;
  const unsigned sbase = (unsigned)(sp * KEYS_PER_WG + w * 16) * 256u;
  v4u kb[RING][4], vb[RING][4];
#define ISSUEK(d, so) do { _Pragma("unroll") for (int s = 0; s < 4; ++s) kb[d][s] = __builtin_amdgcn_raw_buffer_load_b128(rk, koff + s * 64, (so), AUX_NT); __builtin_amdgcn_sched_barrier(0); } while (0)
#define ISSUEV(d, so) do { _Pragma("unroll") for (int s = 0; s < 4; ++s) vb[d][s] = __builtin_amdgcn_raw_buffer_load_b128(rv, voff + s * 256, (so), AUX_NT); __builtin_amdgcn_sched_barrier(0); } while (0)
#pragma unroll
  for (int d = 0; d < RING; ++d) { ISSUEK(d, sbase + d * 16384u); ISSUEV(d, sbase + d * 16384u); }

  {
    const v4u n0 = ln_row(x0), n1 = ln_row(x1);
    if (g == w) { *(LAS v4u*)(L + L_XN + lane * 16) = n0; *(LAS v4u*)(L + L_XN + 1024 + lane * 16) = n1; }
    v4u fr[4];
    transpose_tile(L + L_TR + w * 8192, lane, wqr, fr);
    f32x4 acc = {0.f, 0.f, 0.f, 0.f};
#pragma unroll
    for (int i = 0; i < 4; ++i) acc = mfma32(fr[i], *(LAS v4u*)(L + L_XN + (c & 1) * 1024 + (128 * w + 32 * i + 8 * g) * 2), acc);
    if (c < 2) *(LAS f32x4*)(L + L_RED + w * 128 + c * 64 + g * 16) = acc;
  }
  STAMP(1);
  barrier();
  barrier();
  STAMP(2);
  v4u qf[4];
#pragma unroll
  for (int s = 0; s < 4; ++s) qf[s] = *(LAS v4u*)(L + L_QF + (c >> 2) * 256 + s * 64 + g * 16);

  const unsigned m0 = (c & 3) == 0 ? 0xffffu : (c & 3) == 1 ? 0xffff0000u : 0u;
  const unsigned m1 = (c & 3) == 2 ? 0xffffu : (c & 3) == 3 ? 0xffff0000u : 0u;
  constexpr float SCALE = 0.088388347648318440f, LOG2E = 1.4426950408889634f;
  f32x4 acc0 = {0.f, 0.f, 0.f, 0.f}, acc1 = {0.f, 0.f, 0.f, 0.f};
  float m = -INFINITY, l = 0.f;
  f32x4 sc;
#define QK(d) do { sc = f32x4{0.f, 0.f, 0.f, 0.f}; _Pragma("unroll") for (int s = 0; s < 4; ++s) sc = mfma32(kb[d][s], qf[s], sc); \
    asm volatile("" : "+v"(sc) :: "memory"); __builtin_amdgcn_sched_barrier(0); } while (0)
#define PV(d) do { \
    const unsigned r01 = cvtpk(sc[0], sc[1]), r23 = cvtpk(sc[2], sc[3]); \
    const unsigned t01 = cvtpk(bflo(r01) * SCALE, bfhi(r01) * SCALE), t23 = cvtpk(bflo(r23) * SCALE, bfhi(r23) * SCALE); \
    const float s0 = bflo(t01), s1 = bfhi(t01), s2 = bflo(t23), s3 = bfhi(t23); \
    float mx = fmaxf(fmaxf(s0, s1), fmaxf(s2, s3)); mx = sw16_max(mx, mx); mx = sw32_max(mx, mx); \
    if (__any(mx > m)) { const float mn = fmaxf(m, mx); const float al = __builtin_amdgcn_exp2f((m - mn) * LOG2E); m = mn; l *= al; acc0 *= al; acc1 *= al; } \
    const float sv[4] = {s0, s1, s2, s3}; \
    _Pragma("unroll") for (int i = 0; i < 4; ++i) { \
      const float p_ = __builtin_amdgcn_exp2f((sv[i] - m) * LOG2E); const unsigned pp = cvtpk(p_, p_); l += bflo(pp); \
      const v2u bb = {pp & m0, pp & m1}; const v4u vv = vb[d][i]; \
      acc0 = mfma16(v2u{vv[0], vv[1]}, bb, acc0); acc1 = mfma16(v2u{vv[2], vv[3]}, bb, acc1); } \
    asm volatile("" : "+v"(acc0), "+v"(acc1) :: "memory"); __builtin_amdgcn_sched_barrier(0); } while (0)
  static_assert(NTILE % RING == 0, "");
#pragma unroll 1
  for (int it = 0; it < NTILE - RING; it += RING) {
#pragma unroll
    for (int d = 0; d < RING; ++d) { const unsigned so = sbase + (unsigned)(it + d + RING) * 16384u; QK(d); ISSUEK(d, so); PV(d); ISSUEV(d, so); }
  }
#pragma unroll
  for (int d = 0; d < RING; ++d) { QK(d); PV(d); }
  STAMP(3);

  l = sw16_sum(l, l); l = sw32_sum(l, l);
  {
    LAS float* rec = (LAS float*)(L + L_MRG) + (w * 4 + (c >> 2)) * 132;
#pragma unroll
    for (int r = 0; r < 4; ++r) { rec[4 + 8 * (4 * g + r) + (c & 3)] = acc0[r]; rec[4 + 8 * (4 * g + r) + 4 + (c & 3)] = acc1[r]; }
    if (g == 0 && (c & 3) == 0) *(LAS f32x2*)rec = f32x2{m, l};
  }
  barrier();
  {
    f32x2 ml[4], ov[4];
#pragma unroll
    for (int ww = 0; ww < 4; ++ww) { LAS float* rec = (LAS float*)(L + L_MRG) + (ww * 4 + w) * 132; ml[ww] = *(LAS f32x2*)rec; ov[ww] = *(LAS f32x2*)(rec + 4 + 2 * lane); }
    const float M = fmaxf(fmaxf(ml[0][0], ml[1][0]), fmaxf(ml[2][0], ml[3][0]));
    float Ls = 0.f; f32x2 o = {0.f, 0.f};
#pragma unroll
    for (int ww = 0; ww < 4; ++ww) { const float wt = __builtin_amdgcn_exp2f((ml[ww][0] - M) * LOG2E); Ls = fmaf(wt, ml[ww][1], Ls); o += wt * ov[ww]; }
    const float inv = __builtin_amdgcn_rcpf(Ls);
    const int slot = (b * HQ + kvh * 4 + w) * NSPLIT + sp;
    __builtin_amdgcn_raw_buffer_store_b64(v2u{nosent(__float_as_uint(o[0] * inv)), nosent(__float_as_uint(o[1] * inv))}, rw, OFF_PART + slot * 512 + lane * 8, 0, AUX_SC1);
    if (lane == 0) __builtin_amdgcn_raw_buffer_store_b32(nosent(__float_as_uint(fmaf(M, LOG2E, __builtin_amdgcn_logf(Ls)))), rw, OFF_LSE + slot * 4, 0, AUX_SC1);
  }
  STAMP(4);
  const unsigned dm0 = (c & 7) == 0 ? 0xffffu : (c & 7) == 1 ? 0xffff0000u : 0u, dm1 = (c & 7) == 2 ? 0xffffu : (c & 7) == 3 ? 0xffff0000u : 0u,
                 dm2 = (c & 7) == 4 ? 0xffffu : (c & 7) == 5 ? 0xffff0000u : 0u, dm3 = (c & 7) == 6 ? 0xffffu : (c & 7) == 7 ? 0xffff0000u : 0u;

  if (role == 2) {
    const int head = bi >> 1, s = lane >> 2;
    const int po = OFF_PART + (head * NSPLIT + s) * 512 + (bi & 1) * 256 + w * 64 + (lane & 3) * 16, lo = OFF_LSE + (head * NSPLIT + s) * 4;
    v4u a0, a1; unsigned e0, e1;
    for (unsigned spins = 0; spins < SPIN_CAP; ++spins) {
      asm volatile("" ::: "memory");
      a0 = __builtin_amdgcn_raw_buffer_load_b128(rw, po, 0, AUX_SC1);
      a1 = __builtin_amdgcn_raw_buffer_load_b128(rw, po + HQ * NSPLIT * 512, 0, AUX_SC1);
      e0 = __builtin_amdgcn_raw_buffer_load_b32(rw, lo, 0, AUX_SC1);
      e1 = __builtin_amdgcn_raw_buffer_load_b32(rw, lo + HQ * NSPLIT * 4, 0, AUX_SC1);
      if (__all(ok4(a0) & ok4(a1) & (e0 != SENT) & (e1 != SENT))) break;
    }
    STAMP(5);
    const float l0 = __uint_as_float(e0), l1 = __uint_as_float(e1);
    float x0m = fmaxf(l0, dpp<ROR4>(l0)), x1m = fmaxf(l1, dpp<ROR4>(l1));
    x0m = fmaxf(x0m, dpp<ROR8>(x0m)); x1m = fmaxf(x1m, dpp<ROR8>(x1m));
    x0m = sw16_max(x0m, x0m); x1m = sw16_max(x1m, x1m); x0m = sw32_max(x0m, x0m); x1m = sw32_max(x1m, x1m);
    const float w0 = __builtin_amdgcn_exp2f(l0 - x0m), w1 = __builtin_amdgcn_exp2f(l1 - x1m);
    float v[4];
#pragma unroll
    for (int e = 0; e < 4; ++e) v[e] = sw32_sum(w0 * __uint_as_float(a0[e]), w1 * __uint_as_float(a1[e]));
    float ws_ = sw32_sum(w0, w1);
    float u0 = sw16_sum(v[0], v[1]), u1 = sw16_sum(v[2], v[3]);
    ws_ = sw16_sum(ws_, ws_);
    u0 += dpp<ROR8>(u0); u1 += dpp<ROR8>(u1); ws_ += dpp<ROR8>(ws_);
    u0 += dpp<ROR4>(u0); u1 += dpp<ROR4>(u1); ws_ += dpp<ROR4>(ws_);
    const float inv = __builtin_amdgcn_rcpf(ws_);
    const float uo = ((lane & 4) ? u1 : u0) * inv;
    if ((lane & 8) == 0) *(LAS unsigned*)(L + L_OB + ((g >> 1) * 4 + (g & 1) + ((lane & 4) >> 1)) * 64 + (4 * w + (lane & 3)) * 4) = cvtpk(uo, uo);
    barrier();
    f32x4 acc = {0.f, 0.f, 0.f, 0.f};
#pragma unroll
    for (int q = 0; q < 4; ++q) {
      const v4u pv = *(LAS v4u*)(L + L_OB + ((c >> 3) * 4 + g) * 64 + q * 16);
#pragma unroll
      for (int i = 0; i < 4; ++i) acc = mfma32(wr[q * 4 + i], v4u{pv[i] & dm0, pv[i] & dm1, pv[i] & dm2, pv[i] & dm3}, acc);
    }
    __builtin_amdgcn_raw_buffer_store_b128(nosent4(acc), rw, OFF_P1 + bi * 4096 + tid * 16, 0, AUX_SC1);
    STAMP(6);
  } else if (role == 3) {
    const int rb = rj & 1, col0 = (rj >> 1) * 32;
    const unsigned xp = *(const GAS unsigned*)(x + rb * D + col0 + 2 * c);
    const unsigned tag = *(LAS unsigned*)(L + L_RED + 1020);
    float h0, h1;
    {
      const int po = OFF_P1 + (16 * w + (lane >> 3)) * 4096 + rj * 128 + (lane & 7) * 16;
      v4u a0, a1;
      for (unsigned spins = 0; spins < SPIN_CAP; ++spins) {
        asm volatile("" ::: "memory");
        a0 = __builtin_amdgcn_raw_buffer_load_b128(rw, po, 0, AUX_SC1);
        a1 = __builtin_amdgcn_raw_buffer_load_b128(rw, po + 8 * 4096, 0, AUX_SC1);
        if (__all(ok4(a0) & ok4(a1))) break;
      }
      STAMP(5);
      const f32x4 v = __builtin_bit_cast(f32x4, a0) + __builtin_bit_cast(f32x4, a1);
      float u = sw16_sum(sw32_sum(v[0], v[2]), sw32_sum(v[1], v[3]));
      u += dpp<ROR8>(u);
      if ((lane & 8) == 0) *(LAS float*)(L + L_RR + w * 128 + (8 * g + (lane & 7)) * 4) = u;
      barrier();
      f32x2 s = {0.f, 0.f};
#pragma unroll
      for (int ww = 0; ww < 4; ++ww) s += *(LAS f32x2*)(L + L_RR + ww * 128 + c * 8);
      const unsigned at = cvtpk(s[0], s[1]);
      const unsigned hp = cvtpk(bflo(xp) + bflo(at), bfhi(xp) + bfhi(at));
      h0 = bflo(hp); h1 = bfhi(hp);
      if (w < 2) __builtin_amdgcn_raw_buffer_store_b32(nosent(hp), rw, OFF_HB + (w * 4 + g) * 2048 + rb * 1024 + col0 * 2 + c * 4, 0, AUX_SC1);
      STAMP(6);
    }
    {
      const int pp = lane >> 3;
      const int po = OFF_P2 + (28 * w + pp) * 4096 + rj * 128 + (lane & 7) * 16;
      const int o3 = pp < 4 ? 24 * 4096 : 0;
      v4u a0, a1, a2, a3;
      for (unsigned spins = 0; spins < SPIN_CAP; ++spins) {
        asm volatile("" ::: "memory");
        a0 = __builtin_amdgcn_raw_buffer_load_b128(rw, po, 0, AUX_SC1);
        a1 = __builtin_amdgcn_raw_buffer_load_b128(rw, po + 8 * 4096, 0, AUX_SC1);
        a2 = __builtin_amdgcn_raw_buffer_load_b128(rw, po + 16 * 4096, 0, AUX_SC1);
        a3 = __builtin_amdgcn_raw_buffer_load_b128(rw, po + o3, 0, AUX_SC1);
        if (__all(ok4(a0) & ok4(a1) & ok4(a2) & ok4(a3))) break;
      }
      STAMP(7);
      f32x4 v = (__builtin_bit_cast(f32x4, a0) + __builtin_bit_cast(f32x4, a1)) + __builtin_bit_cast(f32x4, a2);
      if (pp < 4) v += __builtin_bit_cast(f32x4, a3);
      float u = sw16_sum(sw32_sum(v[0], v[2]), sw32_sum(v[1], v[3]));
      u += dpp<ROR8>(u);
      if ((lane & 8) == 0) *(LAS float*)(L + L_RR + 512 + w * 128 + (8 * g + (lane & 7)) * 4) = u;
      barrier();
      if (tid < 16) {
        f32x2 s = {0.f, 0.f};
#pragma unroll
        for (int ww = 0; ww < 4; ++ww) s += *(LAS f32x2*)(L + L_RR + 512 + ww * 128 + c * 8);
        const unsigned dn = cvtpk(s[0], s[1]);
        *(GAS unsigned*)(out + rb * D + col0 + 2 * c) = cvtpk(h0 + bflo(dn), h1 + bfhi(dn));
        if (rj == 0 && tid == 0) __builtin_amdgcn_raw_buffer_store_b32(tag, rw, OFF_EPOCH, 0, AUX_SC1);
      }
      STAMP(8);
    }
  } else if (role == 1) {
    v4u fg[4], fu[4];
    transpose_tile(L + L_TR + w * 8192, lane, wr, fg);
    transpose_tile(L + L_TR + w * 8192 + 4096, lane, wr + 4, fu);
    if (w < 2) {
      const int ho = OFF_HB + (ci & 7) * 2048 + w * 1024 + lane * 16;
      v4u hv;
      for (unsigned spins = 0; spins < SPIN_CAP; ++spins) {
        asm volatile("" ::: "memory");
        hv = __builtin_amdgcn_raw_buffer_load_b128(rw, ho, 0, AUX_SC1);
        if (__all(ok4(hv))) break;
      }
      STAMP(5);
      *(LAS v4u*)(L + L_XN + w * 1024 + lane * 16) = ln_row(hv);
    }
    barrier();
    f32x4 ag = {0.f, 0.f, 0.f, 0.f}, au = {0.f, 0.f, 0.f, 0.f};
#pragma unroll
    for (int i = 0; i < 4; ++i) {
      const v4u xb = *(LAS v4u*)(L + L_XN + (c & 1) * 1024 + (128 * w + 32 * i + 8 * g) * 2);
      ag = mfma32(fg[i], xb, ag); au = mfma32(fu[i], xb, au);
    }
    if (c < 2) {
#pragma unroll
      for (int i = 0; i < 4; ++i) { *(LAS float*)(L + L_RED + (c * 16 + 4 * g + i) * 16 + w * 4) = ag[i]; *(LAS float*)(L + L_RED + 512 + (c * 16 + 4 * g + i) * 16 + w * 4) = au[i]; }
    }
    barrier();
    const int fo = ((c >> 3) * 16 + 4 * (c & 3) + g) * 16;
    const f32x4 pg = *(LAS f32x4*)(L + L_RED + fo), pu = *(LAS f32x4*)(L + L_RED + 512 + fo);
    const unsigned gu = cvtpk((pg[0] + pg[1]) + (pg[2] + pg[3]), (pu[0] + pu[1]) + (pu[2] + pu[3]));
    const float gg = bflo(gu);
    const float sg = rbf(gg * __builtin_amdgcn_rcpf(1.0f + __builtin_amdgcn_exp2f(-gg * LOG2E)));
    const float av = sg * bfhi(gu);
    const unsigned ap = cvtpk(av, av);
    const unsigned p0 = dppu<0x00>(ap), p1 = dppu<0x55>(ap), p2 = dppu<0xAA>(ap), p3 = dppu<0xFF>(ap);
    f32x4 acc = {0.f, 0.f, 0.f, 0.f};
    acc = mfma32(wr[8], v4u{p0 & dm0, p0 & dm1, p0 & dm2, p0 & dm3}, acc);
    acc = mfma32(wr[9], v4u{p1 & dm0, p1 & dm1, p1 & dm2, p1 & dm3}, acc);
    acc = mfma32(wr[10], v4u{p2 & dm0, p2 & dm1, p2 & dm2, p2 & dm3}, acc);
    acc = mfma32(wr[11], v4u{p3 & dm0, p3 & dm1, p3 & dm2, p3 & dm3}, acc);
    __builtin_amdgcn_raw_buffer_store_b128(nosent4(acc), rw, OFF_P2 + ci * 4096 + tid * 16, 0, AUX_SC1);
    STAMP(6);
  }
  if (wg == 0) while ((int)((unsigned)__builtin_amdgcn_s_memrealtime() - (tstart + 7000u)) < 0) __builtin_amdgcn_s_sleep(1);
}

extern "C" void kernel_launch(void* const* d_in, const int* in_sizes, int n_in, void* d_out, int out_size, void* d_ws, size_t ws_size, hipStream_t stream) {
  fused_block<<<NWG, NTHR, 0, stream>>>((const u16*)d_in[0], (const u16*)d_in[1], (const u16*)d_in[2], (const u16*)d_in[3], (const u16*)d_in[4],
                                        (const u16*)d_in[6], (const u16*)d_in[7], (const u16*)d_in[8], (u16*)d_out, (unsigned char*)d_ws);
}
```

```cpp
#pragma clang diagnostic ignored "-Wint-to-pointer-cast"
#include <hip/hip_runtime.h>
#include <hip/hip_bf16.h>
#include <cstdint>

typedef unsigned short u16;
typedef unsigned v4u __attribute__((ext_vector_type(4)));
typedef unsigned v2u __attribute__((ext_vector_type(2)));
typedef float f32x4 __attribute__((ext_vector_type(4)));
typedef float f32x2 __attribute__((ext_vector_type(2)));
typedef short s16x4 __attribute__((ext_vector_type(4)));
typedef __bf16 bf16x8_t __attribute__((ext_vector_type(8)));
typedef __bf16 bf16x2_t __attribute__((ext_vector_type(2)));
#define LAS __attribute__((address_space(3)))
#define GAS __attribute__((address_space(1)))

constexpr int D = 512, HQ = 32, HKV = 8, SK = 32768, DH = 128, F = 1792;
constexpr int NWG = 256, NTHR = 320, NSPLIT = 16;
#define TUNE_PARAM
#define TUNE_ARG
#define ABL 0
#define TL_C 120
#define TL_B 128
#define TL_Q 128
#define T_P 500
#define PD1 2
#define PD2 3
#define PD3 1
#define PD4 3
#define PSL 1
#define SPEC_N 4
#define PACE_N 4
#define NPRE 5
#define CODE_BYTES 16384
constexpr int NB = 64, NC = 56, NR = 64, NREP = 8;
constexpr int OFF_EPOCH = 0;
constexpr int OFF_LSE = 0x1000;
constexpr int OFF_HB = 0x190000;
constexpr int OFF_PART = 0x10000;
constexpr int OFF_P1 = 0x90000;
constexpr int OFF_P2 = 0xD0000;
constexpr int OFF_Q = 0x180000;
constexpr int WS_BYTES = 0x1A0000, OFF_DBG = 0x200000;
constexpr int AUX_NT = 2, AUX_SC1 = 16;
constexpr unsigned SPIN_CAP = 1u << 16;
constexpr int L_W = 0;
constexpr int WREG = 24576;
constexpr int L_QF = 4 * WREG;
constexpr int L_TAG = L_QF + 1024;
constexpr int L_S = L_TAG + 16;
constexpr int L_XN = 0;
constexpr int L_RED = L_XN + 2048;
constexpr int L_MRG = L_RED + 2048;
constexpr int L_OB = L_MRG + 4 * 4 * 132 * 4;
constexpr int L_RR = L_OB + 512;
constexpr int L_ST = L_RR + 1024;
constexpr int S_BYTES = L_ST + 32;
constexpr int L_PF = L_S + S_BYTES;
constexpr int L_DBG = L_PF + 256;
constexpr int L_BYTES = L_DBG + 128;

__device__ __forceinline__ float bflo(unsigned v) { return __uint_as_float(v << 16); }
__device__ __forceinline__ float bfhi(unsigned v) { return __uint_as_float(v & 0xffff0000u); }
__device__ __forceinline__ unsigned cvtpk(float lo, float hi) { return __builtin_bit_cast(unsigned, __builtin_convertvector(f32x2{lo, hi}, bf16x2_t)); }
__device__ __forceinline__ float rbf(float x) { return bflo(cvtpk(x, x)); }
__device__ __forceinline__ bool tagged(v4u v, unsigned tag) { return (v[1] == tag) & (v[3] == tag); }
__device__ __forceinline__ float u2f(unsigned v) { return __uint_as_float(v); }
__device__ __forceinline__ unsigned f2u(float v) { return __float_as_uint(v); }

__device__ __forceinline__ float vmax(float a, float b) { return __builtin_elementwise_maximum(a, b); }
template <int CTRL> __device__ __forceinline__ float dpp(float x) { return __builtin_bit_cast(float, __builtin_amdgcn_mov_dpp(__builtin_bit_cast(int, x), CTRL, 0xf, 0xf, true)); }
template <int CTRL> __device__ __forceinline__ unsigned dppu(unsigned x) { return (unsigned)__builtin_amdgcn_mov_dpp((int)x, CTRL, 0xf, 0xf, true); }
constexpr int XOR1 = 0xB1, XOR2 = 0x4E, ROR8 = 0x128, ROR4 = 0x124, ROR12 = 0x12C;
__device__ __forceinline__ float sw32_sum(float a, float b) { auto r = __builtin_amdgcn_permlane32_swap(f2u(a), f2u(b), false, false); return u2f(r[0]) + u2f(r[1]); }
__device__ __forceinline__ float sw16_sum(float a, float b) { auto r = __builtin_amdgcn_permlane16_swap(f2u(a), f2u(b), false, false); return u2f(r[0]) + u2f(r[1]); }
__device__ __forceinline__ float sw32_max(float a, float b) { auto r = __builtin_amdgcn_permlane32_swap(f2u(a), f2u(b), false, false); return vmax(u2f(r[0]), u2f(r[1])); }
__device__ __forceinline__ float sw16_max(float a, float b) { auto r = __builtin_amdgcn_permlane16_swap(f2u(a), f2u(b), false, false); return vmax(u2f(r[0]), u2f(r[1])); }
__device__ __forceinline__ float wave_sum(float x) {
  x += dpp<XOR1>(x); x += dpp<XOR2>(x); x += dpp<ROR4>(x); x += dpp<ROR8>(x);
  x = sw16_sum(x, x); return sw32_sum(x, x);
}
__device__ __forceinline__ f32x4 mfma32(v4u a, v4u b, f32x4 c) { return __builtin_amdgcn_mfma_f32_16x16x32_bf16(__builtin_bit_cast(bf16x8_t, a), __builtin_bit_cast(bf16x8_t, b), c, 0, 0, 0); }
__device__ __forceinline__ f32x4 mfma16(v2u a, v2u b, f32x4 c) { return __builtin_amdgcn_mfma_f32_16x16x16bf16_1k(__builtin_bit_cast(s16x4, a), __builtin_bit_cast(s16x4, b), c, 0, 0, 0); }
__device__ __forceinline__ void barrier() { asm volatile("s_waitcnt lgkmcnt(0)\n\ts_barrier" ::: "memory"); }
__device__ __forceinline__ v4u tr_frag(LAS unsigned char* lo, LAS unsigned char* hi) {
  const v2u a = __builtin_bit_cast(v2u, __builtin_amdgcn_ds_read_tr16_b64_v4i16((LAS s16x4*)lo));
  const v2u b = __builtin_bit_cast(v2u, __builtin_amdgcn_ds_read_tr16_b64_v4i16((LAS s16x4*)hi));
  return v4u{a[0], a[1], b[0], b[1]};
}

#define AND4(T0, T1, T2, T3, SRC, MOD, SFX) "v_and_b32" SFX " " T0 ", " SRC ", %[m0]" MOD "\n\tv_and_b32" SFX " " T1 ", " SRC ", %[m1]" MOD "\n\tv_and_b32" SFX " " T2 ", " SRC ", %[m2]" MOD "\n\tv_and_b32" SFX " " T3 ", " SRC ", %[m3]" MOD "\n\t"
#define ANDA(SRC, MOD, SFX) AND4("v240", "v241", "v242", "v243", SRC, MOD, SFX)
#define ANDB(SRC, MOD, SFX) AND4("v244", "v245", "v246", "v247", SRC, MOD, SFX)
#define MFA(W) "v_mfma_f32_16x16x32_bf16 %[acc], " W ", v[240:243], %[acc]\n\t"
#define MFB(W) "v_mfma_f32_16x16x32_bf16 %[acc], " W ", v[244:247], %[acc]\n\t"
#define CHAIN_CLOB "v240", "v241", "v242", "v243", "v244", "v245", "v246", "v247"
#define CHAIN_W(W) [w0] "v"(W[0]), [w1] "v"(W[1]), [w2] "v"(W[2]), [w3] "v"(W[3]), [w4] "v"(W[4]), [w5] "v"(W[5]), [w6] "v"(W[6]), [w7] "v"(W[7])
__device__ __forceinline__ void diag_chain_regs(f32x4& acc, const v4u* W, v4u pa, v4u pb, unsigned m0, unsigned m1, unsigned m2, unsigned m3) {
  asm volatile(ANDA("%[p0]", "", "") ANDB("%[p1]", "", "") MFA("%[w0]") ANDA("%[p2]", "", "") MFB("%[w1]") ANDB("%[p3]", "", "") MFA("%[w2]") ANDA("%[p4]", "", "") MFB("%[w3]")
               ANDB("%[p5]", "", "") MFA("%[w4]") ANDA("%[p6]", "", "") MFB("%[w5]") ANDB("%[p7]", "", "") MFA("%[w6]") "s_nop 0\n\t" MFB("%[w7]")
               : [acc] "+v"(acc)
               : CHAIN_W(W), [p0] "v"(pa[0]), [p1] "v"(pa[1]), [p2] "v"(pa[2]), [p3] "v"(pa[3]), [p4] "v"(pb[0]), [p5] "v"(pb[1]), [p6] "v"(pb[2]), [p7] "v"(pb[3]),
                 [m0] "v"(m0), [m1] "v"(m1), [m2] "v"(m2), [m3] "v"(m3)
               : CHAIN_CLOB);
}
#define QP(t) " quad_perm:[" #t "," #t "," #t "," #t "] row_mask:0xf bank_mask:0xf"
__device__ __forceinline__ void diag_chain_quad(f32x4& acc, const v4u* W, unsigned pa, unsigned pb, unsigned m0, unsigned m1, unsigned m2, unsigned m3) {
  asm volatile("s_nop 1\n\t" ANDA("%[pa]", QP(0), "_dpp") ANDB("%[pa]", QP(1), "_dpp") MFA("%[w0]") ANDA("%[pa]", QP(2), "_dpp") MFB("%[w1]") ANDB("%[pa]", QP(3), "_dpp") MFA("%[w2]")
               ANDA("%[pb]", QP(0), "_dpp") MFB("%[w3]") ANDB("%[pb]", QP(1), "_dpp") MFA("%[w4]") ANDA("%[pb]", QP(2), "_dpp") MFB("%[w5]") ANDB("%[pb]", QP(3), "_dpp") MFA("%[w6]")
               "s_nop 0\n\t" MFB("%[w7]")
               : [acc] "+v"(acc)
               : CHAIN_W(W), [pa] "v"(pa), [pb] "v"(pb), [m0] "v"(m0), [m1] "v"(m1), [m2] "v"(m2), [m3] "v"(m3)
               : CHAIN_CLOB);
}
__device__ __forceinline__ void mfma_drain(f32x4& acc) { asm volatile("s_nop 7" : "+v"(acc)); }

__device__ __forceinline__ v4u ln_row(v4u xv) {
  float x[8];
#pragma unroll
  for (int i = 0; i < 4; ++i) { x[2 * i] = bflo(xv[i]); x[2 * i + 1] = bfhi(xv[i]); }
  const float mean = wave_sum(((x[0] + x[1]) + (x[2] + x[3])) + ((x[4] + x[5]) + (x[6] + x[7]))) * (1.0f / D);
  float vs = 0.f;
#pragma unroll
  for (int i = 0; i < 8; ++i) { const float d = x[i] - mean; vs = fmaf(d, d, vs); }
  const float var = wave_sum(vs) * (1.0f / D);
  const float mb = rbf(mean);
  const float rs = __builtin_amdgcn_rcpf(rbf(__builtin_amdgcn_sqrtf(rbf(rbf(var) + 1e-5f))));
  v4u r;
#pragma unroll
  for (int i = 0; i < 4; ++i) { const unsigned d = cvtpk(x[2 * i] - mb, x[2 * i + 1] - mb); r[i] = cvtpk(bflo(d) * rs, bfhi(d) * rs); }
  return r;
}
__device__ __forceinline__ void glds16(const u16* gsrc, unsigned lds_dst) {
  unsigned keep;
  asm volatile("s_mov_b32 %0, m0\n\ts_mov_b32 m0, %2\n\ts_nop 0\n\tglobal_load_lds_dwordx4 %1, off nt\n\ts_mov_b32 m0, %0" : "=&s"(keep) : "v"(gsrc), "s"(lds_dst) : "memory");
}

__device__ __forceinline__ unsigned lowbytes(v4u v) { return __builtin_amdgcn_perm(v[1], v[0], 0x0c0c0400u) | __builtin_amdgcn_perm(v[3], v[2], 0x04000c0cu); }
template <int PD_, int TSH>
__device__ __forceinline__ void poll2(const unsigned char* ws, int o0, int o1, unsigned tag, v4u& r0, v4u& r1) {
  r0 = v4u{~tag, ~tag, ~tag, ~tag}; r1 = r0;
#define PASS() asm volatile("global_load_dwordx4 %0, %2, %4 sc1\n\tglobal_load_dwordx4 %1, %3, %4 sc1" : "+v"(r0), "+v"(r1) : "v"(o0), "v"(o1), "s"(ws) : "memory")
#pragma unroll
  for (int k = 0; k < PD_ - 1; ++k) { PASS(); __builtin_amdgcn_s_sleep(PSL); }
#pragma unroll 1
  for (unsigned k = 0; k < SPIN_CAP; ++k) {
    PASS();
    asm volatile("s_waitcnt vmcnt(%2)" : "+v"(r0), "+v"(r1) : "i"((PD_ - 1) * 2) : "memory");
    if (TSH == 16) { if (__all((__builtin_amdgcn_perm(r0[3], r0[1], 0x07060302u) == tag) & (__builtin_amdgcn_perm(r1[3], r1[1], 0x07060302u) == tag))) break; }
    else if (TSH < 0) { if (__all((lowbytes(r0) == tag) & (lowbytes(r1) == tag))) break; }
    else if (__all((((r0[1] ^ tag) | (r0[3] ^ tag) | (r1[1] ^ tag) | (r1[3] ^ tag)) >> (TSH < 0 ? 0 : TSH)) == 0u)) break;
    if (PD_ > 1) __builtin_amdgcn_s_sleep(PSL);
  }
#undef PASS
  asm volatile("s_nop 3 ; POLL_END" : "+v"(r0), "+v"(r1));
}
__device__ __forceinline__ void poll_done(v4u& r0, v4u& r1) { asm volatile("s_waitcnt vmcnt(0)" : "+v"(r0), "+v"(r1) :: "memory"); }

#define STAMP(i) do { } while (0)
#define DUMP_STAMPS() do { } while (0)
#define LD128(off) __builtin_amdgcn_raw_buffer_load_b128(rw, (off), 0, AUX_SC1)
#define ST128(v, off) __builtin_amdgcn_raw_buffer_store_b128((v), rw, (off), 0, AUX_SC1)
#define SPIN_LOOP _Pragma("unroll 1") for (unsigned spins = 0; spins < SPIN_CAP; ++spins)
#define NOHOIST() asm volatile("" ::: "memory")

__global__ void __launch_bounds__(NTHR, 2) fused_block(const u16* x, const u16* kc, const u16* vc, const u16* wq, unsigned char* ws,
                                                       const u16* wo, const u16* wgate, const u16* wup, const u16* wdown, u16* out TUNE_PARAM) {
  __shared__ __attribute__((aligned(16))) unsigned char smem[L_BYTES];
  LAS unsigned char* L = (LAS unsigned char*)smem;
  STAMP(0);
  const unsigned tstart = (unsigned)__builtin_amdgcn_s_memrealtime();
  const int wg = blockIdx.x, xcd = wg & 7, idx = wg >> 3;
  const int kvh = xcd, b = idx & 1, sp = idx >> 1;
  int lane = threadIdx.x & 63;
  const int w = __builtin_amdgcn_readfirstlane(threadIdx.x >> 6);
  int c = lane & 15, g = lane >> 4;
  if (w == 4 && !(ABL & 32)) {
    unsigned long long pc;
    asm volatile("s_getpc_b64 %0" : "=s"(pc));
    if ((lane * 32 + idx) * 128 < CODE_BYTES) {
      unsigned keep;
      asm volatile("s_mov_b32 %0, m0\n\ts_mov_b32 m0, %3\n\ts_nop 0\n\tglobal_load_lds_dword %1, %2\n\ts_mov_b32 m0, %0"
                   : "=&s"(keep) : "v"((lane * 32 + idx) * 128), "s"(pc & ~255ull), "s"((unsigned)(uintptr_t)L + L_PF) : "memory");
    }
  }
  asm volatile("" : "+s"(x), "+s"(kc), "+s"(vc), "+s"(wq), "+s"(ws), "+s"(wo), "+s"(wgate), "+s"(wup), "+s"(wdown), "+s"(out));
  const int nc = 4 - b, nbw = 3 + 2 * b;
  const int role = sp < nc ? 1 : sp < nc + nbw ? 2 : (sp >= 8 && sp < 10) ? 3 : 0;
  const bool qrole = sp >= 12;
  const int ci = xcd * 7 + 4 * b + sp, bi = xcd * 8 + 3 * b + sp - nc, rj = xcd * 4 + idx - 16, qi = xcd * 8 + idx - 24;
  const __amdgpu_buffer_rsrc_t rw = __builtin_amdgcn_make_buffer_rsrc((void*)ws, 0, WS_BYTES, 0x00020000);
  const unsigned lds0 = (unsigned)(uintptr_t)L;
  LAS unsigned char* S = L + L_S;
#define WAIT_UNTIL(t, slp) do { while ((int)((unsigned)__builtin_amdgcn_s_memrealtime() - (tstart + (unsigned)(t))) < 0) __builtin_amdgcn_s_sleep(slp); } while (0)
  int q4 = (lane >> 2) & 3, p4 = lane & 3;
  const unsigned m0 = (c & 3) == 0 ? 0xffffu : (c & 3) == 1 ? 0xffff0000u : 0u;
  const unsigned m1 = (c & 3) == 2 ? 0xffffu : (c & 3) == 3 ? 0xffff0000u : 0u;
  constexpr float SCALE = 0.088388347648318440f, LOG2E = 1.4426950408889634f;
  v4u kb[NPRE + 1][4], vb[NPRE + 1][4];
  v4u qf[4];
  f32x4 acc0 = {0.f, 0.f, 0.f, 0.f}, acc1 = {0.f, 0.f, 0.f, 0.f}, sc;
  float m = -INFINITY, l = 0.f;
  unsigned tag;
#define QK(d) do { sc = f32x4{0.f, 0.f, 0.f, 0.f}; _Pragma("unroll") for (int s = 0; s < 4; ++s) sc = mfma32(kb[d][s], qf[s], sc); \
    asm volatile("" : "+v"(sc) :: "memory"); __builtin_amdgcn_sched_barrier(0); } while (0)
#define PV(d) do { \
    const unsigned r01 = cvtpk(sc[0], sc[1]), r23 = cvtpk(sc[2], sc[3]); \
    const unsigned t01 = cvtpk(bflo(r01) * SCALE, bfhi(r01) * SCALE), t23 = cvtpk(bflo(r23) * SCALE, bfhi(r23) * SCALE); \
    const float s0 = bflo(t01), s1 = bfhi(t01), s2 = bflo(t23), s3 = bfhi(t23); \
    float mx = vmax(vmax(s0, s1), vmax(s2, s3)); mx = sw16_max(mx, mx); mx = sw32_max(mx, mx); \
    if (__any(!(mx <= m))) { const float mn = vmax(m, mx); const float al = __builtin_amdgcn_exp2f((m - mn) * LOG2E); m = mn; l *= al; acc0 *= al; acc1 *= al; } \
    const float sv[4] = {s0, s1, s2, s3}; \
    _Pragma("unroll") for (int i = 0; i < 4; ++i) { \
      const float p_ = __builtin_amdgcn_exp2f((sv[i] - m) * LOG2E); const unsigned pp = cvtpk(p_, p_); l += bflo(pp); \
      const v2u bb = {pp & m0, pp & m1}; const v4u vv = vb[d][i]; \
      acc0 = mfma16(v2u{vv[0], vv[1]}, bb, acc0); acc1 = mfma16(v2u{vv[2], vv[3]}, bb, acc1); } \
    asm volatile("" : "+v"(acc0), "+v"(acc1) :: "memory"); __builtin_amdgcn_sched_barrier(0); } while (0)

  if (w == 4) {
    __builtin_amdgcn_s_setprio(3);
    if (qrole) {
      const v4u x0 = *(const GAS v4u*)(x + lane * 8), x1 = *(const GAS v4u*)(x + D + lane * 8);
      tag = __builtin_amdgcn_raw_buffer_load_b32(rw, OFF_EPOCH, 0, AUX_SC1) + 1u;
      const v4u n0 = ln_row(x0), n1 = ln_row(x1);
      *(LAS v4u*)(S + L_XN + lane * 16) = n0; *(LAS v4u*)(S + L_XN + 1024 + lane * 16) = n1;
      STAMP(9);
      barrier();
      barrier();
      f32x2 s = {0.f, 0.f};
#pragma unroll
      for (int ww = 0; ww < 4; ++ww) s += *(LAS f32x2*)(S + L_RED + ww * 512 + lane * 8);
      __builtin_amdgcn_raw_buffer_store_b64(v2u{cvtpk(s[0], s[1]), tag}, rw, OFF_Q + ((lane >> 5) * 2048 + qi * 32 + (lane & 31)) * 8, 0, AUX_SC1);
    } else {
      tag = __builtin_amdgcn_raw_buffer_load_b32(rw, OFF_EPOCH, 0, AUX_SC1) + 1u;
    }
    STAMP(1);
    if (lane == 0) *(LAS unsigned*)(L + L_TAG) = tag;
    {
      const int qo = OFF_Q + (b * 2048 + kvh * 256) * 8 + lane * 16;
      v4u q0, q1;
      bool ok0 = false, ok1 = false;
      WAIT_UNTIL(T_P, 2);
      SPIN_LOOP {
        NOHOIST();
        if (!ok0) q0 = LD128(qo);
        if (!ok1) q1 = LD128(qo + 1024);
        ok0 = tagged(q0, tag); ok1 = tagged(q1, tag);
        if (__all(ok0 & ok1)) break;
      }
      *(LAS v2u*)(L + L_QF + lane * 8) = v2u{q0[0], q0[2]};
      *(LAS v2u*)(L + L_QF + 512 + lane * 8) = v2u{q1[0], q1[2]};
    }
    STAMP(2);
    barrier();
    return;
  }
  const int nn = 12 - nc - nbw, sn = sp - nc - nbw;
  const int trest = 2048 - nc * TL_C - nbw * TL_B - 4 * TL_Q, tln = b ? trest >> 2 : trest / 5, tlx = trest - nn * tln;
  const int ntile = role == 1 ? TL_C : role == 2 ? TL_B : qrole ? TL_Q : tln + (sn < tlx ? 1 : 0);
  const int tile0 = role == 1 ? TL_C * sp : role == 2 ? nc * TL_C + TL_B * (sp - nc) : qrole ? 2048 - TL_Q * (16 - sp) : nc * TL_C + nbw * TL_B + tln * sn + (sn < tlx ? sn : tlx);
  const int nstep = (ntile - w + 3) >> 2;
  const size_t pbase = (size_t)(b * HKV + kvh) * SK * DH;
  const __amdgpu_buffer_rsrc_t rk = __builtin_amdgcn_make_buffer_rsrc((void*)(kc + pbase), 0, SK * DH * 2, 0x00020000);
  const __amdgpu_buffer_rsrc_t rv = __builtin_amdgcn_make_buffer_rsrc((void*)(vc + pbase), 0, SK * DH * 2, 0x00020000);
  const int koff = c * 256 + g * 16, voff = g * 1024 + c * 16;
  const unsigned sbase = (unsigned)(tile0 + w) * 4096u;
#define ISSUEK(d, so) do { _Pragma("unroll") for (int s = 0; s < 4; ++s) kb[d][s] = __builtin_amdgcn_raw_buffer_load_b128(rk, koff + s * 64, (so), AUX_NT); __builtin_amdgcn_sched_barrier(0); } while (0)
#define ISSUEV(d, so) do { _Pragma("unroll") for (int s = 0; s < 4; ++s) vb[d][s] = __builtin_amdgcn_raw_buffer_load_b128(rv, voff + s * 256, (so), AUX_NT); __builtin_amdgcn_sched_barrier(0); } while (0)
#define VMWAIT(n) do { asm volatile("s_waitcnt vmcnt(%0)" :: "i"(n) : "memory"); __builtin_amdgcn_sched_barrier(0); } while (0)
  if (qrole) {
    const int r8 = lane >> 3;
    const u16* p = wq + (size_t)(128 * w + r8) * (HQ * DH) + qi * 64;
    const int ch0 = ((lane & 7) ^ (r8 & 2)) * 8;
#pragma unroll 1
    for (int t = 0; t < 16; t += 2) {
      glds16(p + ch0, lds0 + L_W + w * WREG + t * 1024);
      glds16(p + 8 * (HQ * DH) + (ch0 ^ 32), lds0 + L_W + w * WREG + t * 1024 + 1024);
      p += 16 * (HQ * DH);
    }
    STAMP(7);
  }
  STAMP(11);
  ISSUEK(0, sbase); ISSUEV(0, sbase);
  if (qrole) {
    barrier();
    const int sw = (q4 & 2) | ((g & 1) << 2);
    f32x4 qa[4];
#pragma unroll
    for (int t = 0; t < 4; ++t) qa[t] = f32x4{0.f, 0.f, 0.f, 0.f};
    VMWAIT(8);
    STAMP(8);
#pragma unroll 1
    for (int ks = 0; ks < 4; ++ks) {
      const v4u xb = *(LAS v4u*)(S + L_XN + (c & 1) * 1024 + (128 * w + 32 * ks + 8 * g) * 2);
#pragma unroll
      for (int t = 0; t < 4; ++t) {
        LAS unsigned char* a = L + L_W + w * WREG + ks * 4096 + g * 1024 + q4 * 128 + (p4 & 1) * 8 + (((2 * t + (p4 >> 1)) ^ sw) * 16);
        qa[t] = mfma32(tr_frag(a, a + 512), xb, qa[t]);
      }
    }
    if (c < 2) {
#pragma unroll
      for (int t = 0; t < 4; ++t) *(LAS f32x4*)(S + L_RED + w * 512 + c * 256 + t * 64 + g * 16) = qa[t];
    }
    STAMP(1);
    barrier();
  }
  const bool odd = (nstep - NPRE) & 1;
#pragma unroll
  for (int d = 1; d < NPRE; ++d) { VMWAIT(PACE_N); ISSUEK(d, sbase + d * 16384u); VMWAIT(PACE_N); ISSUEV(d, sbase + d * 16384u); }
  if (odd) { VMWAIT(PACE_N); ISSUEK(NPRE, sbase + NPRE * 16384u); VMWAIT(PACE_N); ISSUEV(NPRE, sbase + NPRE * 16384u); }
  STAMP(10);
  barrier();
  STAMP(2);
#pragma unroll
  for (int s = 0; s < 4; ++s) qf[s] = *(LAS v4u*)(L + L_QF + (c >> 2) * 256 + s * 64 + g * 16);
  tag = __builtin_amdgcn_readfirstlane(*(LAS unsigned*)(L + L_TAG));

  unsigned so = sbase + (odd ? NPRE + 1 : NPRE) * 16384u;
  const unsigned send = sbase + (unsigned)nstep * 16384u;
  QK(0); ISSUEK(0, so); PV(0); ISSUEV(0, so);
  QK(1); ISSUEK(1, so + 16384u); PV(1); ISSUEV(1, so + 16384u);
  so += 32768u;
#pragma unroll
  for (int d = 2; d < NPRE; ++d) { QK(d); PV(d); }
  if (odd) { QK(NPRE); PV(NPRE); }
  asm volatile("v_mbcnt_lo_u32_b32 %0, -1, 0\n\tv_mbcnt_hi_u32_b32 %0, -1, %0" : "=v"(lane));
  c = lane & 15; g = lane >> 4;
  const int ffo = (128 * w + (lane >> 2)) * F + ci * 32 + (((lane & 3) ^ ((lane >> 4) & 2)) * 8);
  const u16* wsrc = role == 1 ? wgate + ffo : wo + (size_t)(bi * 64 + g) * D + 128 * w + 8 * c;
  int wstr = role == 1 ? 16 * F : 4 * D;
  int wp = 0;
  const int wn = (ABL & 2) ? 0 : role == 1 ? 24 : role == 2 ? 16 : 0;
#define WPIECE() do { if (wp < wn) { glds16(wsrc, lds0 + L_W + w * WREG + wp * 1024); ++wp; wsrc += wstr; \
      if (role == 1 && wp == 8) wsrc = wup + ffo; \
      if (role == 1 && wp == 16) { wsrc = wdown + (size_t)(ci * 32 + g) * D + 128 * w + 8 * c; wstr = 4 * D; } } } while (0)
#pragma unroll 1
  for (; so < send; so += 32768u) {
    QK(0); ISSUEK(0, so); WPIECE(); PV(0); ISSUEV(0, so); WPIECE();
    QK(1); ISSUEK(1, so + 16384u); PV(1); ISSUEV(1, so + 16384u); WPIECE();
  }
  asm volatile("v_mbcnt_lo_u32_b32 %0, -1, 0\n\tv_mbcnt_hi_u32_b32 %0, -1, %0" : "=v"(lane));
  c = lane & 15; g = lane >> 4; q4 = (lane >> 2) & 3; p4 = lane & 3;
  const int tid = w * 64 + lane;
#define PIN(x) asm volatile("" : "+v"(x))
  const unsigned tagh = (tag * 0x9E3779B1u) ^ ((tag * 0x9E3779B1u) >> 15);
  const int hd = 2 * (w & 1) + (lane >> 5);
  unsigned mw = lds0 + L_S + L_MRG + ((w * 4 + (c >> 2)) * 132 + 4 + 32 * g + (c & 3)) * 4, mr = lds0 + L_S + L_MRG + hd * 528;
  int pao = OFF_PART + ((b * HQ + kvh * 4 + hd) * NSPLIT + sp) * 512 + (lane & 31) * 16;
  unsigned mr2 = mr + 16 + (lane & 31) * 16;
  PIN(mw); PIN(mr); PIN(mr2); PIN(pao);
  QK(0); PV(0); QK(1); PV(1);
  STAMP(3);

  l = sw16_sum(l, l); l = sw32_sum(l, l);
  {
    LAS float* rec = (LAS float*)mw;
#pragma unroll
    for (int r = 0; r < 4; ++r) { rec[8 * r] = acc0[r]; rec[8 * r + 4] = acc1[r]; }
    if (g == 0 && (c & 3) == 0) *(LAS f32x2*)(rec - 4) = f32x2{m, l};
  }
  barrier();
  if (w < 2) {
    f32x2 ml[4]; f32x4 ov[4];
#pragma unroll
    for (int ww = 0; ww < 4; ++ww) { ml[ww] = *(LAS f32x2*)(mr + ww * 2112); ov[ww] = *(LAS f32x4*)(mr2 + ww * 2112); }
    const float Ml = vmax(vmax(ml[0][0], ml[1][0]), vmax(ml[2][0], ml[3][0])) * LOG2E;
    float wt[4];
#pragma unroll
    for (int ww = 0; ww < 4; ++ww) wt[ww] = __builtin_amdgcn_exp2f(fmaf(ml[ww][0], LOG2E, -Ml));
    const float Ls = fmaf(wt[1], ml[1][1], wt[0] * ml[0][1]) + fmaf(wt[3], ml[3][1], wt[2] * ml[2][1]);
    const f32x4 o = ((wt[0] * ov[0] + wt[1] * ov[1]) + (wt[2] * ov[2] + wt[3] * ov[3])) * __builtin_amdgcn_rcpf(Ls);
    const unsigned lse = f2u(Ml + __builtin_amdgcn_logf(Ls));
    ST128((v4u{cvtpk(o[0], o[1]), __builtin_amdgcn_perm(tagh, lse, 0x05040100u), cvtpk(o[2], o[3]), __builtin_amdgcn_perm(tagh, lse, 0x07060302u)}), pao);
    if (tid == 0) __builtin_amdgcn_raw_buffer_store_b32(tag, rw, OFF_LSE + (kvh * 32 + b * NSPLIT + sp) * 4, 0, AUX_SC1);
  }
  STAMP(4);
  if (ABL & 8) WAIT_UNTIL(4800, 16);
  unsigned dm0 = (c & 7) == 0 ? 0xffffu : (c & 7) == 1 ? 0xffff0000u : 0u, dm1 = (c & 7) == 2 ? 0xffffu : (c & 7) == 3 ? 0xffff0000u : 0u,
           dm2 = (c & 7) == 4 ? 0xffffu : (c & 7) == 5 ? 0xffff0000u : 0u, dm3 = (c & 7) == 6 ? 0xffffu : (c & 7) == 7 ? 0xffff0000u : 0u;
  unsigned tb0 = tagh & 0xffu, tb1 = (tagh >> 8) & 0xffu, tb2 = (tagh >> 16) & 0xffu, tb3 = tagh >> 24;
#define PK24(x, tb) (((f2u(x) + 0x80u) & 0xffffff00u) | (tb))
#define PUBLISH_ACC(acc, off) ST128((v4u{PK24(acc[0], tb0), PK24(acc[1], tb1), PK24(acc[2], tb2), PK24(acc[3], tb3)}), (off))
#define UP24(x) u2f((x) & 0xffffff00u)

  if (role == 2) {
    v4u wr[16];
#pragma unroll
    for (int i = 0; i < 16; ++i) wr[i] = *(LAS v4u*)(L + L_W + w * WREG + i * 1024 + lane * 16);
    const int head = bi >> 1;
    const int po = OFF_PART + (((lane >> 5) * HQ + head) * NSPLIT + ((lane >> 2) & 7)) * 512 + (bi & 1) * 256 + w * 64 + (lane & 3) * 16;
    unsigned ow = lds0 + L_S + L_OB + ((g >> 1) * 4 + (g & 1) + ((lane & 4) >> 1)) * 64 + (4 * w + (lane & 3)) * 4, orr = lds0 + L_S + L_OB + ((c >> 3) * 4 + g) * 64;
    int so_ = OFF_P1 + bi * 4096 + tid * 16;
    f32x4 acc = {0.f, 0.f, 0.f, 0.f};
    PIN(ow); PIN(orr); PIN(so_); PIN(dm0); PIN(dm1); PIN(dm2); PIN(dm3); PIN(tb0); PIN(tb1); PIN(tb2); PIN(tb3); PIN(acc);
    SPIN_LOOP {
      NOHOIST();
      const unsigned e0 = __builtin_amdgcn_raw_buffer_load_b32(rw, OFF_LSE + ((head >> 2) * 32 + (lane & 31)) * 4, 0, AUX_SC1);
      if (__builtin_popcountll(__ballot(e0 != tag)) <= 2 * SPEC_N) break;
      __builtin_amdgcn_s_sleep(4);
    }
    v4u a0, a1;
    poll2<PD1, 16>(ws, po, po + 8 * 512, tagh, a0, a1);
    STAMP(5);
    const float l0 = u2f(__builtin_amdgcn_perm(a0[3], a0[1], 0x05040100u)), l1 = u2f(__builtin_amdgcn_perm(a1[3], a1[1], 0x05040100u));
    float xm = vmax(l0, l1);
    xm = vmax(vmax(xm, dpp<ROR4>(xm)), vmax(dpp<ROR8>(xm), dpp<ROR12>(xm)));
    xm = sw16_max(xm, xm);
    const float w0 = __builtin_amdgcn_exp2f(l0 - xm), w1 = __builtin_amdgcn_exp2f(l1 - xm);
    float u0 = sw16_sum(fmaf(w1, bflo(a1[0]), w0 * bflo(a0[0])), fmaf(w1, bfhi(a1[0]), w0 * bfhi(a0[0])));
    float u1 = sw16_sum(fmaf(w1, bflo(a1[2]), w0 * bflo(a0[2])), fmaf(w1, bfhi(a1[2]), w0 * bfhi(a0[2])));
    float ws_ = w0 + w1;
    ws_ = sw16_sum(ws_, ws_);
    u0 += dpp<ROR8>(u0); u1 += dpp<ROR8>(u1); ws_ += dpp<ROR8>(ws_);
    u0 += dpp<ROR4>(u0); u1 += dpp<ROR4>(u1); ws_ += dpp<ROR4>(ws_);
    const float uo = ((lane & 4) ? u1 : u0) * __builtin_amdgcn_rcpf(ws_);
    *(LAS unsigned*)ow = cvtpk(uo, uo);
    barrier();
    const v4u pv0 = *(LAS v4u*)orr, pv1 = *(LAS v4u*)(orr + 16), pv2 = *(LAS v4u*)(orr + 32), pv3 = *(LAS v4u*)(orr + 48);
    diag_chain_regs(acc, wr, pv0, pv1, dm0, dm1, dm2, dm3);
    diag_chain_regs(acc, wr + 8, pv2, pv3, dm0, dm1, dm2, dm3);
    mfma_drain(acc);
    PUBLISH_ACC(acc, so_);
    STAMP(6);
    poll_done(a0, a1);
  } else if (role == 3) {
    const int rb = rj & 1, col0 = (rj >> 1) * 32;
    unsigned xp = *(const GAS unsigned*)(x + rb * D + col0 + 2 * c);
    asm volatile("" : "+v"(xp));
    const int pp = lane >> 3;
    float h0, h1;
    unsigned rw_ = lds0 + L_S + L_RR + w * 128 + (8 * g + (lane & 7)) * 4, rr_ = lds0 + L_S + L_RR + c * 8;
    int hbo = OFF_HB + ((w & 1) * 4 + g) * 4096 + rb * 2048 + col0 * 4 + c * 8;
    GAS unsigned* op = (GAS unsigned*)(out + rb * D + col0 + 2 * c);
    PIN(rw_); PIN(rr_); PIN(hbo); PIN(op);
    {
      const int po = OFF_P1 + (16 * w + pp) * 4096 + rj * 128 + (lane & 7) * 16;
      v4u a0, a1;
      poll2<PD2, -1>(ws, po, po + 8 * 4096, tagh, a0, a1);
      STAMP(5);
      float u = sw16_sum(sw32_sum(UP24(a0[0]) + UP24(a1[0]), UP24(a0[2]) + UP24(a1[2])), sw32_sum(UP24(a0[1]) + UP24(a1[1]), UP24(a0[3]) + UP24(a1[3])));
      u += dpp<ROR8>(u);
      *(LAS float*)rw_ = u;
      barrier();
      const f32x2 s = (*(LAS f32x2*)rr_ + *(LAS f32x2*)(rr_ + 128)) + (*(LAS f32x2*)(rr_ + 256) + *(LAS f32x2*)(rr_ + 384));
      const unsigned at = cvtpk(s[0], s[1]);
      const unsigned hp = cvtpk(bflo(xp) + bflo(at), bfhi(xp) + bfhi(at));
      h0 = bflo(hp); h1 = bfhi(hp);
      if (w < 2) __builtin_amdgcn_raw_buffer_store_b64(v2u{hp, tag}, rw, hbo, 0, AUX_SC1);
      STAMP(6);
      poll_done(a0, a1);
    }
    {
      const int po = OFF_P2 + (14 * w + pp) * 4096 + rj * 128 + (lane & 7) * 16;
      v4u a0, b1;
      poll2<PD4, -1>(ws, po, po + (pp < 6 ? 8 * 4096 : 0), tagh, a0, b1);
      STAMP(7);
      const unsigned km = pp < 6 ? 0xffffff00u : 0u;
      float u = sw16_sum(sw32_sum(UP24(a0[0]) + u2f(b1[0] & km), UP24(a0[2]) + u2f(b1[2] & km)), sw32_sum(UP24(a0[1]) + u2f(b1[1] & km), UP24(a0[3]) + u2f(b1[3] & km)));
      u += dpp<ROR8>(u);
      *(LAS float*)(rw_ + 512) = u;
      barrier();
      if (tid < 16) {
        const f32x2 s = (*(LAS f32x2*)(rr_ + 512) + *(LAS f32x2*)(rr_ + 640)) + (*(LAS f32x2*)(rr_ + 768) + *(LAS f32x2*)(rr_ + 896));
        const unsigned dn = cvtpk(s[0], s[1]);
        *op = cvtpk(h0 + bflo(dn), h1 + bfhi(dn));
        if (rj == 0 && tid == 0) __builtin_amdgcn_raw_buffer_store_b32(tag, rw, OFF_EPOCH, 0, AUX_SC1);
      }
      STAMP(8);
      poll_done(a0, b1);
    }
  } else if (role == 1) {
    v4u fg[8], fu[8], wd[8];
#pragma unroll
    for (int ks = 0; ks < 4; ++ks)
#pragma unroll
      for (int t = 0; t < 2; ++t) {
        LAS unsigned char* a = L + L_W + w * WREG + (32 * ks + 8 * g + q4) * 64 + (((2 * t + (p4 >> 1)) ^ ((g & 1) << 1)) * 16) + (p4 & 1) * 8;
        fg[ks * 2 + t] = tr_frag(a, a + 256); fu[ks * 2 + t] = tr_frag(a + 8192, a + 8192 + 256);
      }
#pragma unroll
    for (int i = 0; i < 8; ++i) wd[i] = *(LAS v4u*)(L + L_W + w * WREG + 16384 + i * 1024 + lane * 16);
    f32x4 ag[2], au[2];
    unsigned ro = lds0 + L_S + L_RED + ((c >> 2) & 1) * 1024 + ((c & 1) * 32 + (c & 2) * 8 + 4 * g) * 16 + w * 4;
    unsigned fo = lds0 + L_S + L_RED + ((c >> 3) * 32 + 4 * (c & 3) + g) * 16;
#define GU_MFMA(XB) do { _Pragma("unroll") for (int t = 0; t < 2; ++t) { ag[t] = f32x4{0.f, 0.f, 0.f, 0.f}; au[t] = f32x4{0.f, 0.f, 0.f, 0.f}; } \
      _Pragma("unroll") for (int ks = 0; ks < 4; ++ks) { \
        _Pragma("unroll") for (int t = 0; t < 2; ++t) { ag[t] = mfma32(fg[ks * 2 + t], XB, ag[t]); au[t] = mfma32(fu[ks * 2 + t], XB, au[t]); } } } while (0)
#define GU_STORE() do { const f32x4 sel = (c & 4) ? ((c & 2) ? au[1] : au[0]) : ((c & 2) ? ag[1] : ag[0]); \
      _Pragma("unroll") for (int i = 0; i < 4; ++i) *(LAS float*)(ro + i * 16) = sel[i]; } while (0)
#define SUM4(p_) ({ const f32x2 t_ = f32x2{p_[0], p_[1]} + f32x2{p_[2], p_[3]}; float r_ = t_[0] + t_[1]; PIN(r_); r_; })
    GU_MFMA((v4u{0x3f803f80u, 0x3f803f80u, 0x3f803f80u, 0x3f803f80u}));
    GU_STORE();
    barrier();
    float cg0, cg1, cu0, cu1;
    { const f32x4 p0 = *(LAS f32x4*)fo, p1 = *(LAS f32x4*)(fo + 256), p2 = *(LAS f32x4*)(fo + 1024), p3 = *(LAS f32x4*)(fo + 1280); cg0 = SUM4(p0); cg1 = SUM4(p1); cu0 = SUM4(p2); cu1 = SUM4(p3); }
    unsigned xw = lds0 + L_S + L_XN + (w & 1) * 1024 + lane * 16, xr = lds0 + L_S + L_XN + (c & 1) * 1024 + (128 * w + 8 * g) * 2, sw_ = lds0 + L_S + L_ST + w * 8, sr = lds0 + L_S + L_ST + (c >> 3) * 8;
    int so_ = OFF_P2 + ci * 4096 + tid * 16;
    f32x4 acc = {0.f, 0.f, 0.f, 0.f};
    PIN(ro); PIN(fo); PIN(xw); PIN(xr); PIN(sw_); PIN(sr); PIN(so_); PIN(dm0); PIN(dm1); PIN(dm2); PIN(dm3); PIN(tb0); PIN(tb1); PIN(tb2); PIN(tb3); PIN(acc);
    v4u ha = {0u, 0u, 0u, 0u}, hb = ha;
    if (w < 2) {
      const int ho = OFF_HB + xcd * 4096 + w * 2048 + lane * 32;
      poll2<PD3, 0>(ws, ho, ho + 16, tag, ha, hb);
      STAMP(5);
      *(LAS v4u*)xw = v4u{ha[0], ha[2], hb[0], hb[2]};
    }
    barrier();
    v4u xb[4];
#pragma unroll
    for (int ks = 0; ks < 4; ++ks) xb[ks] = *(LAS v4u*)(xr + 64 * ks);
    __builtin_amdgcn_sched_barrier(0);
    const float pv = bflo(__builtin_amdgcn_readfirstlane(ha[0]));
    const float d0 = bflo(ha[0]) - pv, d1 = bfhi(ha[0]) - pv, d2 = bflo(ha[2]) - pv, d3 = bfhi(ha[2]) - pv, d4 = bflo(hb[0]) - pv, d5 = bfhi(hb[0]) - pv, d6 = bflo(hb[2]) - pv, d7 = bfhi(hb[2]) - pv;
    float s1 = ((d0 + d1) + (d2 + d3)) + ((d4 + d5) + (d6 + d7));
    float s2 = (fmaf(d1, d1, d0 * d0) + fmaf(d3, d3, d2 * d2)) + (fmaf(d5, d5, d4 * d4) + fmaf(d7, d7, d6 * d6));
    __builtin_amdgcn_sched_barrier(0);
    GU_MFMA(xb[ks]);
    {
      float t = sw32_sum(s1, s2);
      t = sw16_sum(t, t); t += dpp<ROR8>(t); t += dpp<ROR4>(t); t += dpp<XOR2>(t); t += dpp<XOR1>(t);
      const float md = u2f(__builtin_amdgcn_readlane(f2u(t), 0)) * (1.0f / D), q2 = u2f(__builtin_amdgcn_readlane(f2u(t), 32)) * (1.0f / D);
      const float var = vmax(fmaf(-md, md, q2), 0.f);
      const float rs = __builtin_amdgcn_rcpf(rbf(__builtin_amdgcn_sqrtf(rbf(rbf(var) + 1e-5f))));
      *(LAS f32x2*)sw_ = f32x2{rbf(pv + md), rs};
    }
    GU_STORE();
    barrier();
    const f32x2 st = *(LAS f32x2*)sr;
    const f32x4 p0 = *(LAS f32x4*)fo, p1 = *(LAS f32x4*)(fo + 256), p2 = *(LAS f32x4*)(fo + 1024), p3 = *(LAS f32x4*)(fo + 1280);
    __builtin_amdgcn_sched_barrier(0);
    const float G0 = SUM4(p0), G1 = SUM4(p1), U0 = SUM4(p2), U1 = SUM4(p3);
    const unsigned gp = cvtpk(fmaf(-st[0], cg0, G0) * st[1], fmaf(-st[0], cg1, G1) * st[1]);
    const unsigned up = cvtpk(fmaf(-st[0], cu0, U0) * st[1], fmaf(-st[0], cu1, U1) * st[1]);
    const float g0 = bflo(gp), g1 = bfhi(gp);
    const unsigned sg = cvtpk(g0 * __builtin_amdgcn_rcpf(1.0f + __builtin_amdgcn_exp2f(-g0 * LOG2E)), g1 * __builtin_amdgcn_rcpf(1.0f + __builtin_amdgcn_exp2f(-g1 * LOG2E)));
    const float av0 = bflo(sg) * bflo(up), av1 = bfhi(sg) * bfhi(up);
    diag_chain_quad(acc, wd, cvtpk(av0, av0), cvtpk(av1, av1), dm0, dm1, dm2, dm3);
    mfma_drain(acc);
    PUBLISH_ACC(acc, so_);
    STAMP(6);
    poll_done(ha, hb);
  }
  DUMP_STAMPS();
}

extern "C" void kernel_launch(void* const* d_in, const int* in_sizes, int n_in, void* d_out, int out_size, void* d_ws, size_t ws_size, hipStream_t stream) {
  fused_block<<<NWG, NTHR, 0, stream>>>((const u16*)d_in[0], (const u16*)d_in[1], (const u16*)d_in[2], (const u16*)d_in[3], (unsigned char*)d_ws,
                                        (const u16*)d_in[4], (const u16*)d_in[6], (const u16*)d_in[7], (const u16*)d_in[8], (u16*)d_out TUNE_ARG);
}
```

```cpp
#pragma clang diagnostic ignored "-Wint-to-pointer-cast"
#include <hip/hip_runtime.h>
#include <hip/hip_bf16.h>
#include <cstdint>

typedef unsigned short u16;
typedef unsigned v4u __attribute__((ext_vector_type(4)));
typedef unsigned v2u __attribute__((ext_vector_type(2)));
typedef float f32x4 __attribute__((ext_vector_type(4)));
typedef float f32x2 __attribute__((ext_vector_type(2)));
typedef short s16x4 __attribute__((ext_vector_type(4)));
typedef __bf16 bf16x8_t __attribute__((ext_vector_type(8)));
typedef __bf16 bf16x2_t __attribute__((ext_vector_type(2)));
#define LAS __attribute__((address_space(3)))
#define GAS __attribute__((address_space(1)))

constexpr int D = 512, HQ = 32, HKV = 8, SK = 32768, DH = 128, F = 1792;
constexpr int NWG = 256, NTHR = 320, NSPLIT = 16;
struct Plan { unsigned e[32]; };
#define TUNE_PARAM
#define TUNE_ARG
#define ABL 0
#define TL_C 121
#define TL_CS 116
#define TL_B 129
#define TL_Q 128
#define TL_QS 124
#define T_P 500
static Plan make_plan() {
  Plan p;
  for (int b = 0; b < 2; ++b) {
    const int nc = 4 - b, nb = 3 + 2 * b;
    int n[16], role[16], ix[16], used = 0, nn = 0;
    for (int sp = 0; sp < 16; ++sp) {
      if (sp < nc) { role[sp] = 1; ix[sp] = 2 * sp + b; n[sp] = sp == 0 ? TL_CS : TL_C; }
      else if (sp < nc + nb) { role[sp] = 2; ix[sp] = 3 * b + sp - nc; n[sp] = TL_B; }
      else if (sp >= 12) { role[sp] = 4; ix[sp] = 2 * (sp - 12) + b; n[sp] = ix[sp] == 3 ? TL_QS : TL_Q; }
      else { role[sp] = (sp == 8 || sp == 9) ? 3 : 0; ix[sp] = role[sp] ? 2 * (sp - 8) + b : 0; n[sp] = 0; ++nn; }
      used += n[sp];
    }
    int rest = 2048 - used, t0 = 0;
    for (int sp = 0; sp < 16; ++sp) {
      if (n[sp] == 0) { n[sp] = (rest + nn - 1) / nn; rest -= n[sp]; --nn; }
      p.e[2 * sp + b] = (unsigned)t0 | ((unsigned)n[sp] << 11) | ((unsigned)role[sp] << 19) | ((unsigned)ix[sp] << 22);
      t0 += n[sp];
    }
  }
  return p;
}
#define PD1 2
#define PD2 3
#define PD3 1
#define PD4 3
#define PSL 1
#define PACE_N 4
#define WMODE 1
#define CODE_BYTES 24576
constexpr int NB = 64, NC = 56, NR = 64, NREP = 8;
constexpr int OFF_EPOCH = 0;
constexpr int OFF_HB = 0x190000;
constexpr int OFF_PART = 0x10000;
constexpr int OFF_P1 = 0x90000;
constexpr int OFF_P2 = 0xD0000;
constexpr int OFF_Q = 0x180000;
constexpr int WS_BYTES = 0x1A0000, OFF_DBG = 0x200000;
constexpr int AUX_NT = 2, AUX_SC1 = 16;
constexpr unsigned SPIN_CAP = 1u << 16;
constexpr int L_W = 0;
constexpr int WREG = 24576;
constexpr int L_QF = 4 * WREG;
constexpr int L_SC = L_QF + 1024;
constexpr int L_TAG = L_SC + 4 * 40 * 128;
constexpr int L_S = L_TAG + 16;
constexpr int L_XN = 0;
constexpr int L_RED = L_XN + 2048;
constexpr int L_MRG = L_RED + 2048;
constexpr int L_OB = L_MRG + 4 * 4 * 132 * 4;
constexpr int L_RR = L_OB + 512;
constexpr int L_ST = L_RR + 1024;
constexpr int S_BYTES = L_ST + 32;
constexpr int L_PF = L_S + S_BYTES;
constexpr int L_DBG = L_PF + 256;
constexpr int L_BYTES = L_DBG + 128;

__device__ __forceinline__ float bflo(unsigned v) { return __uint_as_float(v << 16); }
__device__ __forceinline__ float bfhi(unsigned v) { return __uint_as_float(v & 0xffff0000u); }
__device__ __forceinline__ unsigned cvtpk(float lo, float hi) { return __builtin_bit_cast(unsigned, __builtin_convertvector(f32x2{lo, hi}, bf16x2_t)); }
__device__ __forceinline__ float rbf(float x) { return bflo(cvtpk(x, x)); }
__device__ __forceinline__ bool tagged(v4u v, unsigned tag) { return (v[1] == tag) & (v[3] == tag); }
__device__ __forceinline__ float u2f(unsigned v) { return __uint_as_float(v); }
__device__ __forceinline__ unsigned f2u(float v) { return __float_as_uint(v); }

__device__ __forceinline__ float vmax(float a, float b) { return __builtin_elementwise_maximum(a, b); }
template <int CTRL> __device__ __forceinline__ float dpp(float x) { return __builtin_bit_cast(float, __builtin_amdgcn_mov_dpp(__builtin_bit_cast(int, x), CTRL, 0xf, 0xf, true)); }
template <int CTRL> __device__ __forceinline__ unsigned dppu(unsigned x) { return (unsigned)__builtin_amdgcn_mov_dpp((int)x, CTRL, 0xf, 0xf, true); }
constexpr int XOR1 = 0xB1, XOR2 = 0x4E, ROR8 = 0x128, ROR4 = 0x124, ROR12 = 0x12C;
__device__ __forceinline__ float sw32_sum(float a, float b) { auto r = __builtin_amdgcn_permlane32_swap(f2u(a), f2u(b), false, false); return u2f(r[0]) + u2f(r[1]); }
__device__ __forceinline__ float sw16_sum(float a, float b) { auto r = __builtin_amdgcn_permlane16_swap(f2u(a), f2u(b), false, false); return u2f(r[0]) + u2f(r[1]); }
__device__ __forceinline__ float sw32_max(float a, float b) { auto r = __builtin_amdgcn_permlane32_swap(f2u(a), f2u(b), false, false); return vmax(u2f(r[0]), u2f(r[1])); }
__device__ __forceinline__ float sw16_max(float a, float b) { auto r = __builtin_amdgcn_permlane16_swap(f2u(a), f2u(b), false, false); return vmax(u2f(r[0]), u2f(r[1])); }
__device__ __forceinline__ float wave_sum(float x) {
  x += dpp<XOR1>(x); x += dpp<XOR2>(x); x += dpp<ROR4>(x); x += dpp<ROR8>(x);
  x = sw16_sum(x, x); return sw32_sum(x, x);
}
__device__ __forceinline__ f32x4 mfma32(v4u a, v4u b, f32x4 c) { return __builtin_amdgcn_mfma_f32_16x16x32_bf16(__builtin_bit_cast(bf16x8_t, a), __builtin_bit_cast(bf16x8_t, b), c, 0, 0, 0); }
__device__ __forceinline__ f32x4 mfma16(v2u a, v2u b, f32x4 c) { return __builtin_amdgcn_mfma_f32_16x16x16bf16_1k(__builtin_bit_cast(s16x4, a), __builtin_bit_cast(s16x4, b), c, 0, 0, 0); }
__device__ __forceinline__ void barrier() { asm volatile("s_waitcnt lgkmcnt(0)\n\ts_barrier" ::: "memory"); }
__device__ __forceinline__ v4u tr_frag(LAS unsigned char* lo, LAS unsigned char* hi) {
  const v2u a = __builtin_bit_cast(v2u, __builtin_amdgcn_ds_read_tr16_b64_v4i16((LAS s16x4*)lo));
  const v2u b = __builtin_bit_cast(v2u, __builtin_amdgcn_ds_read_tr16_b64_v4i16((LAS s16x4*)hi));
  return v4u{a[0], a[1], b[0], b[1]};
}

#define AND4(T0, T1, T2, T3, SRC, MOD, SFX) "v_and_b32" SFX " " T0 ", " SRC ", %[m0]" MOD "\n\tv_and_b32" SFX " " T1 ", " SRC ", %[m1]" MOD "\n\tv_and_b32" SFX " " T2 ", " SRC ", %[m2]" MOD "\n\tv_and_b32" SFX " " T3 ", " SRC ", %[m3]" MOD "\n\t"
#define ANDA(SRC, MOD, SFX) AND4("v240", "v241", "v242", "v243", SRC, MOD, SFX)
#define ANDB(SRC, MOD, SFX) AND4("v244", "v245", "v246", "v247", SRC, MOD, SFX)
#define MFA(W) "v_mfma_f32_16x16x32_bf16 %[acc], " W ", v[240:243], %[acc]\n\t"
#define MFB(W) "v_mfma_f32_16x16x32_bf16 %[acc], " W ", v[244:247], %[acc]\n\t"
#define CHAIN_CLOB "v240", "v241", "v242", "v243", "v244", "v245", "v246", "v247"
#define CHAIN_W(W) [w0] "v"(W[0]), [w1] "v"(W[1]), [w2] "v"(W[2]), [w3] "v"(W[3]), [w4] "v"(W[4]), [w5] "v"(W[5]), [w6] "v"(W[6]), [w7] "v"(W[7])
__device__ __forceinline__ void diag_chain_regs(f32x4& acc, const v4u* W, v4u pa, v4u pb, unsigned m0, unsigned m1, unsigned m2, unsigned m3) {
  asm volatile(ANDA("%[p0]", "", "") ANDB("%[p1]", "", "") MFA("%[w0]") ANDA("%[p2]", "", "") MFB("%[w1]") ANDB("%[p3]", "", "") MFA("%[w2]") ANDA("%[p4]", "", "") MFB("%[w3]")
               ANDB("%[p5]", "", "") MFA("%[w4]") ANDA("%[p6]", "", "") MFB("%[w5]") ANDB("%[p7]", "", "") MFA("%[w6]") "s_nop 0\n\t" MFB("%[w7]")
               : [acc] "+v"(acc)
               : CHAIN_W(W), [p0] "v"(pa[0]), [p1] "v"(pa[1]), [p2] "v"(pa[2]), [p3] "v"(pa[3]), [p4] "v"(pb[0]), [p5] "v"(pb[1]), [p6] "v"(pb[2]), [p7] "v"(pb[3]),
                 [m0] "v"(m0), [m1] "v"(m1), [m2] "v"(m2), [m3] "v"(m3)
               : CHAIN_CLOB);
}
#define QP(t) " quad_perm:[" #t "," #t "," #t "," #t "] row_mask:0xf bank_mask:0xf"
__device__ __forceinline__ void diag_chain_quad(f32x4& acc, const v4u* W, unsigned pa, unsigned pb, unsigned m0, unsigned m1, unsigned m2, unsigned m3) {
  asm volatile("s_nop 1\n\t" ANDA("%[pa]", QP(0), "_dpp") ANDB("%[pa]", QP(1), "_dpp") MFA("%[w0]") ANDA("%[pa]", QP(2), "_dpp") MFB("%[w1]") ANDB("%[pa]", QP(3), "_dpp") MFA("%[w2]")
               ANDA("%[pb]", QP(0), "_dpp") MFB("%[w3]") ANDB("%[pb]", QP(1), "_dpp") MFA("%[w4]") ANDA("%[pb]", QP(2), "_dpp") MFB("%[w5]") ANDB("%[pb]", QP(3), "_dpp") MFA("%[w6]")
               "s_nop 0\n\t" MFB("%[w7]")
               : [acc] "+v"(acc)
               : CHAIN_W(W), [pa] "v"(pa), [pb] "v"(pb), [m0] "v"(m0), [m1] "v"(m1), [m2] "v"(m2), [m3] "v"(m3)
               : CHAIN_CLOB);
}
__device__ __forceinline__ void mfma_drain(f32x4& acc) { asm volatile("s_nop 7" : "+v"(acc)); }

__device__ __forceinline__ v4u ln_row(v4u xv) {
  float x[8];
#pragma unroll
  for (int i = 0; i < 4; ++i) { x[2 * i] = bflo(xv[i]); x[2 * i + 1] = bfhi(xv[i]); }
  const float mean = wave_sum(((x[0] + x[1]) + (x[2] + x[3])) + ((x[4] + x[5]) + (x[6] + x[7]))) * (1.0f / D);
  float vs = 0.f;
#pragma unroll
  for (int i = 0; i < 8; ++i) { const float d = x[i] - mean; vs = fmaf(d, d, vs); }
  const float var = wave_sum(vs) * (1.0f / D);
  const float mb = rbf(mean);
  const float rs = __builtin_amdgcn_rcpf(rbf(__builtin_amdgcn_sqrtf(rbf(rbf(var) + 1e-5f))));
  v4u r;
#pragma unroll
  for (int i = 0; i < 4; ++i) { const unsigned d = cvtpk(x[2 * i] - mb, x[2 * i + 1] - mb); r[i] = cvtpk(bflo(d) * rs, bfhi(d) * rs); }
  return r;
}
__device__ __forceinline__ void glds16(const u16* gsrc, unsigned lds_dst) {
  unsigned keep;
  asm volatile("s_mov_b32 %0, m0\n\ts_mov_b32 m0, %2\n\ts_nop 0\n\tglobal_load_lds_dwordx4 %1, off nt\n\ts_mov_b32 m0, %0" : "=&s"(keep) : "v"(gsrc), "s"(lds_dst) : "memory");
}

__device__ __forceinline__ unsigned lowbytes(v4u v) { return __builtin_amdgcn_perm(v[1], v[0], 0x0c0c0400u) | __builtin_amdgcn_perm(v[3], v[2], 0x04000c0cu); }
template <int PD_, int TSH>
__device__ __forceinline__ void poll2(const unsigned char* ws, int o0, int o1, unsigned tag, v4u& r0, v4u& r1) {
  r0 = v4u{~tag, ~tag, ~tag, ~tag}; r1 = r0;
#define PASS() asm volatile("global_load_dwordx4 %0, %2, %4 sc1\n\tglobal_load_dwordx4 %1, %3, %4 sc1" : "+v"(r0), "+v"(r1) : "v"(o0), "v"(o1), "s"(ws) : "memory")
#pragma unroll
  for (int k = 0; k < PD_ - 1; ++k) { PASS(); __builtin_amdgcn_s_sleep(PSL); }
#pragma unroll 1
  for (unsigned k = 0; k < SPIN_CAP; ++k) {
    PASS();
    asm volatile("s_waitcnt vmcnt(%2)" : "+v"(r0), "+v"(r1) : "i"((PD_ - 1) * 2) : "memory");
    if (TSH == 16) { if (__all((__builtin_amdgcn_perm(r0[3], r0[1], 0x07060302u) == tag) & (__builtin_amdgcn_perm(r1[3], r1[1], 0x07060302u) == tag))) break; }
    else if (TSH < 0) { if (__all((lowbytes(r0) == tag) & (lowbytes(r1) == tag))) break; }
    else if (__all((((r0[1] ^ tag) | (r0[3] ^ tag) | (r1[1] ^ tag) | (r1[3] ^ tag)) >> (TSH < 0 ? 0 : TSH)) == 0u)) break;
    if (PD_ > 1) __builtin_amdgcn_s_sleep(PSL);
  }
#undef PASS
  asm volatile("s_nop 3 ; POLL_END" : "+v"(r0), "+v"(r1));
}
__device__ __forceinline__ void poll_done(v4u& r0, v4u& r1) { asm volatile("s_waitcnt vmcnt(0)" : "+v"(r0), "+v"(r1) :: "memory"); }

#define STAMP(i) do { } while (0)
#define DUMP_STAMPS() do { } while (0)
#define LD128(off) __builtin_amdgcn_raw_buffer_load_b128(rw, (off), 0, AUX_SC1)
#define ST128(v, off) __builtin_amdgcn_raw_buffer_store_b128((v), rw, (off), 0, AUX_SC1)
#define SPIN_LOOP _Pragma("unroll 1") for (unsigned spins = 0; spins < SPIN_CAP; ++spins)
#define NOHOIST() asm volatile("" ::: "memory")

__global__ void __launch_bounds__(NTHR, 2) fused_block(const u16* x, const u16* kc, const u16* vc, const u16* wq, unsigned char* ws,
                                                       const u16* wo, const u16* wgate, const u16* wup, const u16* wdown, u16* out, Plan plan TUNE_PARAM) {
  __shared__ __attribute__((aligned(16))) unsigned char smem[L_BYTES];
  LAS unsigned char* L = (LAS unsigned char*)smem;
  STAMP(0);
  const unsigned tstart = (unsigned)__builtin_amdgcn_s_memrealtime();
  const int wg = blockIdx.x, xcd = wg & 7, idx = wg >> 3;
  const int kvh = xcd, b = idx & 1, sp = idx >> 1;
  const unsigned pe = ((const __attribute__((address_space(4))) unsigned*)__builtin_amdgcn_kernarg_segment_ptr())[20 + idx];
  int lane = threadIdx.x & 63;
  const int w = __builtin_amdgcn_readfirstlane(threadIdx.x >> 6);
  int c = lane & 15, g = lane >> 4;
  if (w == 4 && !(ABL & 32)) {
    unsigned long long pc;
    asm volatile("s_getpc_b64 %0" : "=s"(pc));
    if ((lane * 32 + idx) * 128 < CODE_BYTES) {
      unsigned keep;
      asm volatile("s_mov_b32 %0, m0\n\ts_mov_b32 m0, %3\n\ts_nop 0\n\tglobal_load_lds_dword %1, %2\n\ts_mov_b32 m0, %0"
                   : "=&s"(keep) : "v"((lane * 32 + idx) * 128), "s"(pc & ~255ull), "s"((unsigned)(uintptr_t)L + L_PF) : "memory");
    }
  }
  asm volatile("" : "+s"(x), "+s"(kc), "+s"(vc), "+s"(wq), "+s"(ws), "+s"(wo), "+s"(wgate), "+s"(wup), "+s"(wdown), "+s"(out));
  const int role = (pe >> 19) & 7, rix = pe >> 22;
  const bool qrole = role == 4;
  const int cn = xcd * 5 + rix - (xcd < 7 ? 2 : 0);
  const int ci = (xcd < 7 && rix < 2) ? 8 * xcd + 6 + rix : cn + 2 * ((cn * 43) >> 8);
  const int bi = xcd * 8 + rix, rj = xcd * 4 + rix, qi = bi;
  const __amdgpu_buffer_rsrc_t rw = __builtin_amdgcn_make_buffer_rsrc((void*)ws, 0, WS_BYTES, 0x00020000);
  const unsigned lds0 = (unsigned)(uintptr_t)L;
  LAS unsigned char* S = L + L_S;
#define WAIT_UNTIL(t, slp) do { while ((int)((unsigned)__builtin_amdgcn_s_memrealtime() - (tstart + (unsigned)(t))) < 0) __builtin_amdgcn_s_sleep(slp); } while (0)
  int q4 = (lane >> 2) & 3, p4 = lane & 3;
  constexpr float SCALE = 0.088388347648318440f, LOG2E = 1.4426950408889634f;
  unsigned tag;

  if (w == 4) {
    __builtin_amdgcn_s_setprio(3);
    if (qrole) {
      const v4u x0 = *(const GAS v4u*)(x + lane * 8), x1 = *(const GAS v4u*)(x + D + lane * 8);
      tag = __builtin_amdgcn_raw_buffer_load_b32(rw, OFF_EPOCH, 0, AUX_SC1) + 1u;
      const v4u n0 = ln_row(x0), n1 = ln_row(x1);
      *(LAS v4u*)(S + L_XN + lane * 16) = n0; *(LAS v4u*)(S + L_XN + 1024 + lane * 16) = n1;
      STAMP(9);
      barrier();
      barrier();
      f32x2 s = {0.f, 0.f};
#pragma unroll
      for (int ww = 0; ww < 4; ++ww) s += *(LAS f32x2*)(S + L_RED + ww * 512 + lane * 8);
      __builtin_amdgcn_raw_buffer_store_b64(v2u{cvtpk(s[0], s[1]), tag}, rw, OFF_Q + ((lane >> 5) * 2048 + qi * 32 + (lane & 31)) * 8, 0, AUX_SC1);
    } else {
      tag = __builtin_amdgcn_raw_buffer_load_b32(rw, OFF_EPOCH, 0, AUX_SC1) + 1u;
    }
    STAMP(1);
    if (lane == 0) *(LAS unsigned*)(L + L_TAG) = tag;
    {
      const int qo = OFF_Q + (b * 2048 + kvh * 256) * 8 + lane * 16;
      v4u q0, q1;
      bool ok0 = false, ok1 = false;
      WAIT_UNTIL(T_P, 2);
      SPIN_LOOP {
        NOHOIST();
        if (!ok0) q0 = LD128(qo);
        if (!ok1) q1 = LD128(qo + 1024);
        ok0 = tagged(q0, tag); ok1 = tagged(q1, tag);
        if (__all(ok0 & ok1)) break;
      }
      *(LAS v2u*)(L + L_QF + lane * 8) = v2u{q0[0], q0[2]};
      *(LAS v2u*)(L + L_QF + 512 + lane * 8) = v2u{q1[0], q1[2]};
    }
    STAMP(2);
    barrier();
    return;
  }
  const int tile0 = pe & 2047, ntile = (pe >> 11) & 255;
  const int nstep = (ntile - w + 3) >> 2;
  const size_t pbase = (size_t)(b * HKV + kvh) * SK * DH;
  const __amdgpu_buffer_rsrc_t rk = __builtin_amdgcn_make_buffer_rsrc((void*)(kc + pbase), 0, SK * DH * 2, 0x00020000);
  const __amdgpu_buffer_rsrc_t rv = __builtin_amdgcn_make_buffer_rsrc((void*)(vc + pbase), 0, SK * DH * 2, 0x00020000);
  const int koff1 = c * 256 + g * 16, koff2 = (c >> 2) * 1024 + g * 64 + (c & 3) * 16;
  const unsigned sbase = (unsigned)(tile0 + w) * 4096u;
  v4u kb[15][4];
#define SB() __builtin_amdgcn_sched_barrier(0)
#define ISSUEK(d, j) do { _Pragma("unroll") for (int s = 0; s < 4; ++s) \
      kb[d][s] = __builtin_amdgcn_raw_buffer_load_b128(rk, ((d) & 1) ? koff2 + s * 256 : koff1 + s * 64, sbase + (unsigned)(j) * 16384u, AUX_NT); SB(); } while (0)
#define VMWAIT(n) do { asm volatile("s_waitcnt vmcnt(%0)" :: "i"(n) : "memory"); SB(); } while (0)
#define RELANE() do { asm volatile("v_mbcnt_lo_u32_b32 %0, -1, 0\n\tv_mbcnt_hi_u32_b32 %0, -1, %0" : "=v"(lane)); c = lane & 15; g = lane >> 4; } while (0)
  int ffo, wstr, wp = 0; const u16* wsrc;
  const int wn = (ABL & 2) ? 0 : role == 1 ? 24 : role == 2 ? 16 : 0;
#define WSETUP() do { ffo = (128 * w + (lane >> 2)) * F + ci * 32 + (((lane & 3) ^ ((lane >> 4) & 2)) * 8); \
    wsrc = role == 1 ? wgate + ffo : wo + (size_t)(bi * 64 + g) * D + 128 * w + 8 * c; wstr = role == 1 ? 16 * F : 4 * D; } while (0)
#define WPIECE() do { if (wp < wn) { glds16(wsrc, lds0 + L_W + w * WREG + wp * 1024); ++wp; wsrc += wstr; \
      if (role == 1 && wp == 8) wsrc = wup + ffo; \
      if (role == 1 && wp == 16) { wsrc = wdown + (size_t)(ci * 32 + g) * D + 128 * w + 8 * c; wstr = 4 * D; } } } while (0)
#define WALL() do { WSETUP(); _Pragma("unroll 1") while (wp < wn) { VMWAIT(PACE_N + 2); WPIECE(); WPIECE(); } } while (0)
  if (qrole) {
    const int r8 = lane >> 3;
    const u16* p = wq + (size_t)(128 * w + r8) * (HQ * DH) + qi * 64;
    const int ch0 = ((lane & 7) ^ (r8 & 2)) * 8;
#pragma unroll 1
    for (int t = 0; t < 16; t += 2) {
      glds16(p + ch0, lds0 + L_W + w * WREG + t * 1024);
      glds16(p + 8 * (HQ * DH) + (ch0 ^ 32), lds0 + L_W + w * WREG + t * 1024 + 1024);
      p += 16 * (HQ * DH);
    }
    STAMP(7);
  }
  STAMP(11);
  if (!qrole) { ISSUEK(0, 0); ISSUEK(1, 1); }
  else {
    const int sw = (q4 & 2) | ((g & 1) << 2);
    v4u fr[16];
#define QFRAGS(ks) do { _Pragma("unroll") for (int t = 0; t < 4; ++t) { \
        LAS unsigned char* a = L + L_W + w * WREG + (ks) * 4096 + g * 1024 + q4 * 128 + (p4 & 1) * 8 + (((2 * t + (p4 >> 1)) ^ sw) * 16); fr[(ks) * 4 + t] = tr_frag(a, a + 512); } } while (0)
    VMWAIT(8); QFRAGS(0); QFRAGS(1); ISSUEK(0, 0);
    VMWAIT(8); QFRAGS(2); ISSUEK(1, 1);
    barrier();
    f32x4 qa[4];
    v4u xb[4];
#pragma unroll
    for (int ks = 0; ks < 4; ++ks) xb[ks] = *(LAS v4u*)(S + L_XN + (c & 1) * 1024 + (128 * w + 32 * ks + 8 * g) * 2);
#pragma unroll
    for (int t = 0; t < 4; ++t) qa[t] = f32x4{0.f, 0.f, 0.f, 0.f};
#pragma unroll
    for (int ks = 0; ks < 3; ++ks)
#pragma unroll
      for (int t = 0; t < 4; ++t) qa[t] = mfma32(fr[ks * 4 + t], xb[ks], qa[t]);
    VMWAIT(8); STAMP(8); QFRAGS(3);
#pragma unroll
    for (int t = 0; t < 4; ++t) qa[t] = mfma32(fr[12 + t], xb[3], qa[t]);
    if (c < 2) {
#pragma unroll
      for (int t = 0; t < 4; ++t) *(LAS f32x4*)(S + L_RED + w * 512 + c * 256 + t * 64 + g * 16) = qa[t];
    }
    STAMP(1);
    barrier();
  }
  const int rem = nstep & 3;
  if (WMODE == 1) WALL();
#pragma unroll
  for (int d = 2; d < 12; ++d) { VMWAIT(PACE_N); ISSUEK(d, d); }
  STAMP(10);
  barrier();
  STAMP(2);
  v4u qf[4];
#pragma unroll
  for (int s = 0; s < 4; ++s) qf[s] = *(LAS v4u*)(L + L_QF + (c >> 2) * 256 + s * 64 + g * 16);
  const v4u qf2 = *(LAS v4u*)(L + L_QF + (c >> 2) * 256 + g * 64 + (c & 3) * 16);
  tag = __builtin_amdgcn_readfirstlane(*(LAS unsigned*)(L + L_TAG));

  LAS unsigned char* scp = L + L_SC + w * 5120 + g * 32 + (c >> 2) * 8;
  float m = -INFINITY;
  f32x4 sc;
#define QK(d) do { \
    if ((d) & 1) { _Pragma("unroll") for (int s = 0; s < 4; ++s) { const f32x4 dd = mfma32(kb[d][s], qf2, f32x4{0.f, 0.f, 0.f, 0.f}); sc[s] = (c & 2) ? ((c & 1) ? dd[3] : dd[2]) : ((c & 1) ? dd[1] : dd[0]); } } \
    else { sc = f32x4{0.f, 0.f, 0.f, 0.f}; _Pragma("unroll") for (int s = 0; s < 4; ++s) sc = mfma32(kb[d][s], qf[s], sc); } \
    asm volatile("" : "+v"(sc) :: "memory"); SB(); } while (0)
#define SCORE(d, j) do { \
    if ((d) & 1) { _Pragma("unroll") for (int s = 0; s < 4; ++s) { float t_ = sc[s]; t_ += dpp<XOR1>(t_); t_ += dpp<XOR2>(t_); sc[s] = t_; } } \
    const unsigned r01 = cvtpk(sc[0], sc[1]), r23 = cvtpk(sc[2], sc[3]); \
    const unsigned t01 = cvtpk(bflo(r01) * SCALE, bfhi(r01) * SCALE), t23 = cvtpk(bflo(r23) * SCALE, bfhi(r23) * SCALE); \
    m = vmax(vmax(m, vmax(bflo(t01), bfhi(t01))), vmax(bflo(t23), bfhi(t23))); \
    *(LAS v2u*)(scp + (j) * 128) = v2u{t01, t23}; asm volatile("" : "+v"(m)); SB(); } while (0)
#pragma unroll
  for (int d = 0; d < 4; ++d) { QK(d); ISSUEK(d, 12 + rem + d); SCORE(d, d); }
#pragma unroll
  for (int d = 4; d < 12; ++d) { QK(d); SCORE(d, d); }
#pragma unroll
  for (int d = 12; d < 15; ++d) if (d - 12 < rem) ISSUEK(d, d);
  if (WMODE == 0) { RELANE(); WSETUP(); }
  int j = 12 + rem;
#pragma unroll 1
  do {
#pragma unroll
    for (int d = 0; d < 4; ++d) { QK(d); ISSUEK(d, j + 4 + d); if (WMODE == 0) { WPIECE(); WPIECE(); } SCORE(d, j + d); }
    j += 4;
  } while (j + 4 < nstep);
#pragma unroll
  for (int d = 12; d < 15; ++d) if (d - 12 < rem) { QK(d); SCORE(d, d); }
  if (WMODE == 2) { RELANE(); WALL(); }
  const int voff = g * 1024 + c * 16;
  v2u vl[7][4], vh[7][4];
#define ISSUEV(d, j) do { _Pragma("unroll") for (int s = 0; s < 4; ++s) { \
      if ((d) & 1) { const v4u t_ = __builtin_amdgcn_raw_buffer_load_b128(rv, voff + s * 256, sbase + (unsigned)(j) * 16384u, AUX_NT); vl[d][s] = v2u{t_[0], t_[1]}; vh[d][s] = v2u{t_[2], t_[3]}; } \
      else { vl[d][s] = __builtin_amdgcn_raw_buffer_load_b64(rv, voff + s * 256, sbase + (unsigned)(j) * 16384u, AUX_NT); NOHOIST(); SB(); \
             vh[d][s] = __builtin_amdgcn_raw_buffer_load_b64(rv, voff + s * 256 + 8, sbase + (unsigned)(j) * 16384u, AUX_NT); NOHOIST(); } } SB(); } while (0)
#pragma unroll
  for (int d = 0; d < 4; ++d) { QK(d); ISSUEV(d, d); SCORE(d, j + d); }
#pragma unroll
  for (int d = 4; d < 7; ++d) if (d - 4 < rem) ISSUEV(d, d);
  STAMP(9);
  m = sw16_max(m, m); m = sw32_max(m, m);
  const float mlog = m * LOG2E;
  const unsigned m0 = (c & 3) == 0 ? 0xffffu : (c & 3) == 1 ? 0xffff0000u : 0u;
  const unsigned m1 = (c & 3) == 2 ? 0xffffu : (c & 3) == 3 ? 0xffff0000u : 0u;
  f32x4 acc0 = {0.f, 0.f, 0.f, 0.f}, acc1 = {0.f, 0.f, 0.f, 0.f};
  float l = 0.f;
#define PV(d, j) do { \
    const v2u t_ = *(LAS v2u*)(scp + (j) * 128); \
    const float sv[4] = {bflo(t_[0]), bfhi(t_[0]), bflo(t_[1]), bfhi(t_[1])}; \
    v2u bb[4]; \
    _Pragma("unroll") for (int i = 0; i < 4; ++i) { const float p_ = __builtin_amdgcn_exp2f(fmaf(sv[i], LOG2E, -mlog)); const unsigned pp = cvtpk(p_, p_); l += bflo(pp); bb[i] = v2u{pp & m0, pp & m1}; } \
    SB(); \
    _Pragma("unroll") for (int i = 0; i < 4; ++i) { acc0 = mfma16(vl[d][i], bb[i], acc0); acc1 = mfma16(vh[d][i], bb[i], acc1); } \
    asm volatile("" : "+v"(acc0), "+v"(acc1) :: "memory"); SB(); } while (0)
#pragma unroll
  for (int d = 0; d < 4; ++d) { PV(d, d); ISSUEV(d, 4 + rem + d); }
#pragma unroll
  for (int d = 4; d < 7; ++d) if (d - 4 < rem) PV(d, d);
  j = 4 + rem;
#pragma unroll 1
  do {
#pragma unroll
    for (int d = 0; d < 4; ++d) { PV(d, j + d); ISSUEV(d, j + 4 + d); }
    j += 4;
  } while (j + 4 < nstep);
  asm volatile("v_mbcnt_lo_u32_b32 %0, -1, 0\n\tv_mbcnt_hi_u32_b32 %0, -1, %0" : "=v"(lane));
  c = lane & 15; g = lane >> 4; q4 = (lane >> 2) & 3; p4 = lane & 3;
  const int tid = w * 64 + lane;
#define PIN(x) asm volatile("" : "+v"(x))
  const unsigned tagh = (tag * 0x9E3779B1u) ^ ((tag * 0x9E3779B1u) >> 15);
  const int hd = 2 * (w & 1) + (lane >> 5);
  unsigned mw = lds0 + L_S + L_MRG + ((w * 4 + (c >> 2)) * 132 + 4 + 32 * g + (c & 3)) * 4, mr = lds0 + L_S + L_MRG + hd * 528;
  int pao = OFF_PART + ((b * HQ + kvh * 4 + hd) * NSPLIT + sp) * 512 + (lane & 31) * 16;
  unsigned mr2 = mr + 16 + (lane & 31) * 16;
  PIN(mw); PIN(mr); PIN(mr2); PIN(pao);
#pragma unroll
  for (int d = 0; d < 4; ++d) PV(d, j + d);
  STAMP(3);

  l = sw16_sum(l, l); l = sw32_sum(l, l);
  {
    LAS float* rec = (LAS float*)mw;
#pragma unroll
    for (int r = 0; r < 4; ++r) { rec[8 * r] = acc0[r]; rec[8 * r + 4] = acc1[r]; }
    if (g == 0 && (c & 3) == 0) *(LAS f32x2*)(rec - 4) = f32x2{m, l};
  }
  barrier();
  if (w < 2) {
    f32x2 ml[4]; f32x4 ov[4];
#pragma unroll
    for (int ww = 0; ww < 4; ++ww) { ml[ww] = *(LAS f32x2*)(mr + ww * 2112); ov[ww] = *(LAS f32x4*)(mr2 + ww * 2112); }
    const float Ml = vmax(vmax(ml[0][0], ml[1][0]), vmax(ml[2][0], ml[3][0])) * LOG2E;
    float wt[4];
#pragma unroll
    for (int ww = 0; ww < 4; ++ww) wt[ww] = __builtin_amdgcn_exp2f(fmaf(ml[ww][0], LOG2E, -Ml));
    const float Ls = fmaf(wt[1], ml[1][1], wt[0] * ml[0][1]) + fmaf(wt[3], ml[3][1], wt[2] * ml[2][1]);
    const f32x4 o = ((wt[0] * ov[0] + wt[1] * ov[1]) + (wt[2] * ov[2] + wt[3] * ov[3])) * __builtin_amdgcn_rcpf(Ls);
    const unsigned lse = f2u(Ml + __builtin_amdgcn_logf(Ls));
    ST128((v4u{cvtpk(o[0], o[1]), __builtin_amdgcn_perm(tagh, lse, 0x05040100u), cvtpk(o[2], o[3]), __builtin_amdgcn_perm(tagh, lse, 0x07060302u)}), pao);
  }
  STAMP(4);
  if (ABL & 8) WAIT_UNTIL(4800, 16);
  unsigned dm0 = (c & 7) == 0 ? 0xffffu : (c & 7) == 1 ? 0xffff0000u : 0u, dm1 = (c & 7) == 2 ? 0xffffu : (c & 7) == 3 ? 0xffff0000u : 0u,
           dm2 = (c & 7) == 4 ? 0xffffu : (c & 7) == 5 ? 0xffff0000u : 0u, dm3 = (c & 7) == 6 ? 0xffffu : (c & 7) == 7 ? 0xffff0000u : 0u;
  unsigned tb0 = tagh & 0xffu, tb1 = (tagh >> 8) & 0xffu, tb2 = (tagh >> 16) & 0xffu, tb3 = tagh >> 24;
#define PK24(x, tb) (((f2u(x) + 0x80u) & 0xffffff00u) | (tb))
#define PUBLISH_ACC(acc, off) ST128((v4u{PK24(acc[0], tb0), PK24(acc[1], tb1), PK24(acc[2], tb2), PK24(acc[3], tb3)}), (off))
#define UP24(x) u2f((x) & 0xffffff00u)

  if (role == 2) {
    v4u wr[16];
#pragma unroll
    for (int i = 0; i < 16; ++i) wr[i] = *(LAS v4u*)(L + L_W + w * WREG + i * 1024 + lane * 16);
    const int head = bi >> 1;
    const int po = OFF_PART + (((lane >> 5) * HQ + head) * NSPLIT + ((lane >> 2) & 7)) * 512 + (bi & 1) * 256 + w * 64 + (lane & 3) * 16;
    unsigned ow = lds0 + L_S + L_OB + ((g >> 1) * 4 + (g & 1) + ((lane & 4) >> 1)) * 64 + (4 * w + (lane & 3)) * 4, orr = lds0 + L_S + L_OB + ((c >> 3) * 4 + g) * 64;
    int so_ = OFF_P1 + bi * 4096 + tid * 16;
    f32x4 acc = {0.f, 0.f, 0.f, 0.f};
    PIN(ow); PIN(orr); PIN(so_); PIN(dm0); PIN(dm1); PIN(dm2); PIN(dm3); PIN(tb0); PIN(tb1); PIN(tb2); PIN(tb3); PIN(acc);
    v4u a0, a1;
    poll2<PD1, 16>(ws, po, po + 8 * 512, tagh, a0, a1);
    STAMP(5);
    const float l0 = u2f(__builtin_amdgcn_perm(a0[3], a0[1], 0x05040100u)), l1 = u2f(__builtin_amdgcn_perm(a1[3], a1[1], 0x05040100u));
    float xm = vmax(l0, l1);
    xm = vmax(vmax(xm, dpp<ROR4>(xm)), vmax(dpp<ROR8>(xm), dpp<ROR12>(xm)));
    xm = sw16_max(xm, xm);
    const float w0 = __builtin_amdgcn_exp2f(l0 - xm), w1 = __builtin_amdgcn_exp2f(l1 - xm);
    float u0 = sw16_sum(fmaf(w1, bflo(a1[0]), w0 * bflo(a0[0])), fmaf(w1, bfhi(a1[0]), w0 * bfhi(a0[0])));
    float u1 = sw16_sum(fmaf(w1, bflo(a1[2]), w0 * bflo(a0[2])), fmaf(w1, bfhi(a1[2]), w0 * bfhi(a0[2])));
    float ws_ = w0 + w1;
    ws_ = sw16_sum(ws_, ws_);
    u0 += dpp<ROR8>(u0); u1 += dpp<ROR8>(u1); ws_ += dpp<ROR8>(ws_);
    u0 += dpp<ROR4>(u0); u1 += dpp<ROR4>(u1); ws_ += dpp<ROR4>(ws_);
    const float uo = ((lane & 4) ? u1 : u0) * __builtin_amdgcn_rcpf(ws_);
    *(LAS unsigned*)ow = cvtpk(uo, uo);
    barrier();
    const v4u pv0 = *(LAS v4u*)orr, pv1 = *(LAS v4u*)(orr + 16), pv2 = *(LAS v4u*)(orr + 32), pv3 = *(LAS v4u*)(orr + 48);
    diag_chain_regs(acc, wr, pv0, pv1, dm0, dm1, dm2, dm3);
    diag_chain_regs(acc, wr + 8, pv2, pv3, dm0, dm1, dm2, dm3);
    mfma_drain(acc);
    PUBLISH_ACC(acc, so_);
    STAMP(6);
    poll_done(a0, a1);
  } else if (role == 3) {
    const int rb = rj & 1, col0 = (rj >> 1) * 32;
    unsigned xp = *(const GAS unsigned*)(x + rb * D + col0 + 2 * c);
    asm volatile("" : "+v"(xp));
    const int pp = lane >> 3;
    float h0, h1;
    unsigned rw_ = lds0 + L_S + L_RR + w * 128 + (8 * g + (lane & 7)) * 4, rr_ = lds0 + L_S + L_RR + c * 8;
    int hbo = OFF_HB + ((w & 1) * 4 + g) * 4096 + rb * 2048 + col0 * 4 + c * 8;
    GAS unsigned* op = (GAS unsigned*)(out + rb * D + col0 + 2 * c);
    PIN(rw_); PIN(rr_); PIN(hbo); PIN(op);
    {
      const int po = OFF_P1 + (16 * w + pp) * 4096 + rj * 128 + (lane & 7) * 16;
      v4u a0, a1;
      poll2<PD2, -1>(ws, po, po + 8 * 4096, tagh, a0, a1);
      STAMP(5);
      float u = sw16_sum(sw32_sum(UP24(a0[0]) + UP24(a1[0]), UP24(a0[2]) + UP24(a1[2])), sw32_sum(UP24(a0[1]) + UP24(a1[1]), UP24(a0[3]) + UP24(a1[3])));
      u += dpp<ROR8>(u);
      *(LAS float*)rw_ = u;
      barrier();
      const f32x2 s = (*(LAS f32x2*)rr_ + *(LAS f32x2*)(rr_ + 128)) + (*(LAS f32x2*)(rr_ + 256) + *(LAS f32x2*)(rr_ + 384));
      const unsigned at = cvtpk(s[0], s[1]);
      const unsigned hp = cvtpk(bflo(xp) + bflo(at), bfhi(xp) + bfhi(at));
      h0 = bflo(hp); h1 = bfhi(hp);
      if (w < 2) __builtin_amdgcn_raw_buffer_store_b64(v2u{hp, tag}, rw, hbo, 0, AUX_SC1);
      STAMP(6);
      poll_done(a0, a1);
    }
    {
      const int po = OFF_P2 + (14 * w + pp) * 4096 + rj * 128 + (lane & 7) * 16;
      v4u a0, b1;
      poll2<PD4, -1>(ws, po, po + (pp < 6 ? 8 * 4096 : 0), tagh, a0, b1);
      STAMP(7);
      const unsigned km = pp < 6 ? 0xffffff00u : 0u;
      float u = sw16_sum(sw32_sum(UP24(a0[0]) + u2f(b1[0] & km), UP24(a0[2]) + u2f(b1[2] & km)), sw32_sum(UP24(a0[1]) + u2f(b1[1] & km), UP24(a0[3]) + u2f(b1[3] & km)));
      u += dpp<ROR8>(u);
      *(LAS float*)(rw_ + 512) = u;
      barrier();
      if (tid < 16) {
        const f32x2 s = (*(LAS f32x2*)(rr_ + 512) + *(LAS f32x2*)(rr_ + 640)) + (*(LAS f32x2*)(rr_ + 768) + *(LAS f32x2*)(rr_ + 896));
        const unsigned dn = cvtpk(s[0], s[1]);
        *op = cvtpk(h0 + bflo(dn), h1 + bfhi(dn));
        if (rj == 0 && tid == 0) __builtin_amdgcn_raw_buffer_store_b32(tag, rw, OFF_EPOCH, 0, AUX_SC1);
      }
      STAMP(8);
      poll_done(a0, b1);
    }
  } else if (role == 1) {
    v4u fg[8], fu[8], wd[8];
#pragma unroll
    for (int ks = 0; ks < 4; ++ks)
#pragma unroll
      for (int t = 0; t < 2; ++t) {
        LAS unsigned char* a = L + L_W + w * WREG + (32 * ks + 8 * g + q4) * 64 + (((2 * t + (p4 >> 1)) ^ ((g & 1) << 1)) * 16) + (p4 & 1) * 8;
        fg[ks * 2 + t] = tr_frag(a, a + 256); fu[ks * 2 + t] = tr_frag(a + 8192, a + 8192 + 256);
      }
#pragma unroll
    for (int i = 0; i < 8; ++i) wd[i] = *(LAS v4u*)(L + L_W + w * WREG + 16384 + i * 1024 + lane * 16);
    f32x4 ag[2], au[2];
    unsigned ro = lds0 + L_S + L_RED + ((c >> 2) & 1) * 1024 + ((c & 1) * 32 + (c & 2) * 8 + 4 * g) * 16 + w * 4;
    unsigned fo = lds0 + L_S + L_RED + ((c >> 3) * 32 + 4 * (c & 3) + g) * 16;
#define GU_MFMA(XB) do { _Pragma("unroll") for (int t = 0; t < 2; ++t) { ag[t] = f32x4{0.f, 0.f, 0.f, 0.f}; au[t] = f32x4{0.f, 0.f, 0.f, 0.f}; } \
      _Pragma("unroll") for (int ks = 0; ks < 4; ++ks) { \
        _Pragma("unroll") for (int t = 0; t < 2; ++t) { ag[t] = mfma32(fg[ks * 2 + t], XB, ag[t]); au[t] = mfma32(fu[ks * 2 + t], XB, au[t]); } } } while (0)
#define GU_STORE() do { const f32x4 sel = (c & 4) ? ((c & 2) ? au[1] : au[0]) : ((c & 2) ? ag[1] : ag[0]); \
      _Pragma("unroll") for (int i = 0; i < 4; ++i) *(LAS float*)(ro + i * 16) = sel[i]; } while (0)
#define SUM4(p_) ({ const f32x2 t_ = f32x2{p_[0], p_[1]} + f32x2{p_[2], p_[3]}; float r_ = t_[0] + t_[1]; PIN(r_); r_; })
    GU_MFMA((v4u{0x3f803f80u, 0x3f803f80u, 0x3f803f80u, 0x3f803f80u}));
    GU_STORE();
    barrier();
    float cg0, cg1, cu0, cu1;
    { const f32x4 p0 = *(LAS f32x4*)fo, p1 = *(LAS f32x4*)(fo + 256), p2 = *(LAS f32x4*)(fo + 1024), p3 = *(LAS f32x4*)(fo + 1280); cg0 = SUM4(p0); cg1 = SUM4(p1); cu0 = SUM4(p2); cu1 = SUM4(p3); }
    unsigned xw = lds0 + L_S + L_XN + (w & 1) * 1024 + lane * 16, xr = lds0 + L_S + L_XN + (c & 1) * 1024 + (128 * w + 8 * g) * 2, sw_ = lds0 + L_S + L_ST + w * 8, sr = lds0 + L_S + L_ST + (c >> 3) * 8;
    int so_ = OFF_P2 + ci * 4096 + tid * 16;
    f32x4 acc = {0.f, 0.f, 0.f, 0.f};
    PIN(ro); PIN(fo); PIN(xw); PIN(xr); PIN(sw_); PIN(sr); PIN(so_); PIN(dm0); PIN(dm1); PIN(dm2); PIN(dm3); PIN(tb0); PIN(tb1); PIN(tb2); PIN(tb3); PIN(acc);
    v4u ha = {0u, 0u, 0u, 0u}, hb = ha;
    if (w < 2) {
      const int ho = OFF_HB + xcd * 4096 + w * 2048 + lane * 32;
      poll2<PD3, 0>(ws, ho, ho + 16, tag, ha, hb);
      STAMP(5);
      *(LAS v4u*)xw = v4u{ha[0], ha[2], hb[0], hb[2]};
    }
    barrier();
    v4u xb[4];
#pragma unroll
    for (int ks = 0; ks < 4; ++ks) xb[ks] = *(LAS v4u*)(xr + 64 * ks);
    __builtin_amdgcn_sched_barrier(0);
    const float pv = bflo(__builtin_amdgcn_readfirstlane(ha[0]));
    const float d0 = bflo(ha[0]) - pv, d1 = bfhi(ha[0]) - pv, d2 = bflo(ha[2]) - pv, d3 = bfhi(ha[2]) - pv, d4 = bflo(hb[0]) - pv, d5 = bfhi(hb[0]) - pv, d6 = bflo(hb[2]) - pv, d7 = bfhi(hb[2]) - pv;
    float s1 = ((d0 + d1) + (d2 + d3)) + ((d4 + d5) + (d6 + d7));
    float s2 = (fmaf(d1, d1, d0 * d0) + fmaf(d3, d3, d2 * d2)) + (fmaf(d5, d5, d4 * d4) + fmaf(d7, d7, d6 * d6));
    __builtin_amdgcn_sched_barrier(0);
    GU_MFMA(xb[ks]);
    {
      float t = sw32_sum(s1, s2);
      t = sw16_sum(t, t); t += dpp<ROR8>(t); t += dpp<ROR4>(t); t += dpp<XOR2>(t); t += dpp<XOR1>(t);
      const float md = u2f(__builtin_amdgcn_readlane(f2u(t), 0)) * (1.0f / D), q2 = u2f(__builtin_amdgcn_readlane(f2u(t), 32)) * (1.0f / D);
      const float var = vmax(fmaf(-md, md, q2), 0.f);
      const float rs = __builtin_amdgcn_rcpf(rbf(__builtin_amdgcn_sqrtf(rbf(rbf(var) + 1e-5f))));
      *(LAS f32x2*)sw_ = f32x2{rbf(pv + md), rs};
    }
    GU_STORE();
    barrier();
    const f32x2 st = *(LAS f32x2*)sr;
    const f32x4 p0 = *(LAS f32x4*)fo, p1 = *(LAS f32x4*)(fo + 256), p2 = *(LAS f32x4*)(fo + 1024), p3 = *(LAS f32x4*)(fo + 1280);
    __builtin_amdgcn_sched_barrier(0);
    const float G0 = SUM4(p0), G1 = SUM4(p1), U0 = SUM4(p2), U1 = SUM4(p3);
    const unsigned gp = cvtpk(fmaf(-st[0], cg0, G0) * st[1], fmaf(-st[0], cg1, G1) * st[1]);
    const unsigned up = cvtpk(fmaf(-st[0], cu0, U0) * st[1], fmaf(-st[0], cu1, U1) * st[1]);
    const float g0 = bflo(gp), g1 = bfhi(gp);
    const unsigned sg = cvtpk(g0 * __builtin_amdgcn_rcpf(1.0f + __builtin_amdgcn_exp2f(-g0 * LOG2E)), g1 * __builtin_amdgcn_rcpf(1.0f + __builtin_amdgcn_exp2f(-g1 * LOG2E)));
    const float av0 = bflo(sg) * bflo(up), av1 = bfhi(sg) * bfhi(up);
    diag_chain_quad(acc, wd, cvtpk(av0, av0), cvtpk(av1, av1), dm0, dm1, dm2, dm3);
    mfma_drain(acc);
    PUBLISH_ACC(acc, so_);
    STAMP(6);
    poll_done(ha, hb);
  }
  DUMP_STAMPS();
}

extern "C" void kernel_launch(void* const* d_in, const int* in_sizes, int n_in, void* d_out, int out_size, void* d_ws, size_t ws_size, hipStream_t stream) {
  fused_block<<<NWG, NTHR, 0, stream>>>((const u16*)d_in[0], (const u16*)d_in[1], (const u16*)d_in[2], (const u16*)d_in[3], (unsigned char*)d_ws,
                                        (const u16*)d_in[4], (const u16*)d_in[6], (const u16*)d_in[7], (const u16*)d_in[8], (u16*)d_out, make_plan() TUNE_ARG);
}
```

```cpp
#pragma clang diagnostic ignored "-Wint-to-pointer-cast"
#include <hip/hip_runtime.h>
#include <hip/hip_bf16.h>
#include <cstdint>

typedef unsigned short u16;
typedef unsigned v4u __attribute__((ext_vector_type(4)));
typedef unsigned v2u __attribute__((ext_vector_type(2)));
typedef float f32x4 __attribute__((ext_vector_type(4)));
typedef float f32x2 __attribute__((ext_vector_type(2)));
typedef short s16x4 __attribute__((ext_vector_type(4)));
typedef __bf16 bf16x8_t __attribute__((ext_vector_type(8)));
typedef __bf16 bf16x2_t __attribute__((ext_vector_type(2)));
#define LAS __attribute__((address_space(3)))
#define GAS __attribute__((address_space(1)))

constexpr int D = 512, HQ = 32, HKV = 8, SK = 32768, DH = 128, F = 1792;
constexpr int NWG = 256, NTHR = 320, NSPLIT = 16;
struct Plan { unsigned e[32]; };
#define TUNE_PARAM
#define TUNE_ARG
#define ABL 0
#define TL_C 130
#define TL_CS 128
#define TL_B 128
#define TL_Q 123
#define TL_QS 119
#define WE_C 0
#define WE_B 0
#define T_P 530
static Plan make_plan() {
  Plan p;
  for (int b = 0; b < 2; ++b) {
    const int nc = 4 - b, nb = 3 + 2 * b;
    int n[16], role[16], ix[16], used = 0, nn = 0;
    for (int sp = 0; sp < 16; ++sp) {
      if (sp < nc) { role[sp] = 1; ix[sp] = 2 * sp + b; n[sp] = sp == 0 ? TL_CS : TL_C; }
      else if (sp < nc + nb) { role[sp] = 2; ix[sp] = 3 * b + sp - nc; n[sp] = TL_B; }
      else if (sp >= 12) { role[sp] = 4; ix[sp] = 2 * (sp - 12) + b; n[sp] = ix[sp] == 3 ? TL_QS : TL_Q; }
      else { role[sp] = (sp == 8 || sp == 9) ? 3 : 0; ix[sp] = role[sp] ? 2 * (sp - 8) + b : 0; n[sp] = 0; ++nn; }
      used += n[sp];
    }
    int rest = 2048 - used, t0 = 0;
    for (int sp = 0; sp < 16; ++sp) {
      if (n[sp] == 0) { n[sp] = (rest + nn - 1) / nn; rest -= n[sp]; --nn; }
      p.e[2 * sp + b] = (unsigned)t0 | ((unsigned)n[sp] << 11) | ((unsigned)role[sp] << 19) | ((unsigned)ix[sp] << 22);
      t0 += n[sp];
    }
  }
  return p;
}
#define PD1 2
#define PD2 3
#define PD3 1
#define PD4 3
#define PSL 1
#define PACE_N 4
#define CODE_BYTES 24576
constexpr int NB = 64, NC = 56, NR = 64, NREP = 8;
constexpr int OFF_EPOCH = 0;
constexpr int OFF_HB = 0x190000;
constexpr int OFF_PART = 0x10000;
constexpr int OFF_P1 = 0x90000;
constexpr int OFF_P2 = 0xD0000;
constexpr int OFF_Q = 0x180000;
constexpr int WS_BYTES = 0x1A0000, OFF_DBG = 0x200000;
constexpr int AUX_NT = 2, AUX_SC1 = 16;
constexpr unsigned SPIN_CAP = 1u << 16;
constexpr int L_W = 0;
constexpr int WREG = 24576;
constexpr int L_QF = 4 * WREG;
constexpr int L_SC = L_QF + 1024;
constexpr int L_TAG = L_SC + 4 * 40 * 128;
constexpr int L_S = L_TAG + 16;
constexpr int L_XN = 0;
constexpr int L_RED = L_XN + 2048;
constexpr int L_MRG = L_RED + 2048;
constexpr int L_OB = L_MRG + 4 * 4 * 132 * 4;
constexpr int L_RR = L_OB + 512;
constexpr int L_ST = L_RR + 1024;
constexpr int S_BYTES = L_ST + 32;
constexpr int L_PF = L_S + S_BYTES;
constexpr int L_DBG = L_PF + 256;
constexpr int L_BYTES = L_DBG + 128;

__device__ __forceinline__ float bflo(unsigned v) { return __uint_as_float(v << 16); }
__device__ __forceinline__ float bfhi(unsigned v) { return __uint_as_float(v & 0xffff0000u); }
__device__ __forceinline__ unsigned cvtpk(float lo, float hi) { return __builtin_bit_cast(unsigned, __builtin_convertvector(f32x2{lo, hi}, bf16x2_t)); }
__device__ __forceinline__ float rbf(float x) { return bflo(cvtpk(x, x)); }
__device__ __forceinline__ bool tagged(v4u v, unsigned tag) { return (v[1] == tag) & (v[3] == tag); }
__device__ __forceinline__ float u2f(unsigned v) { return __uint_as_float(v); }
__device__ __forceinline__ unsigned f2u(float v) { return __float_as_uint(v); }

__device__ __forceinline__ float vmax(float a, float b) { return __builtin_elementwise_maximum(a, b); }
template <int CTRL> __device__ __forceinline__ float dpp(float x) { return __builtin_bit_cast(float, __builtin_amdgcn_mov_dpp(__builtin_bit_cast(int, x), CTRL, 0xf, 0xf, true)); }
template <int CTRL> __device__ __forceinline__ unsigned dppu(unsigned x) { return (unsigned)__builtin_amdgcn_mov_dpp((int)x, CTRL, 0xf, 0xf, true); }
constexpr int XOR1 = 0xB1, XOR2 = 0x4E, ROR8 = 0x128, ROR4 = 0x124, ROR12 = 0x12C;
__device__ __forceinline__ float sw32_sum(float a, float b) { auto r = __builtin_amdgcn_permlane32_swap(f2u(a), f2u(b), false, false); return u2f(r[0]) + u2f(r[1]); }
__device__ __forceinline__ float sw16_sum(float a, float b) { auto r = __builtin_amdgcn_permlane16_swap(f2u(a), f2u(b), false, false); return u2f(r[0]) + u2f(r[1]); }
__device__ __forceinline__ float sw32_max(float a, float b) { auto r = __builtin_amdgcn_permlane32_swap(f2u(a), f2u(b), false, false); return vmax(u2f(r[0]), u2f(r[1])); }
__device__ __forceinline__ float sw16_max(float a, float b) { auto r = __builtin_amdgcn_permlane16_swap(f2u(a), f2u(b), false, false); return vmax(u2f(r[0]), u2f(r[1])); }
__device__ __forceinline__ float wave_sum(float x) {
  x += dpp<XOR1>(x); x += dpp<XOR2>(x); x += dpp<ROR4>(x); x += dpp<ROR8>(x);
  x = sw16_sum(x, x); return sw32_sum(x, x);
}
__device__ __forceinline__ f32x4 mfma32(v4u a, v4u b, f32x4 c) { return __builtin_amdgcn_mfma_f32_16x16x32_bf16(__builtin_bit_cast(bf16x8_t, a), __builtin_bit_cast(bf16x8_t, b), c, 0, 0, 0); }
__device__ __forceinline__ f32x4 mfma16(v2u a, v2u b, f32x4 c) { return __builtin_amdgcn_mfma_f32_16x16x16bf16_1k(__builtin_bit_cast(s16x4, a), __builtin_bit_cast(s16x4, b), c, 0, 0, 0); }
__device__ __forceinline__ void barrier() { asm volatile("s_waitcnt lgkmcnt(0)\n\ts_barrier" ::: "memory"); }
__device__ __forceinline__ v4u tr_frag(LAS unsigned char* lo, LAS unsigned char* hi) {
  const v2u a = __builtin_bit_cast(v2u, __builtin_amdgcn_ds_read_tr16_b64_v4i16((LAS s16x4*)lo));
  const v2u b = __builtin_bit_cast(v2u, __builtin_amdgcn_ds_read_tr16_b64_v4i16((LAS s16x4*)hi));
  return v4u{a[0], a[1], b[0], b[1]};
}

#define AND4(T0, T1, T2, T3, SRC, MOD, SFX) "v_and_b32" SFX " " T0 ", " SRC ", %[m0]" MOD "\n\tv_and_b32" SFX " " T1 ", " SRC ", %[m1]" MOD "\n\tv_and_b32" SFX " " T2 ", " SRC ", %[m2]" MOD "\n\tv_and_b32" SFX " " T3 ", " SRC ", %[m3]" MOD "\n\t"
#define ANDA(SRC, MOD, SFX) AND4("v240", "v241", "v242", "v243", SRC, MOD, SFX)
#define ANDB(SRC, MOD, SFX) AND4("v244", "v245", "v246", "v247", SRC, MOD, SFX)
#define MFA(W) "v_mfma_f32_16x16x32_bf16 %[acc], " W ", v[240:243], %[acc]\n\t"
#define MFB(W) "v_mfma_f32_16x16x32_bf16 %[acc], " W ", v[244:247], %[acc]\n\t"
#define CHAIN_CLOB "v240", "v241", "v242", "v243", "v244", "v245", "v246", "v247"
#define CHAIN_W(W) [w0] "v"(W[0]), [w1] "v"(W[1]), [w2] "v"(W[2]), [w3] "v"(W[3]), [w4] "v"(W[4]), [w5] "v"(W[5]), [w6] "v"(W[6]), [w7] "v"(W[7])
__device__ __forceinline__ void diag_chain_regs(f32x4& acc, const v4u* W, v4u pa, v4u pb, unsigned m0, unsigned m1, unsigned m2, unsigned m3) {
  asm volatile(ANDA("%[p0]", "", "") ANDB("%[p1]", "", "") MFA("%[w0]") ANDA("%[p2]", "", "") MFB("%[w1]") ANDB("%[p3]", "", "") MFA("%[w2]") ANDA("%[p4]", "", "") MFB("%[w3]")
               ANDB("%[p5]", "", "") MFA("%[w4]") ANDA("%[p6]", "", "") MFB("%[w5]") ANDB("%[p7]", "", "") MFA("%[w6]") "s_nop 0\n\t" MFB("%[w7]")
               : [acc] "+v"(acc)
               : CHAIN_W(W), [p0] "v"(pa[0]), [p1] "v"(pa[1]), [p2] "v"(pa[2]), [p3] "v"(pa[3]), [p4] "v"(pb[0]), [p5] "v"(pb[1]), [p6] "v"(pb[2]), [p7] "v"(pb[3]),
                 [m0] "v"(m0), [m1] "v"(m1), [m2] "v"(m2), [m3] "v"(m3)
               : CHAIN_CLOB);
}
#define QP(t) " quad_perm:[" #t "," #t "," #t "," #t "] row_mask:0xf bank_mask:0xf"
__device__ __forceinline__ void diag_chain_quad(f32x4& acc, const v4u* W, unsigned pa, unsigned pb, unsigned m0, unsigned m1, unsigned m2, unsigned m3) {
  asm volatile("s_nop 1\n\t" ANDA("%[pa]", QP(0), "_dpp") ANDB("%[pa]", QP(1), "_dpp") MFA("%[w0]") ANDA("%[pa]", QP(2), "_dpp") MFB("%[w1]") ANDB("%[pa]", QP(3), "_dpp") MFA("%[w2]")
               ANDA("%[pb]", QP(0), "_dpp") MFB("%[w3]") ANDB("%[pb]", QP(1), "_dpp") MFA("%[w4]") ANDA("%[pb]", QP(2), "_dpp") MFB("%[w5]") ANDB("%[pb]", QP(3), "_dpp") MFA("%[w6]")
               "s_nop 0\n\t" MFB("%[w7]")
               : [acc] "+v"(acc)
               : CHAIN_W(W), [pa] "v"(pa), [pb] "v"(pb), [m0] "v"(m0), [m1] "v"(m1), [m2] "v"(m2), [m3] "v"(m3)
               : CHAIN_CLOB);
}
__device__ __forceinline__ void mfma_drain(f32x4& acc) { asm volatile("s_nop 7" : "+v"(acc)); }

__device__ __forceinline__ v4u ln_row(v4u xv) {
  float x[8];
#pragma unroll
  for (int i = 0; i < 4; ++i) { x[2 * i] = bflo(xv[i]); x[2 * i + 1] = bfhi(xv[i]); }
  const float mean = wave_sum(((x[0] + x[1]) + (x[2] + x[3])) + ((x[4] + x[5]) + (x[6] + x[7]))) * (1.0f / D);
  float vs = 0.f;
#pragma unroll
  for (int i = 0; i < 8; ++i) { const float d = x[i] - mean; vs = fmaf(d, d, vs); }
  const float var = wave_sum(vs) * (1.0f / D);
  const float mb = rbf(mean);
  const float rs = __builtin_amdgcn_rcpf(rbf(__builtin_amdgcn_sqrtf(rbf(rbf(var) + 1e-5f))));
  v4u r;
#pragma unroll
  for (int i = 0; i < 4; ++i) { const unsigned d = cvtpk(x[2 * i] - mb, x[2 * i + 1] - mb); r[i] = cvtpk(bflo(d) * rs, bfhi(d) * rs); }
  return r;
}
__device__ __forceinline__ void glds16(const u16* gsrc, unsigned lds_dst) {
  unsigned keep;
  asm volatile("s_mov_b32 %0, m0\n\ts_mov_b32 m0, %2\n\ts_nop 0\n\tglobal_load_lds_dwordx4 %1, off nt\n\ts_mov_b32 m0, %0" : "=&s"(keep) : "v"(gsrc), "s"(lds_dst) : "memory");
}

__device__ __forceinline__ unsigned lowbytes(v4u v) { return __builtin_amdgcn_perm(v[1], v[0], 0x0c0c0400u) | __builtin_amdgcn_perm(v[3], v[2], 0x04000c0cu); }
template <int PD_, int TSH>
__device__ __forceinline__ void poll2(const unsigned char* ws, int o0, int o1, unsigned tag, v4u& r0, v4u& r1) {
  r0 = v4u{~tag, ~tag, ~tag, ~tag}; r1 = r0;
#define PASS() asm volatile("global_load_dwordx4 %0, %2, %4 sc1\n\tglobal_load_dwordx4 %1, %3, %4 sc1" : "+v"(r0), "+v"(r1) : "v"(o0), "v"(o1), "s"(ws) : "memory")
#pragma unroll
  for (int k = 0; k < PD_ - 1; ++k) { PASS(); __builtin_amdgcn_s_sleep(PSL); }
#pragma unroll 1
  for (unsigned k = 0; k < SPIN_CAP; ++k) {
    PASS();
    asm volatile("s_waitcnt vmcnt(%2)" : "+v"(r0), "+v"(r1) : "i"((PD_ - 1) * 2) : "memory");
    if (TSH == 16) { if (__all((__builtin_amdgcn_perm(r0[3], r0[1], 0x07060302u) == tag) & (__builtin_amdgcn_perm(r1[3], r1[1], 0x07060302u) == tag))) break; }
    else if (TSH < 0) { if (__all((lowbytes(r0) == tag) & (lowbytes(r1) == tag))) break; }
    else if (__all((((r0[1] ^ tag) | (r0[3] ^ tag) | (r1[1] ^ tag) | (r1[3] ^ tag)) >> (TSH < 0 ? 0 : TSH)) == 0u)) break;
    if (PD_ > 1) __builtin_amdgcn_s_sleep(PSL);
  }
#undef PASS
  asm volatile("s_nop 3 ; POLL_END" : "+v"(r0), "+v"(r1));
}
__device__ __forceinline__ void poll_done(v4u& r0, v4u& r1) { asm volatile("s_waitcnt vmcnt(0)" : "+v"(r0), "+v"(r1) :: "memory"); }

#define STAMP(i) do { } while (0)
#define DUMP_STAMPS() do { } while (0)
#define LD128(off) __builtin_amdgcn_raw_buffer_load_b128(rw, (off), 0, AUX_SC1)
#define ST128(v, off) __builtin_amdgcn_raw_buffer_store_b128((v), rw, (off), 0, AUX_SC1)
#define SPIN_LOOP _Pragma("unroll 1") for (unsigned spins = 0; spins < SPIN_CAP; ++spins)
#define NOHOIST() asm volatile("" ::: "memory")

__global__ void __launch_bounds__(NTHR, 2) fused_block(const u16* x, const u16* kc, const u16* vc, const u16* wq, unsigned char* ws,
                                                       const u16* wo, const u16* wgate, const u16* wup, const u16* wdown, u16* out, Plan plan TUNE_PARAM) {
  __shared__ __attribute__((aligned(16))) unsigned char smem[L_BYTES];
  LAS unsigned char* L = (LAS unsigned char*)smem;
  STAMP(0);
  const unsigned tstart = (unsigned)__builtin_amdgcn_s_memrealtime();
  const int wg = blockIdx.x, xcd = wg & 7, idx = wg >> 3;
  const int kvh = xcd, b = idx & 1, sp = idx >> 1;
  const unsigned pe = ((const __attribute__((address_space(4))) unsigned*)__builtin_amdgcn_kernarg_segment_ptr())[20 + idx];
  int lane = threadIdx.x & 63;
  const int w = __builtin_amdgcn_readfirstlane(threadIdx.x >> 6);
  int c = lane & 15, g = lane >> 4;
  if (w == 4 && !(ABL & 32)) {
    unsigned long long pc;
    asm volatile("s_getpc_b64 %0" : "=s"(pc));
    if ((lane * 32 + idx) * 128 < CODE_BYTES) {
      unsigned keep;
      asm volatile("s_mov_b32 %0, m0\n\ts_mov_b32 m0, %3\n\ts_nop 0\n\tglobal_load_lds_dword %1, %2\n\ts_mov_b32 m0, %0"
                   : "=&s"(keep) : "v"((lane * 32 + idx) * 128), "s"(pc & ~255ull), "s"((unsigned)(uintptr_t)L + L_PF) : "memory");
    }
  }
  asm volatile("" : "+s"(x), "+s"(kc), "+s"(vc), "+s"(wq), "+s"(ws), "+s"(wo), "+s"(wgate), "+s"(wup), "+s"(wdown), "+s"(out));
  const int role = (pe >> 19) & 7, rix = pe >> 22;
  const bool qrole = role == 4;
  const int cn = xcd * 5 + rix - (xcd < 7 ? 2 : 0);
  const int ci = (xcd < 7 && rix < 2) ? 8 * xcd + 6 + rix : cn + 2 * ((cn * 43) >> 8);
  const int bi = xcd * 8 + rix, rj = xcd * 4 + rix, qi = bi;
  const __amdgpu_buffer_rsrc_t rw = __builtin_amdgcn_make_buffer_rsrc((void*)ws, 0, WS_BYTES, 0x00020000);
  const unsigned lds0 = (unsigned)(uintptr_t)L;
  LAS unsigned char* S = L + L_S;
#define WAIT_UNTIL(t, slp) do { while ((int)((unsigned)__builtin_amdgcn_s_memrealtime() - (tstart + (unsigned)(t))) < 0) __builtin_amdgcn_s_sleep(slp); } while (0)
  int q4 = (lane >> 2) & 3, p4 = lane & 3;
  constexpr float SCALE = 0.088388347648318440f, LOG2E = 1.4426950408889634f;
  unsigned tag;

  if (w == 4) {
    __builtin_amdgcn_s_setprio(3);
    if (qrole) {
      const v4u x0 = *(const GAS v4u*)(x + lane * 8), x1 = *(const GAS v4u*)(x + D + lane * 8);
      tag = __builtin_amdgcn_raw_buffer_load_b32(rw, OFF_EPOCH, 0, AUX_SC1) + 1u;
      const v4u n0 = ln_row(x0), n1 = ln_row(x1);
      *(LAS v4u*)(S + L_XN + lane * 16) = n0; *(LAS v4u*)(S + L_XN + 1024 + lane * 16) = n1;
      STAMP(9);
      barrier();
      barrier();
      f32x2 s = {0.f, 0.f};
#pragma unroll
      for (int ww = 0; ww < 4; ++ww) s += *(LAS f32x2*)(S + L_RED + ww * 512 + lane * 8);
      __builtin_amdgcn_raw_buffer_store_b64(v2u{cvtpk(s[0], s[1]), tag}, rw, OFF_Q + ((lane >> 5) * 2048 + qi * 32 + (lane & 31)) * 8, 0, AUX_SC1);
    } else {
      tag = __builtin_amdgcn_raw_buffer_load_b32(rw, OFF_EPOCH, 0, AUX_SC1) + 1u;
    }
    STAMP(1);
    if (lane == 0) *(LAS unsigned*)(L + L_TAG) = tag;
    {
      const int qo = OFF_Q + (b * 2048 + kvh * 256) * 8 + lane * 16;
      v4u q0, q1;
      bool ok0 = false, ok1 = false;
      WAIT_UNTIL(T_P, 2);
      SPIN_LOOP {
        NOHOIST();
        if (!ok0) q0 = LD128(qo);
        if (!ok1) q1 = LD128(qo + 1024);
        ok0 = tagged(q0, tag); ok1 = tagged(q1, tag);
        if (__all(ok0 & ok1)) break;
      }
      *(LAS v2u*)(L + L_QF + lane * 8) = v2u{q0[0], q0[2]};
      *(LAS v2u*)(L + L_QF + 512 + lane * 8) = v2u{q1[0], q1[2]};
    }
    STAMP(2);
    barrier();
    return;
  }
  const int tile0 = pe & 2047, ntile = (pe >> 11) & 255;
  const int nstep = (ntile - w + 3) >> 2;
  const size_t pbase = (size_t)(b * HKV + kvh) * SK * DH;
  const __amdgpu_buffer_rsrc_t rk = __builtin_amdgcn_make_buffer_rsrc((void*)(kc + pbase), 0, SK * DH * 2, 0x00020000);
  const __amdgpu_buffer_rsrc_t rv = __builtin_amdgcn_make_buffer_rsrc((void*)(vc + pbase), 0, SK * DH * 2, 0x00020000);
  const int koff = (c >> 2) * 1024 + g * 64 + (c & 3) * 16;
  const unsigned sbase = (unsigned)(tile0 + w) * 4096u;
  v4u kb[15][4];
#define SB() __builtin_amdgcn_sched_barrier(0)
#define ISSUEK(d, j) do { _Pragma("unroll") for (int s = 0; s < 4; ++s) { \
      if ((d) & 1) kb[d][s] = __builtin_amdgcn_raw_buffer_load_b128(rk, koff + s * 256, sbase + (unsigned)(j) * 16384u, AUX_NT); \
      else { const v2u lo_ = __builtin_amdgcn_raw_buffer_load_b64(rk, koff + s * 256, sbase + (unsigned)(j) * 16384u, AUX_NT); NOHOIST(); SB(); \
             const v2u hi_ = __builtin_amdgcn_raw_buffer_load_b64(rk, koff + s * 256 + 8, sbase + (unsigned)(j) * 16384u, AUX_NT); NOHOIST(); \
             kb[d][s] = v4u{lo_[0], lo_[1], hi_[0], hi_[1]}; } } SB(); } while (0)
#define VMWAIT(n) do { asm volatile("s_waitcnt vmcnt(%0)" :: "i"(n) : "memory"); SB(); } while (0)
#define RELANE() do { asm volatile("v_mbcnt_lo_u32_b32 %0, -1, 0\n\tv_mbcnt_hi_u32_b32 %0, -1, %0" : "=v"(lane)); c = lane & 15; g = lane >> 4; } while (0)
  int wp = 0;
#define WUPTO(n, pace) do { _Pragma("unroll 1") while (wp < (n)) { \
      const u16* src_ = role == 2 ? wo + (size_t)(bi * 64 + 4 * wp + g) * D + 128 * w + 8 * c \
                      : wp < 16 ? (wp < 8 ? wgate : wup) + (size_t)(128 * w + 16 * (wp & 7) + (lane >> 2)) * F + ci * 32 + (((lane & 3) ^ ((lane >> 4) & 2)) * 8) \
                                : wdown + (size_t)(ci * 32 + 4 * (wp & 7) + g) * D + 128 * w + 8 * c; \
      VMWAIT(pace); glds16(src_, lds0 + L_W + w * WREG + wp * 1024); ++wp; } } while (0)
  if (qrole) {
    const int r8 = lane >> 3;
    const u16* p = wq + (size_t)(128 * w + r8) * (HQ * DH) + qi * 64;
    const int ch0 = ((lane & 7) ^ (r8 & 2)) * 8;
#pragma unroll 1
    for (int t = 0; t < 16; t += 2) {
      glds16(p + ch0, lds0 + L_W + w * WREG + t * 1024);
      glds16(p + 8 * (HQ * DH) + (ch0 ^ 32), lds0 + L_W + w * WREG + t * 1024 + 1024);
      p += 16 * (HQ * DH);
    }
    STAMP(7);
  }
  STAMP(11);
  if (!qrole) { ISSUEK(0, 0); ISSUEK(1, 1); }
  else {
    const int sw = (q4 & 2) | ((g & 1) << 2);
    v4u fr[16];
#define QFRAGS(ks) do { _Pragma("unroll") for (int t = 0; t < 4; ++t) { \
        LAS unsigned char* a = L + L_W + w * WREG + (ks) * 4096 + g * 1024 + q4 * 128 + (p4 & 1) * 8 + (((2 * t + (p4 >> 1)) ^ sw) * 16); fr[(ks) * 4 + t] = tr_frag(a, a + 512); } } while (0)
    VMWAIT(8); QFRAGS(0); QFRAGS(1); ISSUEK(0, 0);
    VMWAIT(12); QFRAGS(2); ISSUEK(1, 1);
    barrier();
    f32x4 qa[4];
    v4u xb[4];
#pragma unroll
    for (int ks = 0; ks < 4; ++ks) xb[ks] = *(LAS v4u*)(S + L_XN + (c & 1) * 1024 + (128 * w + 32 * ks + 8 * g) * 2);
#pragma unroll
    for (int t = 0; t < 4; ++t) qa[t] = f32x4{0.f, 0.f, 0.f, 0.f};
#pragma unroll
    for (int ks = 0; ks < 3; ++ks)
#pragma unroll
      for (int t = 0; t < 4; ++t) qa[t] = mfma32(fr[ks * 4 + t], xb[ks], qa[t]);
    VMWAIT(12); STAMP(8); QFRAGS(3);
#pragma unroll
    for (int t = 0; t < 4; ++t) qa[t] = mfma32(fr[12 + t], xb[3], qa[t]);
    if (c < 2) {
#pragma unroll
      for (int t = 0; t < 4; ++t) *(LAS f32x4*)(S + L_RED + w * 512 + c * 256 + t * 64 + g * 16) = qa[t];
    }
    STAMP(1);
    barrier();
  }
  const int rem = nstep & 3;
  WUPTO(role == 1 ? WE_C : role == 2 ? WE_B : 0, PACE_N + 3);
#pragma unroll
  for (int d = 2; d < 12; ++d) { if (d & 1) VMWAIT(2 * PACE_N); else VMWAIT(PACE_N); ISSUEK(d, d); }
  STAMP(10);
  barrier();
  STAMP(2);
  const v4u qf = *(LAS v4u*)(L + L_QF + (c >> 2) * 256 + g * 64 + (c & 3) * 16);
  tag = __builtin_amdgcn_readfirstlane(*(LAS unsigned*)(L + L_TAG));

  LAS unsigned char* scp = L + L_SC + w * 5120 + g * 32 + (c >> 2) * 8;
  float m = -INFINITY;
  f32x4 sc;
#define QK(d) do { \
    _Pragma("unroll") for (int s = 0; s < 4; ++s) { const f32x4 dd = mfma32(kb[d][s], qf, f32x4{0.f, 0.f, 0.f, 0.f}); sc[s] = (c & 2) ? ((c & 1) ? dd[3] : dd[2]) : ((c & 1) ? dd[1] : dd[0]); } \
    asm volatile("" : "+v"(sc) :: "memory"); SB(); } while (0)
#define SCORE(d, j) do { \
    _Pragma("unroll") for (int s = 0; s < 4; ++s) { float t_ = sc[s]; t_ += dpp<XOR1>(t_); t_ += dpp<XOR2>(t_); sc[s] = t_; } \
    const unsigned r01 = cvtpk(sc[0], sc[1]), r23 = cvtpk(sc[2], sc[3]); \
    const unsigned t01 = cvtpk(bflo(r01) * SCALE, bfhi(r01) * SCALE), t23 = cvtpk(bflo(r23) * SCALE, bfhi(r23) * SCALE); \
    m = vmax(vmax(m, vmax(bflo(t01), bfhi(t01))), vmax(bflo(t23), bfhi(t23))); \
    *(LAS v2u*)(scp + (j) * 128) = v2u{t01, t23}; asm volatile("" : "+v"(m)); SB(); } while (0)
#pragma unroll
  for (int d = 0; d < 4; ++d) { QK(d); ISSUEK(d, 12 + rem + d); SCORE(d, d); }
#pragma unroll
  for (int d = 4; d < 12; ++d) { QK(d); SCORE(d, d); }
#pragma unroll
  for (int d = 12; d < 15; ++d) if (d - 12 < rem) ISSUEK(d, d);
  int j = 12 + rem;
#pragma unroll 1
  do {
#pragma unroll
    for (int d = 0; d < 4; ++d) { QK(d); ISSUEK(d, j + 4 + d); SCORE(d, j + d); }
    j += 4;
  } while (j + 4 < nstep);
#pragma unroll
  for (int d = 12; d < 15; ++d) if (d - 12 < rem) { QK(d); SCORE(d, d); }
  const int voff = g * 1024 + c * 16;
  v2u vl[7][4], vh[7][4];
#define ISSUEV(d, j) do { _Pragma("unroll") for (int s = 0; s < 4; ++s) { \
      if ((d) & 1) { const v4u t_ = __builtin_amdgcn_raw_buffer_load_b128(rv, voff + s * 256, sbase + (unsigned)(j) * 16384u, AUX_NT); vl[d][s] = v2u{t_[0], t_[1]}; vh[d][s] = v2u{t_[2], t_[3]}; } \
      else { vl[d][s] = __builtin_amdgcn_raw_buffer_load_b64(rv, voff + s * 256, sbase + (unsigned)(j) * 16384u, AUX_NT); NOHOIST(); SB(); \
             vh[d][s] = __builtin_amdgcn_raw_buffer_load_b64(rv, voff + s * 256 + 8, sbase + (unsigned)(j) * 16384u, AUX_NT); NOHOIST(); } } SB(); } while (0)
#pragma unroll
  for (int d = 0; d < 4; ++d) { QK(d); ISSUEV(d, d); SCORE(d, j + d); }
#pragma unroll
  for (int d = 4; d < 7; ++d) if (d - 4 < rem) ISSUEV(d, d);
  STAMP(9);
  m = sw16_max(m, m); m = sw32_max(m, m);
  const float mlog = m * LOG2E;
  const unsigned m0 = (c & 3) == 0 ? 0xffffu : (c & 3) == 1 ? 0xffff0000u : 0u;
  const unsigned m1 = (c & 3) == 2 ? 0xffffu : (c & 3) == 3 ? 0xffff0000u : 0u;
  f32x4 acc0 = {0.f, 0.f, 0.f, 0.f}, acc1 = {0.f, 0.f, 0.f, 0.f};
  float l = 0.f;
#define PV(d, j) do { \
    const v2u t_ = *(LAS v2u*)(scp + (j) * 128); \
    const float sv[4] = {bflo(t_[0]), bfhi(t_[0]), bflo(t_[1]), bfhi(t_[1])}; \
    v2u bb[4]; \
    _Pragma("unroll") for (int i = 0; i < 4; ++i) { const float p_ = __builtin_amdgcn_exp2f(fmaf(sv[i], LOG2E, -mlog)); const unsigned pp = cvtpk(p_, p_); l += bflo(pp); bb[i] = v2u{pp & m0, pp & m1}; } \
    SB(); \
    _Pragma("unroll") for (int i = 0; i < 4; ++i) { acc0 = mfma16(vl[d][i], bb[i], acc0); acc1 = mfma16(vh[d][i], bb[i], acc1); } \
    asm volatile("" : "+v"(acc0), "+v"(acc1) :: "memory"); SB(); } while (0)
#pragma unroll
  for (int d = 0; d < 4; ++d) { PV(d, d); ISSUEV(d, 4 + rem + d); }
#pragma unroll
  for (int d = 4; d < 7; ++d) if (d - 4 < rem) PV(d, d);
  j = 4 + rem;
#pragma unroll 1
  do {
#pragma unroll
    for (int d = 0; d < 4; ++d) { PV(d, j + d); ISSUEV(d, j + 4 + d); }
    j += 4;
  } while (j + 4 < nstep);
  asm volatile("v_mbcnt_lo_u32_b32 %0, -1, 0\n\tv_mbcnt_hi_u32_b32 %0, -1, %0" : "=v"(lane));
  c = lane & 15; g = lane >> 4; q4 = (lane >> 2) & 3; p4 = lane & 3;
  const int tid = w * 64 + lane;
#define PIN(x) asm volatile("" : "+v"(x))
  const unsigned tagh = (tag * 0x9E3779B1u) ^ ((tag * 0x9E3779B1u) >> 15);
  const int hd = 2 * (w & 1) + (lane >> 5);
  unsigned mw = lds0 + L_S + L_MRG + ((w * 4 + (c >> 2)) * 132 + 4 + 32 * g + (c & 3)) * 4, mr = lds0 + L_S + L_MRG + hd * 528;
  int pao = OFF_PART + ((b * HQ + kvh * 4 + hd) * NSPLIT + sp) * 512 + (lane & 31) * 16;
  unsigned mr2 = mr + 16 + (lane & 31) * 16;
  PIN(mw); PIN(mr); PIN(mr2); PIN(pao);
#pragma unroll
  for (int d = 0; d < 4; ++d) PV(d, j + d);
  STAMP(3);

  l = sw16_sum(l, l); l = sw32_sum(l, l);
  {
    LAS float* rec = (LAS float*)mw;
#pragma unroll
    for (int r = 0; r < 4; ++r) { rec[8 * r] = acc0[r]; rec[8 * r + 4] = acc1[r]; }
    if (g == 0 && (c & 3) == 0) *(LAS f32x2*)(rec - 4) = f32x2{m, l};
  }
  barrier();
  if (w < 2) {
    f32x2 ml[4]; f32x4 ov[4];
#pragma unroll
    for (int ww = 0; ww < 4; ++ww) { ml[ww] = *(LAS f32x2*)(mr + ww * 2112); ov[ww] = *(LAS f32x4*)(mr2 + ww * 2112); }
    const float Ml = vmax(vmax(ml[0][0], ml[1][0]), vmax(ml[2][0], ml[3][0])) * LOG2E;
    float wt[4];
#pragma unroll
    for (int ww = 0; ww < 4; ++ww) wt[ww] = __builtin_amdgcn_exp2f(fmaf(ml[ww][0], LOG2E, -Ml));
    const float Ls = fmaf(wt[1], ml[1][1], wt[0] * ml[0][1]) + fmaf(wt[3], ml[3][1], wt[2] * ml[2][1]);
    const f32x4 o = ((wt[0] * ov[0] + wt[1] * ov[1]) + (wt[2] * ov[2] + wt[3] * ov[3])) * __builtin_amdgcn_rcpf(Ls);
    const unsigned lse = f2u(Ml + __builtin_amdgcn_logf(Ls));
    ST128((v4u{cvtpk(o[0], o[1]), __builtin_amdgcn_perm(tagh, lse, 0x05040100u), cvtpk(o[2], o[3]), __builtin_amdgcn_perm(tagh, lse, 0x07060302u)}), pao);
  }
  STAMP(4);
  WUPTO(role == 1 ? 24 : role == 2 ? 16 : 0, 40);
  unsigned dm0 = (c & 7) == 0 ? 0xffffu : (c & 7) == 1 ? 0xffff0000u : 0u, dm1 = (c & 7) == 2 ? 0xffffu : (c & 7) == 3 ? 0xffff0000u : 0u,
           dm2 = (c & 7) == 4 ? 0xffffu : (c & 7) == 5 ? 0xffff0000u : 0u, dm3 = (c & 7) == 6 ? 0xffffu : (c & 7) == 7 ? 0xffff0000u : 0u;
  unsigned tb0 = tagh & 0xffu, tb1 = (tagh >> 8) & 0xffu, tb2 = (tagh >> 16) & 0xffu, tb3 = tagh >> 24;
#define PK24(x, tb) (((f2u(x) + 0x80u) & 0xffffff00u) | (tb))
#define PUBLISH_ACC(acc, off) ST128((v4u{PK24(acc[0], tb0), PK24(acc[1], tb1), PK24(acc[2], tb2), PK24(acc[3], tb3)}), (off))
#define UP24(x) u2f((x) & 0xffffff00u)

  if (role == 2) {
    unsigned wra = lds0 + L_W + w * WREG + lane * 16;
    PIN(wra);
    const int head = bi >> 1;
    const int po = OFF_PART + (((lane >> 5) * HQ + head) * NSPLIT + ((lane >> 2) & 7)) * 512 + (bi & 1) * 256 + w * 64 + (lane & 3) * 16;
    unsigned ow = lds0 + L_S + L_OB + ((g >> 1) * 4 + (g & 1) + ((lane & 4) >> 1)) * 64 + (4 * w + (lane & 3)) * 4, orr = lds0 + L_S + L_OB + ((c >> 3) * 4 + g) * 64;
    int so_ = OFF_P1 + bi * 4096 + tid * 16;
    f32x4 acc = {0.f, 0.f, 0.f, 0.f};
    PIN(ow); PIN(orr); PIN(so_); PIN(dm0); PIN(dm1); PIN(dm2); PIN(dm3); PIN(tb0); PIN(tb1); PIN(tb2); PIN(tb3); PIN(acc);
    v4u a0, a1;
    poll2<PD1, 16>(ws, po, po + 8 * 512, tagh, a0, a1);
    STAMP(5);
    v4u wr[16];
#pragma unroll
    for (int i = 0; i < 16; ++i) wr[i] = *(LAS v4u*)(wra + i * 1024);
    SB();
    const float l0 = u2f(__builtin_amdgcn_perm(a0[3], a0[1], 0x05040100u)), l1 = u2f(__builtin_amdgcn_perm(a1[3], a1[1], 0x05040100u));
    float xm = vmax(l0, l1);
    xm = vmax(vmax(xm, dpp<ROR4>(xm)), vmax(dpp<ROR8>(xm), dpp<ROR12>(xm)));
    xm = sw16_max(xm, xm);
    const float w0 = __builtin_amdgcn_exp2f(l0 - xm), w1 = __builtin_amdgcn_exp2f(l1 - xm);
    float u0 = sw16_sum(fmaf(w1, bflo(a1[0]), w0 * bflo(a0[0])), fmaf(w1, bfhi(a1[0]), w0 * bfhi(a0[0])));
    float u1 = sw16_sum(fmaf(w1, bflo(a1[2]), w0 * bflo(a0[2])), fmaf(w1, bfhi(a1[2]), w0 * bfhi(a0[2])));
    float ws_ = w0 + w1;
    ws_ = sw16_sum(ws_, ws_);
    u0 += dpp<ROR8>(u0); u1 += dpp<ROR8>(u1); ws_ += dpp<ROR8>(ws_);
    u0 += dpp<ROR4>(u0); u1 += dpp<ROR4>(u1); ws_ += dpp<ROR4>(ws_);
    const float uo = ((lane & 4) ? u1 : u0) * __builtin_amdgcn_rcpf(ws_);
    *(LAS unsigned*)ow = cvtpk(uo, uo);
    barrier();
    const v4u pv0 = *(LAS v4u*)orr, pv1 = *(LAS v4u*)(orr + 16), pv2 = *(LAS v4u*)(orr + 32), pv3 = *(LAS v4u*)(orr + 48);
    diag_chain_regs(acc, wr, pv0, pv1, dm0, dm1, dm2, dm3);
    diag_chain_regs(acc, wr + 8, pv2, pv3, dm0, dm1, dm2, dm3);
    mfma_drain(acc);
    PUBLISH_ACC(acc, so_);
    STAMP(6);
    poll_done(a0, a1);
  } else if (role == 3) {
    const int rb = rj & 1, col0 = (rj >> 1) * 32;
    unsigned xp = *(const GAS unsigned*)(x + rb * D + col0 + 2 * c);
    asm volatile("" : "+v"(xp));
    const int pp = lane >> 3;
    float h0, h1;
    unsigned rw_ = lds0 + L_S + L_RR + w * 128 + (8 * g + (lane & 7)) * 4, rr_ = lds0 + L_S + L_RR + c * 8;
    int hbo = OFF_HB + ((w & 1) * 4 + g) * 4096 + rb * 2048 + col0 * 4 + c * 8;
    GAS unsigned* op = (GAS unsigned*)(out + rb * D + col0 + 2 * c);
    PIN(rw_); PIN(rr_); PIN(hbo); PIN(op);
    {
      const int po = OFF_P1 + (16 * w + pp) * 4096 + rj * 128 + (lane & 7) * 16;
      v4u a0, a1;
      poll2<PD2, -1>(ws, po, po + 8 * 4096, tagh, a0, a1);
      STAMP(5);
      float u = sw16_sum(sw32_sum(UP24(a0[0]) + UP24(a1[0]), UP24(a0[2]) + UP24(a1[2])), sw32_sum(UP24(a0[1]) + UP24(a1[1]), UP24(a0[3]) + UP24(a1[3])));
      u += dpp<ROR8>(u);
      *(LAS float*)rw_ = u;
      barrier();
      const f32x2 s = (*(LAS f32x2*)rr_ + *(LAS f32x2*)(rr_ + 128)) + (*(LAS f32x2*)(rr_ + 256) + *(LAS f32x2*)(rr_ + 384));
      const unsigned at = cvtpk(s[0], s[1]);
      const unsigned hp = cvtpk(bflo(xp) + bflo(at), bfhi(xp) + bfhi(at));
      h0 = bflo(hp); h1 = bfhi(hp);
      if (w < 2) __builtin_amdgcn_raw_buffer_store_b64(v2u{hp, tag}, rw, hbo, 0, AUX_SC1);
      STAMP(6);
      poll_done(a0, a1);
    }
    {
      const int po = OFF_P2 + (14 * w + pp) * 4096 + rj * 128 + (lane & 7) * 16;
      v4u a0, b1;
      poll2<PD4, -1>(ws, po, po + (pp < 6 ? 8 * 4096 : 0), tagh, a0, b1);
      STAMP(7);
      const unsigned km = pp < 6 ? 0xffffff00u : 0u;
      float u = sw16_sum(sw32_sum(UP24(a0[0]) + u2f(b1[0] & km), UP24(a0[2]) + u2f(b1[2] & km)), sw32_sum(UP24(a0[1]) + u2f(b1[1] & km), UP24(a0[3]) + u2f(b1[3] & km)));
      u += dpp<ROR8>(u);
      *(LAS float*)(rw_ + 512) = u;
      barrier();
      if (tid < 16) {
        const f32x2 s = (*(LAS f32x2*)(rr_ + 512) + *(LAS f32x2*)(rr_ + 640)) + (*(LAS f32x2*)(rr_ + 768) + *(LAS f32x2*)(rr_ + 896));
        const unsigned dn = cvtpk(s[0], s[1]);
        *op = cvtpk(h0 + bflo(dn), h1 + bfhi(dn));
        if (rj == 0 && tid == 0) __builtin_amdgcn_raw_buffer_store_b32(tag, rw, OFF_EPOCH, 0, AUX_SC1);
      }
      STAMP(8);
      poll_done(a0, b1);
    }
  } else if (role == 1) {
    v4u fg[8], fu[8], wd[8];
    LAS unsigned char* fa = L + L_W + w * WREG + (8 * g + q4) * 64 + (p4 & 1) * 8;
    VMWAIT(16);
#pragma unroll
    for (int ks = 0; ks < 4; ++ks)
#pragma unroll
      for (int t = 0; t < 2; ++t) { LAS unsigned char* a = fa + 2048 * ks + (((2 * t + (p4 >> 1)) ^ ((g & 1) << 1)) * 16); fg[ks * 2 + t] = tr_frag(a, a + 256); }
    VMWAIT(8);
    STAMP(10);
#pragma unroll
    for (int ks = 0; ks < 4; ++ks)
#pragma unroll
      for (int t = 0; t < 2; ++t) { LAS unsigned char* a = fa + 8192 + 2048 * ks + (((2 * t + (p4 >> 1)) ^ ((g & 1) << 1)) * 16); fu[ks * 2 + t] = tr_frag(a, a + 256); }
    f32x4 ag[2], au[2];
    unsigned ro = lds0 + L_S + L_RED + ((c >> 2) & 1) * 1024 + ((c & 1) * 32 + (c & 2) * 8 + 4 * g) * 16 + w * 4;
    unsigned fo = lds0 + L_S + L_RED + ((c >> 3) * 32 + 4 * (c & 3) + g) * 16;
#define GU_MFMA(XB) do { _Pragma("unroll") for (int t = 0; t < 2; ++t) { ag[t] = f32x4{0.f, 0.f, 0.f, 0.f}; au[t] = f32x4{0.f, 0.f, 0.f, 0.f}; } \
      _Pragma("unroll") for (int ks = 0; ks < 4; ++ks) { \
        _Pragma("unroll") for (int t = 0; t < 2; ++t) { ag[t] = mfma32(fg[ks * 2 + t], XB, ag[t]); au[t] = mfma32(fu[ks * 2 + t], XB, au[t]); } } } while (0)
#define GU_STORE() do { const f32x4 sel = (c & 4) ? ((c & 2) ? au[1] : au[0]) : ((c & 2) ? ag[1] : ag[0]); \
      _Pragma("unroll") for (int i = 0; i < 4; ++i) *(LAS float*)(ro + i * 16) = sel[i]; } while (0)
#define SUM4(p_) ({ const f32x2 t_ = f32x2{p_[0], p_[1]} + f32x2{p_[2], p_[3]}; float r_ = t_[0] + t_[1]; PIN(r_); r_; })
    GU_MFMA((v4u{0x3f803f80u, 0x3f803f80u, 0x3f803f80u, 0x3f803f80u}));
    GU_STORE();
    barrier();
    float cg0, cg1, cu0, cu1;
    { const f32x4 p0 = *(LAS f32x4*)fo, p1 = *(LAS f32x4*)(fo + 256), p2 = *(LAS f32x4*)(fo + 1024), p3 = *(LAS f32x4*)(fo + 1280); cg0 = SUM4(p0); cg1 = SUM4(p1); cu0 = SUM4(p2); cu1 = SUM4(p3); }
    unsigned xw = lds0 + L_S + L_XN + (w & 1) * 1024 + lane * 16, xr = lds0 + L_S + L_XN + (c & 1) * 1024 + (128 * w + 8 * g) * 2, sw_ = lds0 + L_S + L_ST + w * 8, sr = lds0 + L_S + L_ST + (c >> 3) * 8;
    int so_ = OFF_P2 + ci * 4096 + tid * 16;
    f32x4 acc = {0.f, 0.f, 0.f, 0.f};
    unsigned wda = lds0 + L_W + w * WREG + 16384 + lane * 16;
    PIN(wda); PIN(ro); PIN(fo); PIN(xw); PIN(xr); PIN(sw_); PIN(sr); PIN(so_); PIN(dm0); PIN(dm1); PIN(dm2); PIN(dm3); PIN(tb0); PIN(tb1); PIN(tb2); PIN(tb3); PIN(acc);
    v4u ha = {0u, 0u, 0u, 0u}, hb = ha;
    if (w < 2) {
      const int ho = OFF_HB + xcd * 4096 + w * 2048 + lane * 32;
      poll2<PD3, 0>(ws, ho, ho + 16, tag, ha, hb);
      STAMP(5);
      *(LAS v4u*)xw = v4u{ha[0], ha[2], hb[0], hb[2]};
    } else VMWAIT(0);
    barrier();
    v4u xb[4];
#pragma unroll
    for (int ks = 0; ks < 4; ++ks) xb[ks] = *(LAS v4u*)(xr + 64 * ks);
#pragma unroll
    for (int i = 0; i < 8; ++i) wd[i] = *(LAS v4u*)(wda + i * 1024);
    __builtin_amdgcn_sched_barrier(0);
    const float pv = bflo(__builtin_amdgcn_readfirstlane(ha[0]));
    const float d0 = bflo(ha[0]) - pv, d1 = bfhi(ha[0]) - pv, d2 = bflo(ha[2]) - pv, d3 = bfhi(ha[2]) - pv, d4 = bflo(hb[0]) - pv, d5 = bfhi(hb[0]) - pv, d6 = bflo(hb[2]) - pv, d7 = bfhi(hb[2]) - pv;
    float s1 = ((d0 + d1) + (d2 + d3)) + ((d4 + d5) + (d6 + d7));
    float s2 = (fmaf(d1, d1, d0 * d0) + fmaf(d3, d3, d2 * d2)) + (fmaf(d5, d5, d4 * d4) + fmaf(d7, d7, d6 * d6));
    __builtin_amdgcn_sched_barrier(0);
    GU_MFMA(xb[ks]);
    {
      float t = sw32_sum(s1, s2);
      t = sw16_sum(t, t); t += dpp<ROR8>(t); t += dpp<ROR4>(t); t += dpp<XOR2>(t); t += dpp<XOR1>(t);
      const float md = u2f(__builtin_amdgcn_readlane(f2u(t), 0)) * (1.0f / D), q2 = u2f(__builtin_amdgcn_readlane(f2u(t), 32)) * (1.0f / D);
      const float var = vmax(fmaf(-md, md, q2), 0.f);
      const float rs = __builtin_amdgcn_rcpf(rbf(__builtin_amdgcn_sqrtf(rbf(rbf(var) + 1e-5f))));
      *(LAS f32x2*)sw_ = f32x2{rbf(pv + md), rs};
    }
    GU_STORE();
    barrier();
    const f32x2 st = *(LAS f32x2*)sr;
    const f32x4 p0 = *(LAS f32x4*)fo, p1 = *(LAS f32x4*)(fo + 256), p2 = *(LAS f32x4*)(fo + 1024), p3 = *(LAS f32x4*)(fo + 1280);
    __builtin_amdgcn_sched_barrier(0);
    const float G0 = SUM4(p0), G1 = SUM4(p1), U0 = SUM4(p2), U1 = SUM4(p3);
    const unsigned gp = cvtpk(fmaf(-st[0], cg0, G0) * st[1], fmaf(-st[0], cg1, G1) * st[1]);
    const unsigned up = cvtpk(fmaf(-st[0], cu0, U0) * st[1], fmaf(-st[0], cu1, U1) * st[1]);
    const float g0 = bflo(gp), g1 = bfhi(gp);
    const unsigned sg = cvtpk(g0 * __builtin_amdgcn_rcpf(1.0f + __builtin_amdgcn_exp2f(-g0 * LOG2E)), g1 * __builtin_amdgcn_rcpf(1.0f + __builtin_amdgcn_exp2f(-g1 * LOG2E)));
    const float av0 = bflo(sg) * bflo(up), av1 = bfhi(sg) * bfhi(up);
    diag_chain_quad(acc, wd, cvtpk(av0, av0), cvtpk(av1, av1), dm0, dm1, dm2, dm3);
    mfma_drain(acc);
    PUBLISH_ACC(acc, so_);
    STAMP(6);
    poll_done(ha, hb);
  }
  DUMP_STAMPS();
}

extern "C" void kernel_launch(void* const* d_in, const int* in_sizes, int n_in, void* d_out, int out_size, void* d_ws, size_t ws_size, hipStream_t stream) {
  fused_block<<<NWG, NTHR, 0, stream>>>((const u16*)d_in[0], (const u16*)d_in[1], (const u16*)d_in[2], (const u16*)d_in[3], (unsigned char*)d_ws,
                                        (const u16*)d_in[4], (const u16*)d_in[6], (const u16*)d_in[7], (const u16*)d_in[8], (u16*)d_out, make_plan() TUNE_ARG);
}
```

```cpp
#pragma clang diagnostic ignored "-Wint-to-pointer-cast"
#include <hip/hip_runtime.h>
#include <hip/hip_bf16.h>
#include <cstdint>

typedef unsigned short u16;
typedef unsigned v4u __attribute__((ext_vector_type(4)));
typedef unsigned v2u __attribute__((ext_vector_type(2)));
typedef float f32x4 __attribute__((ext_vector_type(4)));
typedef float f32x2 __attribute__((ext_vector_type(2)));
typedef short s16x4 __attribute__((ext_vector_type(4)));
typedef __bf16 bf16x8_t __attribute__((ext_vector_type(8)));
typedef __bf16 bf16x2_t __attribute__((ext_vector_type(2)));
#define LAS __attribute__((address_space(3)))
#define GAS __attribute__((address_space(1)))

constexpr int D = 512, HQ = 32, HKV = 8, SK = 32768, DH = 128, F = 1792;
constexpr int NWG = 256, NTHR = 320, NSPLIT = 16;
struct Plan { unsigned e[32]; };
#define TUNE_PARAM
#define TUNE_ARG
#define ABL 0
#define TL_C 130
#define TL_CS 129
#define TL_B 128
#define TL_Q 124
#define TL_QS 122
#define T_WC 0
#define T_WB 0
#define T_L 3500
#define T_PD 150
#define T_P 480
static Plan make_plan() {
  Plan p;
  for (int b = 0; b < 2; ++b) {
    const int nc = 4 - b;
    int n[16], role[16], ix[16], used = 0, nn = 0;
    for (int sp = 0; sp < 16; ++sp) {
      if (sp < nc) { role[sp] = 1; ix[sp] = 2 * sp + b; n[sp] = sp == 0 ? TL_CS : TL_C; }
      else if (sp >= 4 && sp < 8) { role[sp] = 2; ix[sp] = 2 * (sp - 4) + b; n[sp] = TL_B; }
      else if (sp >= 12) { role[sp] = 4; ix[sp] = 2 * (sp - 12) + b; n[sp] = (ix[sp] >> 1) == 1 ? TL_QS : TL_Q; }
      else { role[sp] = (sp == 8 || sp == 9) ? 3 : 0; ix[sp] = role[sp] ? 2 * (sp - 8) + b : 0; n[sp] = 0; ++nn; }
      used += n[sp];
    }
    int rest = 2048 - used, t0 = 0;
    for (int sp = 0; sp < 16; ++sp) {
      if (n[sp] == 0) { n[sp] = (rest + nn - 1) / nn; rest -= n[sp]; --nn; }
      p.e[2 * sp + b] = (unsigned)t0 | ((unsigned)n[sp] << 11) | ((unsigned)role[sp] << 19) | ((unsigned)ix[sp] << 22);
      t0 += n[sp];
    }
  }
  return p;
}
#define HMASK 0x0F0F
#define HEAVY(d, s) ((HMASK >> (4 * ((d) & 3) + (s))) & 1)
#define NINST(d) (4 + __builtin_popcount((HMASK >> (4 * ((d) & 3))) & 15))
#define NLR 2
#define PD1 2
#define PD2 3
#define PD3 1
#define PD4 3
#define PSL 1
#define PACE_N 4
#define CODE_BYTES 31744
constexpr int NB = 64, NC = 56, NR = 64, NREP = 8;
constexpr int OFF_EPOCH = 0;
constexpr int OFF_HB = 0x190000;
constexpr int OFF_PART = 0x10000;
constexpr int OFF_P1 = 0x90000;
constexpr int OFF_P2 = 0xD0000;
constexpr int OFF_Q = 0x180000;
constexpr int WS_BYTES = 0x1A0000, OFF_DBG = 0x200000;
constexpr int AUX_NT = 2, AUX_SC1 = 16;
constexpr unsigned SPIN_CAP = 1u << 16;
constexpr int L_W = 0;
constexpr int WREG = 24576;
constexpr int L_QF = 4 * WREG;
constexpr int L_SC = L_QF + 1024;
constexpr int L_TAG = L_SC + 4 * 40 * 128;
constexpr int L_S = L_TAG + 16;
constexpr int L_XN = 0;
constexpr int L_RED = L_XN + 2048;
constexpr int L_MRG = L_RED + 2048;
constexpr int L_OB = L_MRG + 4 * 4 * 132 * 4;
constexpr int L_RR = L_OB + 512;
constexpr int L_ST = L_RR + 1024;
constexpr int S_BYTES = L_ST + 32;
constexpr int L_PF = L_S + S_BYTES;
constexpr int L_DBG = L_PF + 256;
constexpr int L_BYTES = L_DBG + 128;

__device__ __forceinline__ float bflo(unsigned v) { return __uint_as_float(v << 16); }
__device__ __forceinline__ float bfhi(unsigned v) { return __uint_as_float(v & 0xffff0000u); }
__device__ __forceinline__ unsigned cvtpk(float lo, float hi) { return __builtin_bit_cast(unsigned, __builtin_convertvector(f32x2{lo, hi}, bf16x2_t)); }
__device__ __forceinline__ float rbf(float x) { return bflo(cvtpk(x, x)); }
__device__ __forceinline__ bool tagged(v4u v, unsigned tag) { return (v[1] == tag) & (v[3] == tag); }
__device__ __forceinline__ float u2f(unsigned v) { return __uint_as_float(v); }
__device__ __forceinline__ unsigned f2u(float v) { return __float_as_uint(v); }

__device__ __forceinline__ float vmax(float a, float b) { return __builtin_elementwise_maximum(a, b); }
template <int CTRL> __device__ __forceinline__ float dpp(float x) { return __builtin_bit_cast(float, __builtin_amdgcn_mov_dpp(__builtin_bit_cast(int, x), CTRL, 0xf, 0xf, true)); }
template <int CTRL> __device__ __forceinline__ unsigned dppu(unsigned x) { return (unsigned)__builtin_amdgcn_mov_dpp((int)x, CTRL, 0xf, 0xf, true); }
constexpr int XOR1 = 0xB1, XOR2 = 0x4E, ROR8 = 0x128, ROR4 = 0x124, ROR12 = 0x12C;
__device__ __forceinline__ float sw32_sum(float a, float b) { auto r = __builtin_amdgcn_permlane32_swap(f2u(a), f2u(b), false, false); return u2f(r[0]) + u2f(r[1]); }
__device__ __forceinline__ float sw16_sum(float a, float b) { auto r = __builtin_amdgcn_permlane16_swap(f2u(a), f2u(b), false, false); return u2f(r[0]) + u2f(r[1]); }
__device__ __forceinline__ float sw32_max(float a, float b) { auto r = __builtin_amdgcn_permlane32_swap(f2u(a), f2u(b), false, false); return vmax(u2f(r[0]), u2f(r[1])); }
__device__ __forceinline__ float sw16_max(float a, float b) { auto r = __builtin_amdgcn_permlane16_swap(f2u(a), f2u(b), false, false); return vmax(u2f(r[0]), u2f(r[1])); }
__device__ __forceinline__ float wave_sum(float x) {
  x += dpp<XOR1>(x); x += dpp<XOR2>(x); x += dpp<ROR4>(x); x += dpp<ROR8>(x);
  x = sw16_sum(x, x); return sw32_sum(x, x);
}
__device__ __forceinline__ f32x4 mfma32(v4u a, v4u b, f32x4 c) { return __builtin_amdgcn_mfma_f32_16x16x32_bf16(__builtin_bit_cast(bf16x8_t, a), __builtin_bit_cast(bf16x8_t, b), c, 0, 0, 0); }
__device__ __forceinline__ f32x4 mfma16(v2u a, v2u b, f32x4 c) { return __builtin_amdgcn_mfma_f32_16x16x16bf16_1k(__builtin_bit_cast(s16x4, a), __builtin_bit_cast(s16x4, b), c, 0, 0, 0); }
__device__ __forceinline__ void barrier() { asm volatile("s_waitcnt lgkmcnt(0)\n\ts_barrier" ::: "memory"); }
__device__ __forceinline__ v4u tr_frag(LAS unsigned char* lo, LAS unsigned char* hi) {
  const v2u a = __builtin_bit_cast(v2u, __builtin_amdgcn_ds_read_tr16_b64_v4i16((LAS s16x4*)lo));
  const v2u b = __builtin_bit_cast(v2u, __builtin_amdgcn_ds_read_tr16_b64_v4i16((LAS s16x4*)hi));
  return v4u{a[0], a[1], b[0], b[1]};
}

#define AND4(T0, T1, T2, T3, SRC, MOD, SFX) "v_and_b32" SFX " " T0 ", " SRC ", %[m0]" MOD "\n\tv_and_b32" SFX " " T1 ", " SRC ", %[m1]" MOD "\n\tv_and_b32" SFX " " T2 ", " SRC ", %[m2]" MOD "\n\tv_and_b32" SFX " " T3 ", " SRC ", %[m3]" MOD "\n\t"
#define ANDA(SRC, MOD, SFX) AND4("v240", "v241", "v242", "v243", SRC, MOD, SFX)
#define ANDB(SRC, MOD, SFX) AND4("v244", "v245", "v246", "v247", SRC, MOD, SFX)
#define MFA(W) "v_mfma_f32_16x16x32_bf16 %[acc], " W ", v[240:243], %[acc]\n\t"
#define MFB(W) "v_mfma_f32_16x16x32_bf16 %[acc], " W ", v[244:247], %[acc]\n\t"
#define CHAIN_CLOB "v240", "v241", "v242", "v243", "v244", "v245", "v246", "v247"
#define CHAIN_W(W) [w0] "v"(W[0]), [w1] "v"(W[1]), [w2] "v"(W[2]), [w3] "v"(W[3]), [w4] "v"(W[4]), [w5] "v"(W[5]), [w6] "v"(W[6]), [w7] "v"(W[7])
__device__ __forceinline__ void diag_chain_regs(f32x4& acc, const v4u* W, v4u pa, v4u pb, unsigned m0, unsigned m1, unsigned m2, unsigned m3) {
  asm volatile(ANDA("%[p0]", "", "") ANDB("%[p1]", "", "") MFA("%[w0]") ANDA("%[p2]", "", "") MFB("%[w1]") ANDB("%[p3]", "", "") MFA("%[w2]") ANDA("%[p4]", "", "") MFB("%[w3]")
               ANDB("%[p5]", "", "") MFA("%[w4]") ANDA("%[p6]", "", "") MFB("%[w5]") ANDB("%[p7]", "", "") MFA("%[w6]") "s_nop 0\n\t" MFB("%[w7]")
               : [acc] "+v"(acc)
               : CHAIN_W(W), [p0] "v"(pa[0]), [p1] "v"(pa[1]), [p2] "v"(pa[2]), [p3] "v"(pa[3]), [p4] "v"(pb[0]), [p5] "v"(pb[1]), [p6] "v"(pb[2]), [p7] "v"(pb[3]),
                 [m0] "v"(m0), [m1] "v"(m1), [m2] "v"(m2), [m3] "v"(m3)
               : CHAIN_CLOB);
}
#define QP(t) " quad_perm:[" #t "," #t "," #t "," #t "] row_mask:0xf bank_mask:0xf"
__device__ __forceinline__ void diag_chain_quad(f32x4& acc, const v4u* W, unsigned pa, unsigned pb, unsigned m0, unsigned m1, unsigned m2, unsigned m3) {
  asm volatile("s_nop 1\n\t" ANDA("%[pa]", QP(0), "_dpp") ANDB("%[pa]", QP(1), "_dpp") MFA("%[w0]") ANDA("%[pa]", QP(2), "_dpp") MFB("%[w1]") ANDB("%[pa]", QP(3), "_dpp") MFA("%[w2]")
               ANDA("%[pb]", QP(0), "_dpp") MFB("%[w3]") ANDB("%[pb]", QP(1), "_dpp") MFA("%[w4]") ANDA("%[pb]", QP(2), "_dpp") MFB("%[w5]") ANDB("%[pb]", QP(3), "_dpp") MFA("%[w6]")
               "s_nop 0\n\t" MFB("%[w7]")
               : [acc] "+v"(acc)
               : CHAIN_W(W), [pa] "v"(pa), [pb] "v"(pb), [m0] "v"(m0), [m1] "v"(m1), [m2] "v"(m2), [m3] "v"(m3)
               : CHAIN_CLOB);
}
__device__ __forceinline__ void mfma_drain(f32x4& acc) { asm volatile("s_nop 7" : "+v"(acc)); }

__device__ __forceinline__ v4u ln_row(v4u xv) {
  float x[8];
#pragma unroll
  for (int i = 0; i < 4; ++i) { x[2 * i] = bflo(xv[i]); x[2 * i + 1] = bfhi(xv[i]); }
  const float mean = wave_sum(((x[0] + x[1]) + (x[2] + x[3])) + ((x[4] + x[5]) + (x[6] + x[7]))) * (1.0f / D);
  float vs = 0.f;
#pragma unroll
  for (int i = 0; i < 8; ++i) { const float d = x[i] - mean; vs = fmaf(d, d, vs); }
  const float var = wave_sum(vs) * (1.0f / D);
  const float mb = rbf(mean);
  const float rs = __builtin_amdgcn_rcpf(rbf(__builtin_amdgcn_sqrtf(rbf(rbf(var) + 1e-5f))));
  v4u r;
#pragma unroll
  for (int i = 0; i < 4; ++i) { const unsigned d = cvtpk(x[2 * i] - mb, x[2 * i + 1] - mb); r[i] = cvtpk(bflo(d) * rs, bfhi(d) * rs); }
  return r;
}
__device__ __forceinline__ void glds16(const u16* gsrc, unsigned lds_dst) {
  unsigned keep;
  asm volatile("s_mov_b32 %0, m0\n\ts_mov_b32 m0, %2\n\ts_nop 0\n\tglobal_load_lds_dwordx4 %1, off nt\n\ts_mov_b32 m0, %0" : "=&s"(keep) : "v"(gsrc), "s"(lds_dst) : "memory");
}

__device__ __forceinline__ unsigned mixtag(unsigned t) { t *= 0x9E3779B1u; return t ^ (t >> 15); }
#define PK24(x, tb) (((f2u(x) + 0x80u) & 0xffffff00u) | (tb))
#define UP24(x) u2f((x) & 0xffffff00u)
__device__ __forceinline__ unsigned lowbytes(v4u v) { return __builtin_amdgcn_perm(v[1], v[0], 0x0c0c0400u) | __builtin_amdgcn_perm(v[3], v[2], 0x04000c0cu); }
template <int PD_, int TSH>
__device__ __forceinline__ void poll2(const unsigned char* ws, int o0, int o1, unsigned tag, v4u& r0, v4u& r1) {
  r0 = v4u{~tag, ~tag, ~tag, ~tag}; r1 = r0;
#define PASS() asm volatile("global_load_dwordx4 %0, %2, %4 sc1\n\tglobal_load_dwordx4 %1, %3, %4 sc1" : "+v"(r0), "+v"(r1) : "v"(o0), "v"(o1), "s"(ws) : "memory")
#pragma unroll
  for (int k = 0; k < PD_ - 1; ++k) { PASS(); __builtin_amdgcn_s_sleep(PSL); }
#pragma unroll 1
  for (unsigned k = 0; k < SPIN_CAP; ++k) {
    PASS();
    asm volatile("s_waitcnt vmcnt(%2)" : "+v"(r0), "+v"(r1) : "i"((PD_ - 1) * 2) : "memory");
    if (TSH == 16) { if (__all((__builtin_amdgcn_perm(r0[3], r0[1], 0x07060302u) == tag) & (__builtin_amdgcn_perm(r1[3], r1[1], 0x07060302u) == tag))) break; }
    else if (TSH < 0) { if (__all((lowbytes(r0) == tag) & (lowbytes(r1) == tag))) break; }
    else if (__all((((r0[1] ^ tag) | (r0[3] ^ tag) | (r1[1] ^ tag) | (r1[3] ^ tag)) >> (TSH < 0 ? 0 : TSH)) == 0u)) break;
    if (PD_ > 1) __builtin_amdgcn_s_sleep(PSL);
  }
#undef PASS
  asm volatile("s_nop 3 ; POLL_END" : "+v"(r0), "+v"(r1));
}
__device__ __forceinline__ void poll_done(v4u& r0, v4u& r1) { asm volatile("s_waitcnt vmcnt(0)" : "+v"(r0), "+v"(r1) :: "memory"); }

#define STAMP(i) do { } while (0)
#define DUMP_STAMPS() do { } while (0)
#define LD128(off) __builtin_amdgcn_raw_buffer_load_b128(rw, (off), 0, AUX_SC1)
#define ST128(v, off) __builtin_amdgcn_raw_buffer_store_b128((v), rw, (off), 0, AUX_SC1)
#define SPIN_LOOP _Pragma("unroll 1") for (unsigned spins = 0; spins < SPIN_CAP; ++spins)
#define NOHOIST() asm volatile("" ::: "memory")

__global__ void __launch_bounds__(NTHR, 2) fused_block(const u16* x_, const u16* kc_, const u16* vc_, const u16* wq_, unsigned char* ws_,
                                                       const u16* wo_, const u16* wgate_, const u16* wup_, const u16* wdown_, u16* out_, Plan plan_ TUNE_PARAM) {
  typedef unsigned long long u64x8 __attribute__((ext_vector_type(8)));
  typedef unsigned long long u64x2 __attribute__((ext_vector_type(2)));
  u64x8 ka; u64x2 kb2; unsigned pe;
  asm volatile("s_load_dwordx16 %0, %3, 0x0\n\ts_load_dwordx4 %1, %3, 0x40\n\ts_load_dword %2, %3, %4 offset:0x50"
               : "=&s"(ka), "=&s"(kb2), "=&s"(pe) : "s"(__builtin_amdgcn_kernarg_segment_ptr()), "s"((blockIdx.x >> 3) * 4));
  __shared__ __attribute__((aligned(16))) unsigned char smem[L_BYTES];
  LAS unsigned char* L = (LAS unsigned char*)smem;
  STAMP(0);
  const unsigned tstart = (unsigned)__builtin_amdgcn_s_memrealtime();
  const int wg = blockIdx.x, xcd = wg & 7, idx = wg >> 3;
  const int kvh = xcd, b = idx & 1, sp = idx >> 1;
  int lane = threadIdx.x & 63;
  const int w = __builtin_amdgcn_readfirstlane(threadIdx.x >> 6);
  int c = lane & 15, g = lane >> 4;
  if (w == 4 && !(ABL & 32)) {
    unsigned long long pc;
    asm volatile("s_getpc_b64 %0" : "=s"(pc));
    if ((lane * 32 + idx) * 128 < CODE_BYTES) {
      unsigned keep;
      asm volatile("s_mov_b32 %0, m0\n\ts_mov_b32 m0, %3\n\ts_nop 0\n\tglobal_load_lds_dword %1, %2\n\ts_mov_b32 m0, %0"
                   : "=&s"(keep) : "v"((lane * 32 + idx) * 128), "s"(pc & ~255ull), "s"((unsigned)(uintptr_t)L + L_PF) : "memory");
    }
  }
  int koff = (c >> 2) * 1024 + g * 64 + (c & 3) * 16;
  unsigned pbase = (unsigned)(b * HKV + kvh) * (SK * DH * 2);
  asm volatile("s_waitcnt lgkmcnt(0)" : "+s"(ka), "+s"(kb2), "+s"(pe), "+v"(koff), "+s"(pbase));
  const u16 *x = (const u16*)ka[0], *wq = (const u16*)ka[3], *wo = (const u16*)ka[5], *wgate = (const u16*)ka[6], *wup = (const u16*)ka[7], *wdown = (const u16*)kb2[0];
  unsigned char* ws = (unsigned char*)ka[4];
  u16* out = (u16*)kb2[1];
  const int tile0 = pe & 2047, ntile = (pe >> 11) & 255;
  const __amdgpu_buffer_rsrc_t rk = __builtin_amdgcn_make_buffer_rsrc((void*)(ka[1] + pbase), 0, SK * DH * 2, 0x00020000);
  const unsigned sbase = (unsigned)(tile0 + w) * 4096u;
  v4u kb[15][4];
#define SB() __builtin_amdgcn_sched_barrier(0)
#define ISSUEK(d, j) do { _Pragma("unroll") for (int s = 0; s < 4; ++s) { \
      if (!HEAVY(d, s)) kb[d][s] = __builtin_amdgcn_raw_buffer_load_b128(rk, koff + s * 256, sbase + (unsigned)(j) * 16384u, AUX_NT); \
      else { const v2u lo_ = __builtin_amdgcn_raw_buffer_load_b64(rk, koff + s * 256, sbase + (unsigned)(j) * 16384u, AUX_NT); NOHOIST(); SB(); \
             const v2u hi_ = __builtin_amdgcn_raw_buffer_load_b64(rk, koff + s * 256 + 8, sbase + (unsigned)(j) * 16384u, AUX_NT); NOHOIST(); \
             kb[d][s] = v4u{lo_[0], lo_[1], hi_[0], hi_[1]}; } } SB(); } while (0)
  const int role = (pe >> 19) & 7, rix = pe >> 22;
  const bool qrole = role == 4;
#define CI() ({ int r_ = rix; asm volatile("" : "+s"(r_)); const int cn_ = xcd * 5 + r_ - (xcd < 7 ? 2 : 0); (xcd < 7 && r_ < 2) ? 8 * xcd + 6 + r_ : cn_ + 2 * ((cn_ * 43) >> 8); })
  const int bi = xcd * 8 + rix, rj = xcd * 4 + rix, qi = bi;
  const __amdgpu_buffer_rsrc_t rw = __builtin_amdgcn_make_buffer_rsrc((void*)ws, 0, WS_BYTES, 0x00020000);
  const unsigned lds0 = (unsigned)(uintptr_t)L;
  LAS unsigned char* S = L + L_S;
#define WAIT_UNTIL(t, slp) do { while ((int)((unsigned)__builtin_amdgcn_s_memrealtime() - (tstart + (unsigned)(t))) < 0) __builtin_amdgcn_s_sleep(slp); } while (0)
  int q4 = (lane >> 2) & 3, p4 = lane & 3;
  constexpr float SCALE = 0.088388347648318440f, LOG2E = 1.4426950408889634f;
  unsigned tag;

  if (w == 4) {
    __builtin_amdgcn_s_setprio(3);
    if (qrole) {
      const v4u x0 = *(const GAS v4u*)(x + lane * 8), x1 = *(const GAS v4u*)(x + D + lane * 8);
      tag = __builtin_amdgcn_raw_buffer_load_b32(rw, OFF_EPOCH, 0, AUX_SC1) + 1u;
      const v4u n0 = ln_row(x0), n1 = ln_row(x1);
      *(LAS v4u*)(S + L_XN + lane * 16) = n0; *(LAS v4u*)(S + L_XN + 1024 + lane * 16) = n1;
      STAMP(9);
      barrier();
      barrier();
      const unsigned th = mixtag(tag);
      const f32x4 s = (*(LAS f32x4*)(S + L_MRG + lane * 16) + *(LAS f32x4*)(S + L_MRG + 1024 + lane * 16)) + (*(LAS f32x4*)(S + L_MRG + 2048 + lane * 16) + *(LAS f32x4*)(S + L_MRG + 3072 + lane * 16));
      ST128((v4u{PK24(s[0], th & 0xffu), PK24(s[1], (th >> 8) & 0xffu), PK24(s[2], (th >> 16) & 0xffu), PK24(s[3], th >> 24)}),
            OFF_Q + (((qi & 1) * 2 + (lane >> 5)) * 4096 + (qi >> 1) * 128) * 4 + (lane & 31) * 16);
    } else {
      tag = __builtin_amdgcn_raw_buffer_load_b32(rw, OFF_EPOCH, 0, AUX_SC1) + 1u;
    }
    STAMP(1);
    if (lane == 0) *(LAS unsigned*)(L + L_TAG) = tag;
    {
      const int qo = OFF_Q + (b * 4096 + kvh * 512) * 4 + lane * 16, qo2 = qo + 32768;
      const unsigned th = mixtag(tag);
      v4u q0 = {~th, ~th, ~th, ~th}, q1 = q0, q2 = q0, q3 = q0;
      WAIT_UNTIL(T_P, 2);
#define QOK() __all((lowbytes(q0) == th) & (lowbytes(q1) == th) & (lowbytes(q2) == th) & (lowbytes(q3) == th))
      bool got = false;
#pragma unroll 1
      for (unsigned k = 0; k < SPIN_CAP && !got; ++k) {
        asm volatile("s_waitcnt vmcnt(12)\n\tglobal_load_dwordx4 %0, %4, %6 sc1\n\tglobal_load_dwordx4 %1, %4, %6 offset:1024 sc1\n\tglobal_load_dwordx4 %2, %5, %6 sc1\n\tglobal_load_dwordx4 %3, %5, %6 offset:1024 sc1"
                     : "+v"(q0), "+v"(q1), "+v"(q2), "+v"(q3) : "v"(qo), "v"(qo2), "s"(ws) : "memory");
        const unsigned nxt = (unsigned)__builtin_amdgcn_s_memrealtime() + T_PD;
        do {
          __builtin_amdgcn_s_sleep(1);
          asm volatile("" : "+v"(q0), "+v"(q1), "+v"(q2), "+v"(q3));
          got = QOK();
        } while (!got && (int)((unsigned)__builtin_amdgcn_s_memrealtime() - nxt) < 0);
      }
      asm volatile("s_nop 3" : "+v"(q0), "+v"(q1), "+v"(q2), "+v"(q3));
      *(LAS v2u*)(L + L_QF + lane * 8) = v2u{cvtpk(UP24(q0[0]) + UP24(q2[0]), UP24(q0[1]) + UP24(q2[1])), cvtpk(UP24(q0[2]) + UP24(q2[2]), UP24(q0[3]) + UP24(q2[3]))};
      *(LAS v2u*)(L + L_QF + 512 + lane * 8) = v2u{cvtpk(UP24(q1[0]) + UP24(q3[0]), UP24(q1[1]) + UP24(q3[1])), cvtpk(UP24(q1[2]) + UP24(q3[2]), UP24(q1[3]) + UP24(q3[3]))};
      STAMP(2);
      barrier();
      asm volatile("s_waitcnt vmcnt(0) ; QPOLL_DONE" : "+v"(q0), "+v"(q1), "+v"(q2), "+v"(q3) :: "memory");
    }
    if (!(ABL & 64)) {
      const int ci = CI();
      const unsigned long long vb_ = ka[2] + pbase + (unsigned)tile0 * 4096u, ke_ = ka[1] + pbase + (unsigned)(tile0 + ntile) * 4096u - 128u;
      const unsigned long long wa_ = role == 1 ? ka[6] + ci * 64 : role == 2 ? ka[5] + bi * 65536 : kb2[1];
      const unsigned long long ad = lane == 0 ? vb_ : lane == 1 ? vb_ + (unsigned)ntile * 4096u - 128u : lane == 2 ? ke_ : lane == 3 ? wa_
                                  : lane == 4 ? wa_ + 511 * F * 2 : lane == 5 ? ka[7] + ci * 64 : lane == 6 ? ka[7] + ci * 64 + 511 * F * 2 : kb2[0] + ci * 32768;
      if (lane < (role == 1 ? 8 : 4)) {
        unsigned keep;
        asm volatile("s_mov_b32 %0, m0\n\ts_mov_b32 m0, %2\n\ts_nop 0\n\tglobal_load_lds_dword %1, off nt\n\ts_mov_b32 m0, %0" : "=&s"(keep) : "v"(ad), "s"(lds0 + L_PF) : "memory");
      }
    }
    asm volatile("s_waitcnt vmcnt(0)" ::: "memory");
    return;
  }
  const int nstep = (ntile - w + 3) >> 2;
  const __amdgpu_buffer_rsrc_t rv = __builtin_amdgcn_make_buffer_rsrc((void*)(ka[2] + pbase), 0, SK * DH * 2, 0x00020000);
#define VMWAIT(n) do { asm volatile("s_waitcnt vmcnt(%0)" :: "i"(n) : "memory"); SB(); } while (0)
#define RELANE() do { asm volatile("v_mbcnt_lo_u32_b32 %0, -1, 0\n\tv_mbcnt_hi_u32_b32 %0, -1, %0" : "=v"(lane)); c = lane & 15; g = lane >> 4; } while (0)
  int wp = 0;
#define WUPTO(n, pace) do { _Pragma("unroll 1") while (wp < (n)) { \
      const u16* src_ = role == 2 ? wo + (size_t)(bi * 64 + 4 * wp + g) * D + 128 * w + 8 * c \
                      : wp < 16 ? (wp < 8 ? wgate : wup) + (size_t)(128 * w + 16 * (wp & 7) + (lane >> 2)) * F + ci * 32 + (((lane & 3) ^ ((lane >> 4) & 2)) * 8) \
                                : wdown + (size_t)(ci * 32 + 4 * (wp & 7) + g) * D + 128 * w + 8 * c; \
      VMWAIT(pace); glds16(src_, lds0 + L_W + w * WREG + wp * 1024); ++wp; } } while (0)
  if (qrole) {
    const u16* p = wq + (size_t)(256 * (qi & 1) + 64 * w + g) * (HQ * DH) + (qi >> 1) * 128;
    const int ch0 = (c ^ (2 * g)) * 8;
#pragma unroll 1
    for (int t = 0; t < 16; t += 2) {
      const int ch = ch0 ^ ((t & 2) * 32);
      glds16(p + ch, lds0 + L_W + w * WREG + t * 1024);
      glds16(p + 4 * (HQ * DH) + ch, lds0 + L_W + w * WREG + t * 1024 + 1024);
      p += 8 * (HQ * DH);
    }
    STAMP(7);
  }
  STAMP(11);
  if (!qrole) { ISSUEK(0, 0); ISSUEK(1, 1); }
  else {
    const int sw = 2 * q4 + 8 * (g & 1);
    v4u fr[16];
#define QFRAGS(ks) do { _Pragma("unroll") for (int t = 0; t < 8; ++t) { \
        LAS unsigned char* a = L + L_W + w * WREG + (ks) * 8192 + g * 2048 + q4 * 256 + (p4 & 1) * 8 + (((2 * t + (p4 >> 1)) ^ sw) * 16); fr[(ks) * 8 + t] = tr_frag(a, a + 1024); } } while (0)
    VMWAIT(8); QFRAGS(0); ISSUEK(0, 0);
    VMWAIT(4 + NINST(0)); ISSUEK(1, 1);
    barrier();
    f32x4 qa[8];
    const v4u xb0 = *(LAS v4u*)(S + L_XN + (c & 1) * 1024 + (256 * (qi & 1) + 64 * w + 8 * g) * 2), xb1 = *(LAS v4u*)(S + L_XN + (c & 1) * 1024 + (256 * (qi & 1) + 64 * w + 32 + 8 * g) * 2);
#pragma unroll
    for (int t = 0; t < 8; ++t) qa[t] = mfma32(fr[t], xb0, f32x4{0.f, 0.f, 0.f, 0.f});
    VMWAIT(NINST(0) + NINST(1)); STAMP(8); QFRAGS(1);
#pragma unroll
    for (int t = 0; t < 8; ++t) qa[t] = mfma32(fr[8 + t], xb1, qa[t]);
    if (c < 2) {
#pragma unroll
      for (int t = 0; t < 8; ++t) *(LAS f32x4*)(S + L_MRG + w * 1024 + c * 512 + t * 64 + g * 16) = qa[t];
    }
    STAMP(1);
    barrier();
  }
  const int rem = nstep & 3;
#pragma unroll
  for (int d = 2; d < 12; ++d) { VMWAIT(NINST(d - 1)); ISSUEK(d, d); }
  STAMP(10);
  barrier();
  STAMP(2);
  const v4u qf = *(LAS v4u*)(L + L_QF + (c >> 2) * 256 + g * 64 + (c & 3) * 16);
  tag = __builtin_amdgcn_readfirstlane(*(LAS unsigned*)(L + L_TAG));

  LAS unsigned char* scp = L + L_SC + w * 5120 + g * 32 + (c >> 2) * 8;
  float m = -INFINITY;
  f32x4 sc;
#define QK(d) do { \
    _Pragma("unroll") for (int s = 0; s < 4; ++s) { const f32x4 dd = mfma32(kb[d][s], qf, f32x4{0.f, 0.f, 0.f, 0.f}); sc[s] = (c & 2) ? ((c & 1) ? dd[3] : dd[2]) : ((c & 1) ? dd[1] : dd[0]); } \
    asm volatile("" : "+v"(sc) :: "memory"); SB(); } while (0)
#define SCORE(d, j) do { \
    _Pragma("unroll") for (int s = 0; s < 4; ++s) { float t_ = sc[s]; t_ += dpp<XOR1>(t_); t_ += dpp<XOR2>(t_); sc[s] = t_; } \
    const unsigned r01 = cvtpk(sc[0], sc[1]), r23 = cvtpk(sc[2], sc[3]); \
    const unsigned t01 = cvtpk(bflo(r01) * SCALE, bfhi(r01) * SCALE), t23 = cvtpk(bflo(r23) * SCALE, bfhi(r23) * SCALE); \
    m = vmax(vmax(m, vmax(bflo(t01), bfhi(t01))), vmax(bflo(t23), bfhi(t23))); \
    *(LAS v2u*)(scp + (j) * 128) = v2u{t01, t23}; asm volatile("" : "+v"(m)); SB(); } while (0)
#pragma unroll
  for (int d = 0; d < 4; ++d) { QK(d); ISSUEK(d, 12 + rem + d); SCORE(d, d); }
#pragma unroll
  for (int d = 4; d < 12; ++d) { QK(d); SCORE(d, d); }
#pragma unroll
  for (int d = 12; d < 15; ++d) if (d - 12 < rem) ISSUEK(d, d);
  int j = 12 + rem;
#pragma unroll 1
  do {
#pragma unroll
    for (int d = 0; d < 4; ++d) { QK(d); ISSUEK(d, j + 4 + d); SCORE(d, j + d); }
    j += 4;
  } while (j + 4 < nstep);
#pragma unroll
  for (int d = 12; d < 15; ++d) if (d - 12 < rem) { QK(d); SCORE(d, d); }
  const int voff = g * 1024 + c * 16;
  v2u vl[7][4], vh[7][4];
#define ISSUEV(d, j) do { _Pragma("unroll") for (int s = 0; s < 4; ++s) { \
      if (!HEAVY(d, s)) { const v4u t_ = __builtin_amdgcn_raw_buffer_load_b128(rv, voff + s * 256, sbase + (unsigned)(j) * 16384u, AUX_NT); vl[d][s] = v2u{t_[0], t_[1]}; vh[d][s] = v2u{t_[2], t_[3]}; } \
      else { vl[d][s] = __builtin_amdgcn_raw_buffer_load_b64(rv, voff + s * 256, sbase + (unsigned)(j) * 16384u, AUX_NT); NOHOIST(); SB(); \
             vh[d][s] = __builtin_amdgcn_raw_buffer_load_b64(rv, voff + s * 256 + 8, sbase + (unsigned)(j) * 16384u, AUX_NT); NOHOIST(); } } SB(); } while (0)
#pragma unroll
  for (int d = 0; d < 4; ++d) { QK(d); ISSUEV(d, d); SCORE(d, j + d); }
#pragma unroll
  for (int d = 4; d < 7; ++d) if (d - 4 < rem) ISSUEV(d, d);
  STAMP(9);
  m = sw16_max(m, m); m = sw32_max(m, m);
  const float mlog = m * LOG2E;
  const unsigned m0 = (c & 3) == 0 ? 0xffffu : (c & 3) == 1 ? 0xffff0000u : 0u;
  const unsigned m1 = (c & 3) == 2 ? 0xffffu : (c & 3) == 3 ? 0xffff0000u : 0u;
  f32x4 acc0 = {0.f, 0.f, 0.f, 0.f}, acc1 = {0.f, 0.f, 0.f, 0.f};
  float l = 0.f;
#define PCALC(bb, j) do { \
    const v2u t_ = *(LAS v2u*)(scp + (j) * 128); \
    const float sv[4] = {bflo(t_[0]), bfhi(t_[0]), bflo(t_[1]), bfhi(t_[1])}; \
    _Pragma("unroll") for (int i = 0; i < 4; ++i) { const float p_ = __builtin_amdgcn_exp2f(fmaf(sv[i], LOG2E, -mlog)); const unsigned pp = cvtpk(p_, p_); l += bflo(pp); bb[i] = v2u{pp & m0, pp & m1}; } } while (0)
#define PMFMA(d, bb) do { SB(); \
    _Pragma("unroll") for (int i = 0; i < 4; ++i) { acc0 = mfma16(vl[d][i], bb[i], acc0); acc1 = mfma16(vh[d][i], bb[i], acc1); } \
    asm volatile("" : "+v"(acc0), "+v"(acc1) :: "memory"); SB(); } while (0)
#define PV(d, j) do { v2u bb_[4]; PCALC(bb_, j); PMFMA(d, bb_); } while (0)
#pragma unroll
  for (int d = 0; d < 4; ++d) { PV(d, d); ISSUEV(d, 4 + rem + d); }
#pragma unroll
  for (int d = 4; d < 7; ++d) if (d - 4 < rem) PV(d, d);
  j = 4 + rem;
#pragma unroll 1
  do {
#pragma unroll
    for (int d = 0; d < 4; ++d) { PV(d, j + d); ISSUEV(d, j + 4 + d); }
    j += 4;
  } while (j + 4 + 4 * NLR < nstep);
  unsigned bal[2] = {0u, 0u};
  const int blo = (lane & 31) * 128;
#pragma unroll
  for (int r = 0; r < NLR; ++r) {
#pragma unroll
    for (int d = 0; d < 4; ++d) {
      if (r > 0 && !(d & 1)) asm volatile("" :: "v"(bal[d >> 1]));
      PV(d, j + d);
#pragma unroll
      for (int s = 0; s < 4; ++s) { const v4u t_ = __builtin_amdgcn_raw_buffer_load_b128(rv, voff + s * 256, sbase + (unsigned)(j + 4 + d) * 16384u, AUX_NT); vl[d][s] = v2u{t_[0], t_[1]}; vh[d][s] = v2u{t_[2], t_[3]}; }
      SB();
      if (!(d & 1)) {
        const bool late = (int)((unsigned)__builtin_amdgcn_s_memrealtime() - (tstart + T_L)) > 0;
        bal[d >> 1] = __builtin_amdgcn_raw_buffer_load_b32(rv, late ? 0 : blo, sbase + (unsigned)(j + 4 + d) * 16384u, AUX_NT); SB();
      }
    }
    j += 4;
  }
  asm volatile("v_mbcnt_lo_u32_b32 %0, -1, 0\n\tv_mbcnt_hi_u32_b32 %0, -1, %0" : "=v"(lane));
  c = lane & 15; g = lane >> 4; q4 = (lane >> 2) & 3; p4 = lane & 3;
  const int tid = w * 64 + lane;
#define PIN(x) asm volatile("" : "+v"(x))
  const unsigned tagh = mixtag(tag);
  v2u lb[4][4];
#pragma unroll
  for (int d = 0; d < 4; ++d) PCALC(lb[d], j + d);
  l = sw16_sum(l, l); l = sw32_sum(l, l);
  *(LAS f32x2*)(S + L_RED + (c >> 2) * 32 + w * 8) = f32x2{mlog, l};
  barrier();
  const int hd = 2 * (w & 1) + (lane >> 5);
  float fw; unsigned t1w, t3w;
  {
#define MERGE_ML(h, M_, L_) do { const f32x4 u0_ = *(LAS f32x4*)(S + L_RED + (h) * 32), u1_ = *(LAS f32x4*)(S + L_RED + (h) * 32 + 16); \
      M_ = vmax(vmax(u0_[0], u0_[2]), vmax(u1_[0], u1_[2])); \
      L_ = fmaf(__builtin_amdgcn_exp2f(u0_[2] - M_), u0_[3], __builtin_amdgcn_exp2f(u0_[0] - M_) * u0_[1]) + fmaf(__builtin_amdgcn_exp2f(u1_[2] - M_), u1_[3], __builtin_amdgcn_exp2f(u1_[0] - M_) * u1_[1]); } while (0)
    float M1, L1, M2, L2;
    MERGE_ML(c >> 2, M1, L1); MERGE_ML(hd, M2, L2);
    fw = __builtin_amdgcn_exp2f(mlog - M1) * __builtin_amdgcn_rcpf(L1);
    const unsigned lse = f2u(M2 + __builtin_amdgcn_logf(L2));
    t1w = __builtin_amdgcn_perm(tagh, lse, 0x05040100u); t3w = __builtin_amdgcn_perm(tagh, lse, 0x07060302u);
  }
  unsigned mw = lds0 + L_S + L_MRG + w * 2048 + lane * 16, mr = lds0 + L_S + L_MRG + ((lane >> 2) & 1) * 1024 + (((lane >> 3) & 3) * 16 + hd * 4 + (lane & 3)) * 16;
  int pao = OFF_PART + ((b * HQ + kvh * 4 + hd) * NSPLIT + sp) * 512 + (lane & 31) * 16;
  PIN(mw); PIN(mr); PIN(pao); PIN(fw); PIN(t1w); PIN(t3w);
#pragma unroll
  for (int d = 0; d < 4; ++d) { PMFMA(d, lb[d]); if (d == 2) asm volatile("" :: "v"(bal[0]), "v"(bal[1])); }
  STAMP(3);
  *(LAS f32x4*)mw = acc0 * fw; *(LAS f32x4*)(mw + 1024) = acc1 * fw;
  barrier();
  if (w < 2) {
    const f32x4 o = (*(LAS f32x4*)mr + *(LAS f32x4*)(mr + 2048)) + (*(LAS f32x4*)(mr + 4096) + *(LAS f32x4*)(mr + 6144));
    ST128((v4u{cvtpk(o[0], o[1]), t1w, cvtpk(o[2], o[3]), t3w}), pao);
  }
  STAMP(4);
  const int ci = CI();
  if (role == 1 || role == 2) WAIT_UNTIL(role == 1 ? T_WC : T_WB, 2);
  WUPTO(role == 1 ? 24 : role == 2 ? 16 : 0, 40);
  unsigned dm0 = (c & 7) == 0 ? 0xffffu : (c & 7) == 1 ? 0xffff0000u : 0u, dm1 = (c & 7) == 2 ? 0xffffu : (c & 7) == 3 ? 0xffff0000u : 0u,
           dm2 = (c & 7) == 4 ? 0xffffu : (c & 7) == 5 ? 0xffff0000u : 0u, dm3 = (c & 7) == 6 ? 0xffffu : (c & 7) == 7 ? 0xffff0000u : 0u;
  unsigned tb0 = tagh & 0xffu, tb1 = (tagh >> 8) & 0xffu, tb2 = (tagh >> 16) & 0xffu, tb3 = tagh >> 24;
#define PUBLISH_ACC(acc, off) ST128((v4u{PK24(acc[0], tb0), PK24(acc[1], tb1), PK24(acc[2], tb2), PK24(acc[3], tb3)}), (off))

  if (role == 2) {
    unsigned wra = lds0 + L_W + w * WREG + lane * 16;
    PIN(wra);
    const int head = bi >> 1;
    const int po = OFF_PART + (((lane >> 5) * HQ + head) * NSPLIT + ((lane >> 2) & 7)) * 512 + (bi & 1) * 256 + w * 64 + (lane & 3) * 16;
    unsigned ow = lds0 + L_S + L_OB + ((g >> 1) * 4 + (lane & 3)) * 64 + (8 * (w >> 1) + 2 * ((g & 1) + ((lane & 4) >> 1)) + (w & 1)) * 4, orr = lds0 + L_S + L_OB + ((c >> 3) * 4 + g) * 64;
    int so_ = OFF_P1 + bi * 4096 + tid * 16;
    f32x4 acc = {0.f, 0.f, 0.f, 0.f};
    PIN(ow); PIN(orr); PIN(so_); PIN(dm0); PIN(dm1); PIN(dm2); PIN(dm3); PIN(tb0); PIN(tb1); PIN(tb2); PIN(tb3); PIN(acc);
    v4u a0, a1;
    poll2<PD1, 16>(ws, po, po + 8 * 512, tagh, a0, a1);
    STAMP(5);
    v4u wr[16];
#pragma unroll
    for (int i = 0; i < 16; ++i) wr[i] = *(LAS v4u*)(wra + i * 1024);
    SB();
    const float l0 = u2f(__builtin_amdgcn_perm(a0[3], a0[1], 0x05040100u)), l1 = u2f(__builtin_amdgcn_perm(a1[3], a1[1], 0x05040100u));
    float xm = vmax(l0, l1);
    xm = vmax(vmax(xm, dpp<ROR4>(xm)), vmax(dpp<ROR8>(xm), dpp<ROR12>(xm)));
    xm = sw16_max(xm, xm);
    const float w0 = __builtin_amdgcn_exp2f(l0 - xm), w1 = __builtin_amdgcn_exp2f(l1 - xm);
    float u0 = sw16_sum(fmaf(w1, bflo(a1[0]), w0 * bflo(a0[0])), fmaf(w1, bfhi(a1[0]), w0 * bfhi(a0[0])));
    float u1 = sw16_sum(fmaf(w1, bflo(a1[2]), w0 * bflo(a0[2])), fmaf(w1, bfhi(a1[2]), w0 * bfhi(a0[2])));
    float ws_ = w0 + w1;
    ws_ = sw16_sum(ws_, ws_);
    u0 += dpp<ROR8>(u0); u1 += dpp<ROR8>(u1); ws_ += dpp<ROR8>(ws_);
    u0 += dpp<ROR4>(u0); u1 += dpp<ROR4>(u1); ws_ += dpp<ROR4>(ws_);
    const float uo = ((lane & 4) ? u1 : u0) * __builtin_amdgcn_rcpf(ws_);
    *(LAS unsigned*)ow = cvtpk(uo, uo);
    barrier();
    const v4u pv0 = *(LAS v4u*)orr, pv1 = *(LAS v4u*)(orr + 16), pv2 = *(LAS v4u*)(orr + 32), pv3 = *(LAS v4u*)(orr + 48);
    diag_chain_regs(acc, wr, pv0, pv1, dm0, dm1, dm2, dm3);
    diag_chain_regs(acc, wr + 8, pv2, pv3, dm0, dm1, dm2, dm3);
    mfma_drain(acc);
    PUBLISH_ACC(acc, so_);
    STAMP(6);
    poll_done(a0, a1);
  } else if (role == 3) {
    const int rb = rj & 1, col0 = (rj >> 1) * 32;
    unsigned xp = *(const GAS unsigned*)(x + rb * D + col0 + 2 * c);
    asm volatile("" : "+v"(xp));
    const int pp = lane >> 3;
    float h0, h1;
    unsigned rw_ = lds0 + L_S + L_RR + w * 128 + (8 * g + (lane & 7)) * 4, rr_ = lds0 + L_S + L_RR + c * 8;
    int hbo = OFF_HB + ((w & 1) * 4 + g) * 4096 + rb * 2048 + col0 * 4 + c * 8;
    GAS unsigned* op = (GAS unsigned*)(out + rb * D + col0 + 2 * c);
    PIN(rw_); PIN(rr_); PIN(hbo); PIN(op);
    {
      const int po = OFF_P1 + (16 * w + pp) * 4096 + rj * 128 + (lane & 7) * 16;
      v4u a0, a1;
      poll2<PD2, -1>(ws, po, po + 8 * 4096, tagh, a0, a1);
      STAMP(5);
      float u = sw16_sum(sw32_sum(UP24(a0[0]) + UP24(a1[0]), UP24(a0[2]) + UP24(a1[2])), sw32_sum(UP24(a0[1]) + UP24(a1[1]), UP24(a0[3]) + UP24(a1[3])));
      u += dpp<ROR8>(u);
      *(LAS float*)rw_ = u;
      barrier();
      const f32x2 s = (*(LAS f32x2*)rr_ + *(LAS f32x2*)(rr_ + 128)) + (*(LAS f32x2*)(rr_ + 256) + *(LAS f32x2*)(rr_ + 384));
      const unsigned at = cvtpk(s[0], s[1]);
      const unsigned hp = cvtpk(bflo(xp) + bflo(at), bfhi(xp) + bfhi(at));
      h0 = bflo(hp); h1 = bfhi(hp);
      if (w < 2) __builtin_amdgcn_raw_buffer_store_b64(v2u{hp, tag}, rw, hbo, 0, AUX_SC1);
      STAMP(6);
      poll_done(a0, a1);
    }
    {
      const int po = OFF_P2 + (14 * w + pp) * 4096 + rj * 128 + (lane & 7) * 16;
      v4u a0, b1;
      poll2<PD4, -1>(ws, po, po + (pp < 6 ? 8 * 4096 : 0), tagh, a0, b1);
      STAMP(7);
      const unsigned km = pp < 6 ? 0xffffff00u : 0u;
      float u = sw16_sum(sw32_sum(UP24(a0[0]) + u2f(b1[0] & km), UP24(a0[2]) + u2f(b1[2] & km)), sw32_sum(UP24(a0[1]) + u2f(b1[1] & km), UP24(a0[3]) + u2f(b1[3] & km)));
      u += dpp<ROR8>(u);
      *(LAS float*)(rw_ + 512) = u;
      barrier();
      if (tid < 16) {
        const f32x2 s = (*(LAS f32x2*)(rr_ + 512) + *(LAS f32x2*)(rr_ + 640)) + (*(LAS f32x2*)(rr_ + 768) + *(LAS f32x2*)(rr_ + 896));
        const unsigned dn = cvtpk(s[0], s[1]);
        *op = cvtpk(h0 + bflo(dn), h1 + bfhi(dn));
        if (rj == 0 && tid == 0) __builtin_amdgcn_raw_buffer_store_b32(tag, rw, OFF_EPOCH, 0, AUX_SC1);
      }
      STAMP(8);
      poll_done(a0, b1);
    }
  } else if (role == 1) {
    v4u fg[8], fu[8], wd[8];
    LAS unsigned char* fa = L + L_W + w * WREG + (8 * g + q4) * 64 + (p4 & 1) * 8;
    VMWAIT(16);
#pragma unroll
    for (int ks = 0; ks < 4; ++ks)
#pragma unroll
      for (int t = 0; t < 2; ++t) { LAS unsigned char* a = fa + 2048 * ks + (((2 * t + (p4 >> 1)) ^ ((g & 1) << 1)) * 16); fg[ks * 2 + t] = tr_frag(a, a + 256); }
    VMWAIT(8);
    STAMP(10);
#pragma unroll
    for (int ks = 0; ks < 4; ++ks)
#pragma unroll
      for (int t = 0; t < 2; ++t) { LAS unsigned char* a = fa + 8192 + 2048 * ks + (((2 * t + (p4 >> 1)) ^ ((g & 1) << 1)) * 16); fu[ks * 2 + t] = tr_frag(a, a + 256); }
    f32x4 ag[2], au[2];
    unsigned ro = lds0 + L_S + L_RED + ((c >> 2) & 1) * 1024 + ((c & 1) * 32 + (c & 2) * 8 + 4 * g) * 16 + w * 4;
    unsigned fo = lds0 + L_S + L_RED + ((c >> 3) * 32 + 4 * (c & 3) + g) * 16;
#define GU_MFMA(XB) do { _Pragma("unroll") for (int t = 0; t < 2; ++t) { ag[t] = f32x4{0.f, 0.f, 0.f, 0.f}; au[t] = f32x4{0.f, 0.f, 0.f, 0.f}; } \
      _Pragma("unroll") for (int ks = 0; ks < 4; ++ks) { \
        _Pragma("unroll") for (int t = 0; t < 2; ++t) { ag[t] = mfma32(fg[ks * 2 + t], XB, ag[t]); au[t] = mfma32(fu[ks * 2 + t], XB, au[t]); } } } while (0)
#define GU_STORE() do { const f32x4 sel = (c & 4) ? ((c & 2) ? au[1] : au[0]) : ((c & 2) ? ag[1] : ag[0]); \
      _Pragma("unroll") for (int i = 0; i < 4; ++i) *(LAS float*)(ro + i * 16) = sel[i]; } while (0)
#define SUM4(p_) ({ const f32x2 t_ = f32x2{p_[0], p_[1]} + f32x2{p_[2], p_[3]}; float r_ = t_[0] + t_[1]; PIN(r_); r_; })
    GU_MFMA((v4u{0x3f803f80u, 0x3f803f80u, 0x3f803f80u, 0x3f803f80u}));
    GU_STORE();
    barrier();
    float cg0, cg1, cu0, cu1;
    { const f32x4 p0 = *(LAS f32x4*)fo, p1 = *(LAS f32x4*)(fo + 256), p2 = *(LAS f32x4*)(fo + 1024), p3 = *(LAS f32x4*)(fo + 1280); cg0 = SUM4(p0); cg1 = SUM4(p1); cu0 = SUM4(p2); cu1 = SUM4(p3); }
    unsigned xw = lds0 + L_S + L_XN + (w & 1) * 1024 + lane * 16, xr = lds0 + L_S + L_XN + (c & 1) * 1024 + (128 * w + 8 * g) * 2, sw_ = lds0 + L_S + L_ST + w * 8, sr = lds0 + L_S + L_ST + (c >> 3) * 8;
    int so_ = OFF_P2 + ci * 4096 + tid * 16;
    f32x4 acc = {0.f, 0.f, 0.f, 0.f};
    unsigned wda = lds0 + L_W + w * WREG + 16384 + lane * 16;
    PIN(wda); PIN(ro); PIN(fo); PIN(xw); PIN(xr); PIN(sw_); PIN(sr); PIN(so_); PIN(dm0); PIN(dm1); PIN(dm2); PIN(dm3); PIN(tb0); PIN(tb1); PIN(tb2); PIN(tb3); PIN(acc);
    v4u ha = {0u, 0u, 0u, 0u}, hb = ha;
    if (w < 2) {
      const int ho = OFF_HB + xcd * 4096 + w * 2048 + lane * 32;
      poll2<PD3, 0>(ws, ho, ho + 16, tag, ha, hb);
      STAMP(5);
      *(LAS v4u*)xw = v4u{ha[0], ha[2], hb[0], hb[2]};
    } else VMWAIT(0);
    barrier();
    v4u xb[4];
#pragma unroll
    for (int ks = 0; ks < 4; ++ks) xb[ks] = *(LAS v4u*)(xr + 64 * ks);
#pragma unroll
    for (int i = 0; i < 8; ++i) wd[i] = *(LAS v4u*)(wda + i * 1024);
    __builtin_amdgcn_sched_barrier(0);
    const float pv = bflo(__builtin_amdgcn_readfirstlane(ha[0]));
    const float d0 = bflo(ha[0]) - pv, d1 = bfhi(ha[0]) - pv, d2 = bflo(ha[2]) - pv, d3 = bfhi(ha[2]) - pv, d4 = bflo(hb[0]) - pv, d5 = bfhi(hb[0]) - pv, d6 = bflo(hb[2]) - pv, d7 = bfhi(hb[2]) - pv;
    float s1 = ((d0 + d1) + (d2 + d3)) + ((d4 + d5) + (d6 + d7));
    float s2 = (fmaf(d1, d1, d0 * d0) + fmaf(d3, d3, d2 * d2)) + (fmaf(d5, d5, d4 * d4) + fmaf(d7, d7, d6 * d6));
    __builtin_amdgcn_sched_barrier(0);
    GU_MFMA(xb[ks]);
    {
      float t = sw32_sum(s1, s2);
      t = sw16_sum(t, t); t += dpp<ROR8>(t); t += dpp<ROR4>(t); t += dpp<XOR2>(t); t += dpp<XOR1>(t);
      const float md = u2f(__builtin_amdgcn_readlane(f2u(t), 0)) * (1.0f / D), q2 = u2f(__builtin_amdgcn_readlane(f2u(t), 32)) * (1.0f / D);
      const float var = vmax(fmaf(-md, md, q2), 0.f);
      const float rs = __builtin_amdgcn_rcpf(rbf(__builtin_amdgcn_sqrtf(rbf(rbf(var) + 1e-5f))));
      *(LAS f32x2*)sw_ = f32x2{rbf(pv + md), rs};
    }
    GU_STORE();
    barrier();
    const f32x2 st = *(LAS f32x2*)sr;
    const f32x4 p0 = *(LAS f32x4*)fo, p1 = *(LAS f32x4*)(fo + 256), p2 = *(LAS f32x4*)(fo + 1024), p3 = *(LAS f32x4*)(fo + 1280);
    __builtin_amdgcn_sched_barrier(0);
    const float G0 = SUM4(p0), G1 = SUM4(p1), U0 = SUM4(p2), U1 = SUM4(p3);
    const unsigned gp = cvtpk(fmaf(-st[0], cg0, G0) * st[1], fmaf(-st[0], cg1, G1) * st[1]);
    const unsigned up = cvtpk(fmaf(-st[0], cu0, U0) * st[1], fmaf(-st[0], cu1, U1) * st[1]);
    const float g0 = bflo(gp), g1 = bfhi(gp);
    const unsigned sg = cvtpk(g0 * __builtin_amdgcn_rcpf(1.0f + __builtin_amdgcn_exp2f(-g0 * LOG2E)), g1 * __builtin_amdgcn_rcpf(1.0f + __builtin_amdgcn_exp2f(-g1 * LOG2E)));
    const float av0 = bflo(sg) * bflo(up), av1 = bfhi(sg) * bfhi(up);
    diag_chain_quad(acc, wd, cvtpk(av0, av0), cvtpk(av1, av1), dm0, dm1, dm2, dm3);
    mfma_drain(acc);
    PUBLISH_ACC(acc, so_);
    STAMP(6);
    poll_done(ha, hb);
  }
  DUMP_STAMPS();
}

extern "C" void kernel_launch(void* const* d_in, const int* in_sizes, int n_in, void* d_out, int out_size, void* d_ws, size_t ws_size, hipStream_t stream) {
  fused_block<<<NWG, NTHR, 0, stream>>>((const u16*)d_in[0], (const u16*)d_in[1], (const u16*)d_in[2], (const u16*)d_in[3], (unsigned char*)d_ws,
                                        (const u16*)d_in[4], (const u16*)d_in[6], (const u16*)d_in[7], (const u16*)d_in[8], (u16*)d_out, make_plan() TUNE_ARG);
}
```

```cpp
#pragma clang diagnostic ignored "-Wint-to-pointer-cast"
#include <hip/hip_runtime.h>
#include <hip/hip_bf16.h>
#include <cstdint>

typedef unsigned short u16;
typedef unsigned v4u __attribute__((ext_vector_type(4)));
typedef unsigned v2u __attribute__((ext_vector_type(2)));
typedef float f32x4 __attribute__((ext_vector_type(4)));
typedef float f32x2 __attribute__((ext_vector_type(2)));
typedef short s16x4 __attribute__((ext_vector_type(4)));
typedef __bf16 bf16x8_t __attribute__((ext_vector_type(8)));
typedef __bf16 bf16x2_t __attribute__((ext_vector_type(2)));
#define LAS __attribute__((address_space(3)))
#define GAS __attribute__((address_space(1)))

constexpr int D = 512, HQ = 32, HKV = 8, SK = 32768, DH = 128, F = 1792;
constexpr int NWG = 256, NTHR = 320, NSPLIT = 16;
struct Plan { unsigned e[32]; };
#define TUNE_PARAM
#define TUNE_ARG
#define ABL 0
#define TL_C 130
#define TL_CS 129
#define TL_B 128
#define TL_Q 124
#define TL_QS 122
#define T_WC 0
#define T_WB 0
#define T_L 3500
#define T_PD 150
#define T_P 480
static Plan make_plan() {
  Plan p;
  for (int b = 0; b < 2; ++b) {
    const int nc = 4 - b;
    int n[16], role[16], ix[16], used = 0, nn = 0;
    for (int sp = 0; sp < 16; ++sp) {
      if (sp < nc) { role[sp] = 1; ix[sp] = 2 * sp + b; n[sp] = sp == 0 ? TL_CS : TL_C; }
      else if (sp >= 4 && sp < 8) { role[sp] = 2; ix[sp] = 2 * (sp - 4) + b; n[sp] = TL_B; }
      else if (sp >= 12) { role[sp] = 4; ix[sp] = 2 * (sp - 12) + b; n[sp] = (ix[sp] >> 1) == 1 ? TL_QS : TL_Q; }
      else { role[sp] = (sp == 8 || sp == 9) ? 3 : 0; ix[sp] = role[sp] ? 2 * (sp - 8) + b : 0; n[sp] = 0; ++nn; }
      used += n[sp];
    }
    int rest = 2048 - used, t0 = 0;
    for (int sp = 0; sp < 16; ++sp) {
      if (n[sp] == 0) { n[sp] = (rest + nn - 1) / nn; rest -= n[sp]; --nn; }
      p.e[2 * sp + b] = (unsigned)t0 | ((unsigned)n[sp] << 11) | ((unsigned)role[sp] << 19) | ((unsigned)ix[sp] << 22);
      t0 += n[sp];
    }
  }
  return p;
}
#define HMASK 0x0F0F
#define HEAVY(d, s) ((HMASK >> (4 * ((d) & 3) + (s))) & 1)
#define NINST(d) (4 + __builtin_popcount((HMASK >> (4 * ((d) & 3))) & 15))
#define NLR 2
#define PD1 2
#define PD2 3
#define PD3 1
#define PD4 3
#define PSL 1
#define PACE_N 4
#define CODE_BYTES 31744
constexpr int NB = 64, NC = 56, NR = 64, NREP = 8;
constexpr int OFF_EPOCH = 0;
constexpr int OFF_HB = 0x190000;
constexpr int OFF_PART = 0x10000;
constexpr int OFF_P1 = 0x90000;
constexpr int OFF_P2 = 0xD0000;
constexpr int OFF_Q = 0x180000;
constexpr int WS_BYTES = 0x1A0000, OFF_DBG = 0x200000;
constexpr int AUX_NT = 2, AUX_SC1 = 16;
constexpr unsigned SPIN_CAP = 1u << 16;
constexpr int L_W = 0;
constexpr int WREG = 24576;
constexpr int L_QF = 4 * WREG;
constexpr int L_SC = L_QF + 1024;
constexpr int L_TAG = L_SC + 4 * 40 * 128;
constexpr int L_S = L_TAG + 16;
constexpr int L_XN = 0;
constexpr int L_RED = L_XN + 2048;
constexpr int L_MRG = L_RED + 2048;
constexpr int L_OB = L_MRG + 4 * 4 * 132 * 4;
constexpr int L_RR = L_OB + 512;
constexpr int L_ST = L_RR + 1024;
constexpr int S_BYTES = L_ST + 32;
constexpr int L_PF = L_S + S_BYTES;
constexpr int L_DBG = L_PF + 256;
constexpr int L_BYTES = L_DBG + 128;

__device__ __forceinline__ float bflo(unsigned v) { return __uint_as_float(v << 16); }
__device__ __forceinline__ float bfhi(unsigned v) { return __uint_as_float(v & 0xffff0000u); }
__device__ __forceinline__ unsigned cvtpk(float lo, float hi) { return __builtin_bit_cast(unsigned, __builtin_convertvector(f32x2{lo, hi}, bf16x2_t)); }
__device__ __forceinline__ float rbf(float x) { return bflo(cvtpk(x, x)); }
__device__ __forceinline__ bool tagged(v4u v, unsigned tag) { return (v[1] == tag) & (v[3] == tag); }
__device__ __forceinline__ float u2f(unsigned v) { return __uint_as_float(v); }
__device__ __forceinline__ unsigned f2u(float v) { return __float_as_uint(v); }

__device__ __forceinline__ float vmax(float a, float b) { return __builtin_elementwise_maximum(a, b); }
template <int CTRL> __device__ __forceinline__ float dpp(float x) { return __builtin_bit_cast(float, __builtin_amdgcn_mov_dpp(__builtin_bit_cast(int, x), CTRL, 0xf, 0xf, true)); }
template <int CTRL> __device__ __forceinline__ unsigned dppu(unsigned x) { return (unsigned)__builtin_amdgcn_mov_dpp((int)x, CTRL, 0xf, 0xf, true); }
constexpr int XOR1 = 0xB1, XOR2 = 0x4E, ROR8 = 0x128, ROR4 = 0x124, ROR12 = 0x12C;
__device__ __forceinline__ float sw32_sum(float a, float b) { auto r = __builtin_amdgcn_permlane32_swap(f2u(a), f2u(b), false, false); return u2f(r[0]) + u2f(r[1]); }
__device__ __forceinline__ float sw16_sum(float a, float b) { auto r = __builtin_amdgcn_permlane16_swap(f2u(a), f2u(b), false, false); return u2f(r[0]) + u2f(r[1]); }
__device__ __forceinline__ float sw32_max(float a, float b) { auto r = __builtin_amdgcn_permlane32_swap(f2u(a), f2u(b), false, false); return vmax(u2f(r[0]), u2f(r[1])); }
__device__ __forceinline__ float sw16_max(float a, float b) { auto r = __builtin_amdgcn_permlane16_swap(f2u(a), f2u(b), false, false); return vmax(u2f(r[0]), u2f(r[1])); }
__device__ __forceinline__ float wave_sum(float x) {
  x += dpp<XOR1>(x); x += dpp<XOR2>(x); x += dpp<ROR4>(x); x += dpp<ROR8>(x);
  x = sw16_sum(x, x); return sw32_sum(x, x);
}
__device__ __forceinline__ f32x4 mfma32(v4u a, v4u b, f32x4 c) { return __builtin_amdgcn_mfma_f32_16x16x32_bf16(__builtin_bit_cast(bf16x8_t, a), __builtin_bit_cast(bf16x8_t, b), c, 0, 0, 0); }
__device__ __forceinline__ f32x4 mfma16(v2u a, v2u b, f32x4 c) { return __builtin_amdgcn_mfma_f32_16x16x16bf16_1k(__builtin_bit_cast(s16x4, a), __builtin_bit_cast(s16x4, b), c, 0, 0, 0); }
__device__ __forceinline__ void barrier() { asm volatile("s_waitcnt lgkmcnt(0)\n\ts_barrier" ::: "memory"); }
__device__ __forceinline__ v4u tr_frag(LAS unsigned char* lo, LAS unsigned char* hi) {
  const v2u a = __builtin_bit_cast(v2u, __builtin_amdgcn_ds_read_tr16_b64_v4i16((LAS s16x4*)lo));
  const v2u b = __builtin_bit_cast(v2u, __builtin_amdgcn_ds_read_tr16_b64_v4i16((LAS s16x4*)hi));
  return v4u{a[0], a[1], b[0], b[1]};
}

#define AND4(T0, T1, T2, T3, SRC, MOD, SFX) "v_and_b32" SFX " " T0 ", " SRC ", %[m0]" MOD "\n\tv_and_b32" SFX " " T1 ", " SRC ", %[m1]" MOD "\n\tv_and_b32" SFX " " T2 ", " SRC ", %[m2]" MOD "\n\tv_and_b32" SFX " " T3 ", " SRC ", %[m3]" MOD "\n\t"
#define ANDA(SRC, MOD, SFX) AND4("v240", "v241", "v242", "v243", SRC, MOD, SFX)
#define ANDB(SRC, MOD, SFX) AND4("v244", "v245", "v246", "v247", SRC, MOD, SFX)
#define MFA(W) "v_mfma_f32_16x16x32_bf16 %[acc], " W ", v[240:243], %[acc]\n\t"
#define MFB(W) "v_mfma_f32_16x16x32_bf16 %[acc], " W ", v[244:247], %[acc]\n\t"
#define CHAIN_CLOB "v240", "v241", "v242", "v243", "v244", "v245", "v246", "v247"
#define CHAIN_W(W) [w0] "v"(W[0]), [w1] "v"(W[1]), [w2] "v"(W[2]), [w3] "v"(W[3]), [w4] "v"(W[4]), [w5] "v"(W[5]), [w6] "v"(W[6]), [w7] "v"(W[7])
__device__ __forceinline__ void diag_chain_regs(f32x4& acc, const v4u* W, v4u pa, v4u pb, unsigned m0, unsigned m1, unsigned m2, unsigned m3) {
  asm volatile(ANDA("%[p0]", "", "") ANDB("%[p1]", "", "") MFA("%[w0]") ANDA("%[p2]", "", "") MFB("%[w1]") ANDB("%[p3]", "", "") MFA("%[w2]") ANDA("%[p4]", "", "") MFB("%[w3]")
               ANDB("%[p5]", "", "") MFA("%[w4]") ANDA("%[p6]", "", "") MFB("%[w5]") ANDB("%[p7]", "", "") MFA("%[w6]") "s_nop 0\n\t" MFB("%[w7]")
               : [acc] "+v"(acc)
               : CHAIN_W(W), [p0] "v"(pa[0]), [p1] "v"(pa[1]), [p2] "v"(pa[2]), [p3] "v"(pa[3]), [p4] "v"(pb[0]), [p5] "v"(pb[1]), [p6] "v"(pb[2]), [p7] "v"(pb[3]),
                 [m0] "v"(m0), [m1] "v"(m1), [m2] "v"(m2), [m3] "v"(m3)
               : CHAIN_CLOB);
}
#define QP(t) " quad_perm:[" #t "," #t "," #t "," #t "] row_mask:0xf bank_mask:0xf"
__device__ __forceinline__ void diag_chain_quad(f32x4& acc, const v4u* W, unsigned pa, unsigned pb, unsigned m0, unsigned m1, unsigned m2, unsigned m3) {
  asm volatile("s_nop 1\n\t" ANDA("%[pa]", QP(0), "_dpp") ANDB("%[pa]", QP(1), "_dpp") MFA("%[w0]") ANDA("%[pa]", QP(2), "_dpp") MFB("%[w1]") ANDB("%[pa]", QP(3), "_dpp") MFA("%[w2]")
               ANDA("%[pb]", QP(0), "_dpp") MFB("%[w3]") ANDB("%[pb]", QP(1), "_dpp") MFA("%[w4]") ANDA("%[pb]", QP(2), "_dpp") MFB("%[w5]") ANDB("%[pb]", QP(3), "_dpp") MFA("%[w6]")
               "s_nop 0\n\t" MFB("%[w7]")
               : [acc] "+v"(acc)
               : CHAIN_W(W), [pa] "v"(pa), [pb] "v"(pb), [m0] "v"(m0), [m1] "v"(m1), [m2] "v"(m2), [m3] "v"(m3)
               : CHAIN_CLOB);
}
__device__ __forceinline__ void mfma_drain(f32x4& acc) { asm volatile("s_nop 7" : "+v"(acc)); }

__device__ __forceinline__ v4u ln_row(v4u xv) {
  float x[8];
#pragma unroll
  for (int i = 0; i < 4; ++i) { x[2 * i] = bflo(xv[i]); x[2 * i + 1] = bfhi(xv[i]); }
  const float mean = wave_sum(((x[0] + x[1]) + (x[2] + x[3])) + ((x[4] + x[5]) + (x[6] + x[7]))) * (1.0f / D);
  float vs = 0.f;
#pragma unroll
  for (int i = 0; i < 8; ++i) { const float d = x[i] - mean; vs = fmaf(d, d, vs); }
  const float var = wave_sum(vs) * (1.0f / D);
  const float mb = rbf(mean);
  const float rs = __builtin_amdgcn_rcpf(rbf(__builtin_amdgcn_sqrtf(rbf(rbf(var) + 1e-5f))));
  v4u r;
#pragma unroll
  for (int i = 0; i < 4; ++i) { const unsigned d = cvtpk(x[2 * i] - mb, x[2 * i + 1] - mb); r[i] = cvtpk(bflo(d) * rs, bfhi(d) * rs); }
  return r;
}
__device__ __forceinline__ void glds16(const u16* gsrc, unsigned lds_dst) {
  unsigned keep;
  asm volatile("s_mov_b32 %0, m0\n\ts_mov_b32 m0, %2\n\ts_nop 0\n\tglobal_load_lds_dwordx4 %1, off nt\n\ts_mov_b32 m0, %0" : "=&s"(keep) : "v"(gsrc), "s"(lds_dst) : "memory");
}

__device__ __forceinline__ unsigned mixtag(unsigned t) { t *= 0x9E3779B1u; return t ^ (t >> 15); }
#define PK24(x, tb) (((f2u(x) + 0x80u) & 0xffffff00u) | (tb))
#define UP24(x) u2f((x) & 0xffffff00u)
__device__ __forceinline__ unsigned lowbytes(v4u v) { return __builtin_amdgcn_perm(v[1], v[0], 0x0c0c0400u) | __builtin_amdgcn_perm(v[3], v[2], 0x04000c0cu); }
template <int PD_, int TSH>
__device__ __forceinline__ void poll2(const unsigned char* ws, int o0, int o1, unsigned tag, v4u& r0, v4u& r1) {
  r0 = v4u{~tag, ~tag, ~tag, ~tag}; r1 = r0;
#define PASS() asm volatile("global_load_dwordx4 %0, %2, %4 sc1\n\tglobal_load_dwordx4 %1, %3, %4 sc1" : "+v"(r0), "+v"(r1) : "v"(o0), "v"(o1), "s"(ws) : "memory")
#pragma unroll
  for (int k = 0; k < PD_ - 1; ++k) { PASS(); __builtin_amdgcn_s_sleep(PSL); }
#pragma unroll 1
  for (unsigned k = 0; k < SPIN_CAP; ++k) {
    PASS();
    asm volatile("s_waitcnt vmcnt(%2)" : "+v"(r0), "+v"(r1) : "i"((PD_ - 1) * 2) : "memory");
    if (TSH == 16) { if (__all((__builtin_amdgcn_perm(r0[3], r0[1], 0x07060302u) == tag) & (__builtin_amdgcn_perm(r1[3], r1[1], 0x07060302u) == tag))) break; }
    else if (TSH < 0) { if (__all((lowbytes(r0) == tag) & (lowbytes(r1) == tag))) break; }
    else if (__all((((r0[1] ^ tag) | (r0[3] ^ tag) | (r1[1] ^ tag) | (r1[3] ^ tag)) >> (TSH < 0 ? 0 : TSH)) == 0u)) break;
    if (PD_ > 1) __builtin_amdgcn_s_sleep(PSL);
  }
#undef PASS
  asm volatile("s_nop 3 ; POLL_END" : "+v"(r0), "+v"(r1));
}
__device__ __forceinline__ void poll_done(v4u& r0, v4u& r1) { asm volatile("s_waitcnt vmcnt(0)" : "+v"(r0), "+v"(r1) :: "memory"); }

#define STAMP(i) do { } while (0)
#define DUMP_STAMPS() do { } while (0)
#define LD128(off) __builtin_amdgcn_raw_buffer_load_b128(rw, (off), 0, AUX_SC1)
#define ST128(v, off) __builtin_amdgcn_raw_buffer_store_b128((v), rw, (off), 0, AUX_SC1)
#define SPIN_LOOP _Pragma("unroll 1") for (unsigned spins = 0; spins < SPIN_CAP; ++spins)
#define NOHOIST() asm volatile("" ::: "memory")

__global__ void __launch_bounds__(NTHR, 2) fused_block(const u16* x_, const u16* kc_, const u16* vc_, const u16* wq_, unsigned char* ws_,
                                                       const u16* wo_, const u16* wgate_, const u16* wup_, const u16* wdown_, u16* out_, Plan plan_ TUNE_PARAM) {
  typedef unsigned long long u64x8 __attribute__((ext_vector_type(8)));
  typedef unsigned long long u64x2 __attribute__((ext_vector_type(2)));
  u64x8 ka; u64x2 kb2; unsigned pe;
  asm volatile("s_load_dwordx16 %0, %3, 0x0\n\ts_load_dwordx4 %1, %3, 0x40\n\ts_load_dword %2, %3, %4 offset:0x50"
               : "=&s"(ka), "=&s"(kb2), "=&s"(pe) : "s"(__builtin_amdgcn_kernarg_segment_ptr()), "s"((blockIdx.x >> 3) * 4));
  __shared__ __attribute__((aligned(16))) unsigned char smem[L_BYTES];
  LAS unsigned char* L = (LAS unsigned char*)smem;
  STAMP(0);
  const unsigned tstart = (unsigned)__builtin_amdgcn_s_memrealtime();
  const int wg = blockIdx.x, xcd = wg & 7, idx = wg >> 3;
  const int kvh = xcd, b = idx & 1, sp = idx >> 1;
  int lane = threadIdx.x & 63;
  const int w = __builtin_amdgcn_readfirstlane(threadIdx.x >> 6);
  int c = lane & 15, g = lane >> 4;
  if (w == 4 && !(ABL & 32)) {
    unsigned long long pc;
    asm volatile("s_getpc_b64 %0" : "=s"(pc));
    if ((lane * 32 + idx) * 128 < CODE_BYTES) {
      unsigned keep;
      asm volatile("s_mov_b32 %0, m0\n\ts_mov_b32 m0, %3\n\ts_nop 0\n\tglobal_load_lds_dword %1, %2\n\ts_mov_b32 m0, %0"
                   : "=&s"(keep) : "v"((lane * 32 + idx) * 128), "s"(pc & ~255ull), "s"((unsigned)(uintptr_t)L + L_PF) : "memory");
    }
  }
  int koff = (c >> 2) * 1024 + g * 64 + (c & 3) * 16;
  unsigned pbase = (unsigned)(b * HKV + kvh) * (SK * DH * 2);
  asm volatile("s_waitcnt lgkmcnt(0)" : "+s"(ka), "+s"(kb2), "+s"(pe), "+v"(koff), "+s"(pbase));
  const u16 *x = (const u16*)ka[0], *wq = (const u16*)ka[3], *wo = (const u16*)ka[5], *wgate = (const u16*)ka[6], *wup = (const u16*)ka[7], *wdown = (const u16*)kb2[0];
  unsigned char* ws = (unsigned char*)ka[4];
  u16* out = (u16*)kb2[1];
  const int tile0 = pe & 2047, ntile = (pe >> 11) & 255;
  const __amdgpu_buffer_rsrc_t rk = __builtin_amdgcn_make_buffer_rsrc((void*)(ka[1] + pbase), 0, SK * DH * 2, 0x00020000);
  const unsigned sbase = (unsigned)(tile0 + w) * 4096u;
  v4u kb[15][4];
#define SB() __builtin_amdgcn_sched_barrier(0)
#define ISSUEK(d, j) do { _Pragma("unroll") for (int s = 0; s < 4; ++s) { \
      if (!HEAVY(d, s)) kb[d][s] = __builtin_amdgcn_raw_buffer_load_b128(rk, koff + s * 256, sbase + (unsigned)(j) * 16384u, AUX_NT); \
      else { const v2u lo_ = __builtin_amdgcn_raw_buffer_load_b64(rk, koff + s * 256, sbase + (unsigned)(j) * 16384u, AUX_NT); NOHOIST(); SB(); \
             const v2u hi_ = __builtin_amdgcn_raw_buffer_load_b64(rk, koff + s * 256 + 8, sbase + (unsigned)(j) * 16384u, AUX_NT); NOHOIST(); \
             kb[d][s] = v4u{lo_[0], lo_[1], hi_[0], hi_[1]}; } } SB(); } while (0)
  const int role = (pe >> 19) & 7, rix = pe >> 22;
  const bool qrole = role == 4;
#define CI() ({ int r_ = rix; asm volatile("" : "+s"(r_)); const int cn_ = xcd * 5 + r_ - (xcd < 7 ? 2 : 0); (xcd < 7 && r_ < 2) ? 8 * xcd + 6 + r_ : cn_ + 2 * ((cn_ * 43) >> 8); })
  const int bi = xcd * 8 + rix, rj = xcd * 4 + rix, qi = bi;
  const __amdgpu_buffer_rsrc_t rw = __builtin_amdgcn_make_buffer_rsrc((void*)ws, 0, WS_BYTES, 0x00020000);
  const unsigned lds0 = (unsigned)(uintptr_t)L;
  LAS unsigned char* S = L + L_S;
#define WAIT_UNTIL(t, slp) do { while ((int)((unsigned)__builtin_amdgcn_s_memrealtime() - (tstart + (unsigned)(t))) < 0) __builtin_amdgcn_s_sleep(slp); } while (0)
  int q4 = (lane >> 2) & 3, p4 = lane & 3;
  constexpr float SCALE = 0.088388347648318440f, LOG2E = 1.4426950408889634f;
  unsigned tag;

  if (w == 4) {
    __builtin_amdgcn_s_setprio(3);
#define TOUCH() \
    if (!(ABL & 64)) {     \
      const int ci = CI(); \
      const unsigned long long vb_ = ka[2] + pbase + (unsigned)tile0 * 4096u, ke_ = ka[1] + pbase + (unsigned)(tile0 + ntile) * 4096u - 128u; \
      const unsigned long long wa_ = role == 1 ? ka[6] + ci * 64 : role == 2 ? ka[5] + bi * 65536 : kb2[1]; \
      const unsigned long long ad = lane == 0 ? vb_ : lane == 1 ? vb_ + (unsigned)ntile * 4096u - 128u : lane == 2 ? ke_ : lane == 3 ? wa_ \
                                  : lane == 4 ? wa_ + 511 * F * 2 : lane == 5 ? ka[7] + ci * 64 : lane == 6 ? ka[7] + ci * 64 + 511 * F * 2 : kb2[0] + ci * 32768; \
      if (lane < (role == 1 ? 8 : 4)) { \
        unsigned keep; \
        asm volatile("s_mov_b32 %0, m0\n\ts_mov_b32 m0, %2\n\ts_nop 0\n\tglobal_load_lds_dword %1, off nt\n\ts_mov_b32 m0, %0" : "=&s"(keep) : "v"(ad), "s"(lds0 + L_PF) : "memory"); \
      } \
    }
    if (qrole) {
      const v4u x0 = *(const GAS v4u*)(x + lane * 8), x1 = *(const GAS v4u*)(x + D + lane * 8);
      tag = __builtin_amdgcn_raw_buffer_load_b32(rw, OFF_EPOCH, 0, AUX_SC1) + 1u;
      const v4u n0 = ln_row(x0), n1 = ln_row(x1);
      *(LAS v4u*)(S + L_XN + lane * 16) = n0; *(LAS v4u*)(S + L_XN + 1024 + lane * 16) = n1;
      STAMP(9);
      barrier();
      TOUCH();
      barrier();
      const unsigned th = mixtag(tag);
      const f32x4 s = (*(LAS f32x4*)(S + L_MRG + lane * 16) + *(LAS f32x4*)(S + L_MRG + 1024 + lane * 16)) + (*(LAS f32x4*)(S + L_MRG + 2048 + lane * 16) + *(LAS f32x4*)(S + L_MRG + 3072 + lane * 16));
      ST128((v4u{PK24(s[0], th & 0xffu), PK24(s[1], (th >> 8) & 0xffu), PK24(s[2], (th >> 16) & 0xffu), PK24(s[3], th >> 24)}),
            OFF_Q + (((qi & 1) * 2 + (lane >> 5)) * 4096 + (qi >> 1) * 128) * 4 + (lane & 31) * 16);
    } else {
      TOUCH();
      tag = __builtin_amdgcn_raw_buffer_load_b32(rw, OFF_EPOCH, 0, AUX_SC1) + 1u;
    }
    STAMP(1);
    if (lane == 0) *(LAS unsigned*)(L + L_TAG) = tag;
    {
      const int qo = OFF_Q + (b * 4096 + kvh * 512) * 4 + lane * 16, qo2 = qo + 32768;
      const unsigned th = mixtag(tag);
      v4u q0 = {~th, ~th, ~th, ~th}, q1 = q0, q2 = q0, q3 = q0;
      WAIT_UNTIL(T_P, 2);
#define QOK() __all((lowbytes(q0) == th) & (lowbytes(q1) == th) & (lowbytes(q2) == th) & (lowbytes(q3) == th))
      bool got = false;
#pragma unroll 1
      for (unsigned k = 0; k < SPIN_CAP && !got; ++k) {
        asm volatile("s_waitcnt vmcnt(12)\n\tglobal_load_dwordx4 %0, %4, %6 sc1\n\tglobal_load_dwordx4 %1, %4, %6 offset:1024 sc1\n\tglobal_load_dwordx4 %2, %5, %6 sc1\n\tglobal_load_dwordx4 %3, %5, %6 offset:1024 sc1"
                     : "+v"(q0), "+v"(q1), "+v"(q2), "+v"(q3) : "v"(qo), "v"(qo2), "s"(ws) : "memory");
        const unsigned nxt = (unsigned)__builtin_amdgcn_s_memrealtime() + T_PD;
        do {
          __builtin_amdgcn_s_sleep(1);
          asm volatile("" : "+v"(q0), "+v"(q1), "+v"(q2), "+v"(q3));
          got = QOK();
        } while (!got && (int)((unsigned)__builtin_amdgcn_s_memrealtime() - nxt) < 0);
      }
      asm volatile("s_nop 3" : "+v"(q0), "+v"(q1), "+v"(q2), "+v"(q3));
      *(LAS v2u*)(L + L_QF + lane * 8) = v2u{cvtpk(UP24(q0[0]) + UP24(q2[0]), UP24(q0[1]) + UP24(q2[1])), cvtpk(UP24(q0[2]) + UP24(q2[2]), UP24(q0[3]) + UP24(q2[3]))};
      *(LAS v2u*)(L + L_QF + 512 + lane * 8) = v2u{cvtpk(UP24(q1[0]) + UP24(q3[0]), UP24(q1[1]) + UP24(q3[1])), cvtpk(UP24(q1[2]) + UP24(q3[2]), UP24(q1[3]) + UP24(q3[3]))};
      STAMP(2);
      barrier();
      asm volatile("s_waitcnt vmcnt(0) ; QPOLL_DONE" : "+v"(q0), "+v"(q1), "+v"(q2), "+v"(q3) :: "memory");
    }
    asm volatile("s_waitcnt vmcnt(0)" ::: "memory");
    return;
  }
  const int nstep = (ntile - w + 3) >> 2;
  const __amdgpu_buffer_rsrc_t rv = __builtin_amdgcn_make_buffer_rsrc((void*)(ka[2] + pbase), 0, SK * DH * 2, 0x00020000);
#define VMWAIT(n) do { asm volatile("s_waitcnt vmcnt(%0)" :: "i"(n) : "memory"); SB(); } while (0)
#define RELANE() do { asm volatile("v_mbcnt_lo_u32_b32 %0, -1, 0\n\tv_mbcnt_hi_u32_b32 %0, -1, %0" : "=v"(lane)); c = lane & 15; g = lane >> 4; } while (0)
  int wp = 0;
#define WUPTO(n, pace) do { _Pragma("unroll 1") while (wp < (n)) { \
      const u16* src_ = role == 2 ? wo + (size_t)(bi * 64 + 4 * wp + g) * D + 128 * w + 8 * c \
                      : wp < 16 ? (wp < 8 ? wgate : wup) + (size_t)(128 * w + 16 * (wp & 7) + (lane >> 2)) * F + ci * 32 + (((lane & 3) ^ ((lane >> 4) & 2)) * 8) \
                                : wdown + (size_t)(ci * 32 + 4 * (wp & 7) + g) * D + 128 * w + 8 * c; \
      VMWAIT(pace); glds16(src_, lds0 + L_W + w * WREG + wp * 1024); ++wp; } } while (0)
  if (qrole) {
    const u16* p = wq + (size_t)(256 * (qi & 1) + 64 * w + g) * (HQ * DH) + (qi >> 1) * 128;
    const int ch0 = (c ^ (2 * g)) * 8;
#pragma unroll 1
    for (int t = 0; t < 16; t += 2) {
      const int ch = ch0 ^ ((t & 2) * 32);
      glds16(p + ch, lds0 + L_W + w * WREG + t * 1024);
      glds16(p + 4 * (HQ * DH) + ch, lds0 + L_W + w * WREG + t * 1024 + 1024);
      p += 8 * (HQ * DH);
    }
    STAMP(7);
  }
  STAMP(11);
  if (!qrole) { ISSUEK(0, 0); ISSUEK(1, 1); }
  else {
    const int sw = 2 * q4 + 8 * (g & 1);
    v4u fr[16];
#define QFRAGS(ks) do { _Pragma("unroll") for (int t = 0; t < 8; ++t) { \
        LAS unsigned char* a = L + L_W + w * WREG + (ks) * 8192 + g * 2048 + q4 * 256 + (p4 & 1) * 8 + (((2 * t + (p4 >> 1)) ^ sw) * 16); fr[(ks) * 8 + t] = tr_frag(a, a + 1024); } } while (0)
    VMWAIT(8); QFRAGS(0); ISSUEK(0, 0);
    VMWAIT(4 + NINST(0)); ISSUEK(1, 1);
    barrier();
    f32x4 qa[8];
    const v4u xb0 = *(LAS v4u*)(S + L_XN + (c & 1) * 1024 + (256 * (qi & 1) + 64 * w + 8 * g) * 2), xb1 = *(LAS v4u*)(S + L_XN + (c & 1) * 1024 + (256 * (qi & 1) + 64 * w + 32 + 8 * g) * 2);
#pragma unroll
    for (int t = 0; t < 8; ++t) qa[t] = mfma32(fr[t], xb0, f32x4{0.f, 0.f, 0.f, 0.f});
    VMWAIT(NINST(0) + NINST(1)); STAMP(8); QFRAGS(1);
#pragma unroll
    for (int t = 0; t < 8; ++t) qa[t] = mfma32(fr[8 + t], xb1, qa[t]);
    if (c < 2) {
#pragma unroll
      for (int t = 0; t < 8; ++t) *(LAS f32x4*)(S + L_MRG + w * 1024 + c * 512 + t * 64 + g * 16) = qa[t];
    }
    STAMP(1);
    barrier();
  }
  const int rem = nstep & 3;
#pragma unroll
  for (int d = 2; d < 12; ++d) { VMWAIT(NINST(d - 1)); ISSUEK(d, d); }
  STAMP(10);
  barrier();
  STAMP(2);
  const v4u qf = *(LAS v4u*)(L + L_QF + (c >> 2) * 256 + g * 64 + (c & 3) * 16);
  tag = __builtin_amdgcn_readfirstlane(*(LAS unsigned*)(L + L_TAG));

  LAS unsigned char* scp = L + L_SC + w * 5120 + g * 32 + (c >> 2) * 8;
  float m = -INFINITY;
  f32x4 sc;
#define QK(d) do { \
    _Pragma("unroll") for (int s = 0; s < 4; ++s) { const f32x4 dd = mfma32(kb[d][s], qf, f32x4{0.f, 0.f, 0.f, 0.f}); sc[s] = (c & 2) ? ((c & 1) ? dd[3] : dd[2]) : ((c & 1) ? dd[1] : dd[0]); } \
    asm volatile("" : "+v"(sc) :: "memory"); SB(); } while (0)
#define SCORE(d, j) do { \
    _Pragma("unroll") for (int s = 0; s < 4; ++s) { float t_ = sc[s]; t_ += dpp<XOR1>(t_); t_ += dpp<XOR2>(t_); sc[s] = t_; } \
    const unsigned r01 = cvtpk(sc[0], sc[1]), r23 = cvtpk(sc[2], sc[3]); \
    const unsigned t01 = cvtpk(bflo(r01) * SCALE, bfhi(r01) * SCALE), t23 = cvtpk(bflo(r23) * SCALE, bfhi(r23) * SCALE); \
    m = vmax(vmax(m, vmax(bflo(t01), bfhi(t01))), vmax(bflo(t23), bfhi(t23))); \
    *(LAS v2u*)(scp + (j) * 128) = v2u{t01, t23}; asm volatile("" : "+v"(m)); SB(); } while (0)
#pragma unroll
  for (int d = 0; d < 4; ++d) { QK(d); ISSUEK(d, 12 + rem + d); SCORE(d, d); }
#pragma unroll
  for (int d = 4; d < 12; ++d) { QK(d); SCORE(d, d); }
#pragma unroll
  for (int d = 12; d < 15; ++d) if (d - 12 < rem) ISSUEK(d, d);
  int j = 12 + rem;
#pragma unroll 1
  do {
#pragma unroll
    for (int d = 0; d < 4; ++d) { QK(d); ISSUEK(d, j + 4 + d); SCORE(d, j + d); }
    j += 4;
  } while (j + 4 < nstep);
#pragma unroll
  for (int d = 12; d < 15; ++d) if (d - 12 < rem) { QK(d); SCORE(d, d); }
  const int voff = g * 1024 + c * 16;
  v2u vl[7][4], vh[7][4];
#define ISSUEV(d, j) do { _Pragma("unroll") for (int s = 0; s < 4; ++s) { \
      if (!HEAVY(d, s)) { const v4u t_ = __builtin_amdgcn_raw_buffer_load_b128(rv, voff + s * 256, sbase + (unsigned)(j) * 16384u, AUX_NT); vl[d][s] = v2u{t_[0], t_[1]}; vh[d][s] = v2u{t_[2], t_[3]}; } \
      else { vl[d][s] = __builtin_amdgcn_raw_buffer_load_b64(rv, voff + s * 256, sbase + (unsigned)(j) * 16384u, AUX_NT); NOHOIST(); SB(); \
             vh[d][s] = __builtin_amdgcn_raw_buffer_load_b64(rv, voff + s * 256 + 8, sbase + (unsigned)(j) * 16384u, AUX_NT); NOHOIST(); } } SB(); } while (0)
#pragma unroll
  for (int d = 0; d < 4; ++d) { QK(d); ISSUEV(d, d); SCORE(d, j + d); }
#pragma unroll
  for (int d = 4; d < 7; ++d) if (d - 4 < rem) ISSUEV(d, d);
  STAMP(9);
  m = sw16_max(m, m); m = sw32_max(m, m);
  const float mlog = m * LOG2E;
  const unsigned m0 = (c & 3) == 0 ? 0xffffu : (c & 3) == 1 ? 0xffff0000u : 0u;
  const unsigned m1 = (c & 3) == 2 ? 0xffffu : (c & 3) == 3 ? 0xffff0000u : 0u;
  f32x4 acc0 = {0.f, 0.f, 0.f, 0.f}, acc1 = {0.f, 0.f, 0.f, 0.f};
  float l = 0.f;
#define PCALC(bb, j) do { \
    const v2u t_ = *(LAS v2u*)(scp + (j) * 128); \
    const float sv[4] = {bflo(t_[0]), bfhi(t_[0]), bflo(t_[1]), bfhi(t_[1])}; \
    _Pragma("unroll") for (int i = 0; i < 4; ++i) { const float p_ = __builtin_amdgcn_exp2f(fmaf(sv[i], LOG2E, -mlog)); const unsigned pp = cvtpk(p_, p_); l += bflo(pp); bb[i] = v2u{pp & m0, pp & m1}; } } while (0)
#define PMFMA(d, bb) do { SB(); \
    _Pragma("unroll") for (int i = 0; i < 4; ++i) { acc0 = mfma16(vl[d][i], bb[i], acc0); acc1 = mfma16(vh[d][i], bb[i], acc1); } \
    asm volatile("" : "+v"(acc0), "+v"(acc1) :: "memory"); SB(); } while (0)
#define PV(d, j) do { v2u bb_[4]; PCALC(bb_, j); PMFMA(d, bb_); } while (0)
#pragma unroll
  for (int d = 0; d < 4; ++d) { PV(d, d); ISSUEV(d, 4 + rem + d); }
#pragma unroll
  for (int d = 4; d < 7; ++d) if (d - 4 < rem) PV(d, d);
  j = 4 + rem;
#pragma unroll 1
  do {
#pragma unroll
    for (int d = 0; d < 4; ++d) { PV(d, j + d); ISSUEV(d, j + 4 + d); }
    j += 4;
  } while (j + 4 + 4 * NLR < nstep);
  unsigned bal[2] = {0u, 0u};
  const int blo = (lane & 31) * 128;
#pragma unroll
  for (int r = 0; r < NLR; ++r) {
#pragma unroll
    for (int d = 0; d < 4; ++d) {
      if (r > 0 && !(d & 1)) asm volatile("" :: "v"(bal[d >> 1]));
      PV(d, j + d);
#pragma unroll
      for (int s = 0; s < 4; ++s) { const v4u t_ = __builtin_amdgcn_raw_buffer_load_b128(rv, voff + s * 256, sbase + (unsigned)(j + 4 + d) * 16384u, AUX_NT); vl[d][s] = v2u{t_[0], t_[1]}; vh[d][s] = v2u{t_[2], t_[3]}; }
      SB();
      if (!(d & 1)) {
        const bool late = (int)((unsigned)__builtin_amdgcn_s_memrealtime() - (tstart + T_L)) > 0;
        bal[d >> 1] = __builtin_amdgcn_raw_buffer_load_b32(rv, late ? 0 : blo, sbase + (unsigned)(j + 4 + d) * 16384u, AUX_NT); SB();
      }
    }
    j += 4;
  }
  asm volatile("v_mbcnt_lo_u32_b32 %0, -1, 0\n\tv_mbcnt_hi_u32_b32 %0, -1, %0" : "=v"(lane));
  c = lane & 15; g = lane >> 4; q4 = (lane >> 2) & 3; p4 = lane & 3;
  const int tid = w * 64 + lane;
#define PIN(x) asm volatile("" : "+v"(x))
  const unsigned tagh = mixtag(tag);
  v2u lb[4][4];
#pragma unroll
  for (int d = 0; d < 4; ++d) PCALC(lb[d], j + d);
  l = sw16_sum(l, l); l = sw32_sum(l, l);
  *(LAS f32x2*)(S + L_RED + (c >> 2) * 32 + w * 8) = f32x2{mlog, l};
  barrier();
  const int hd = 2 * (w & 1) + (lane >> 5);
  float fw; unsigned t1w, t3w;
  {
#define MERGE_ML(h, M_, L_) do { const f32x4 u0_ = *(LAS f32x4*)(S + L_RED + (h) * 32), u1_ = *(LAS f32x4*)(S + L_RED + (h) * 32 + 16); \
      M_ = vmax(vmax(u0_[0], u0_[2]), vmax(u1_[0], u1_[2])); \
      L_ = fmaf(__builtin_amdgcn_exp2f(u0_[2] - M_), u0_[3], __builtin_amdgcn_exp2f(u0_[0] - M_) * u0_[1]) + fmaf(__builtin_amdgcn_exp2f(u1_[2] - M_), u1_[3], __builtin_amdgcn_exp2f(u1_[0] - M_) * u1_[1]); } while (0)
    float M1, L1, M2, L2;
    MERGE_ML(c >> 2, M1, L1); MERGE_ML(hd, M2, L2);
    fw = __builtin_amdgcn_exp2f(mlog - M1) * __builtin_amdgcn_rcpf(L1);
    const unsigned lse = f2u(M2 + __builtin_amdgcn_logf(L2));
    t1w = __builtin_amdgcn_perm(tagh, lse, 0x05040100u); t3w = __builtin_amdgcn_perm(tagh, lse, 0x07060302u);
  }
  unsigned mw = lds0 + L_S + L_MRG + w * 2048 + lane * 16, mr = lds0 + L_S + L_MRG + ((lane >> 2) & 1) * 1024 + (((lane >> 3) & 3) * 16 + hd * 4 + (lane & 3)) * 16;
  int pao = OFF_PART + ((b * HQ + kvh * 4 + hd) * NSPLIT + sp) * 512 + (lane & 31) * 16;
  PIN(mw); PIN(mr); PIN(pao); PIN(fw); PIN(t1w); PIN(t3w);
#pragma unroll
  for (int d = 0; d < 4; ++d) { PMFMA(d, lb[d]); if (d == 2) asm volatile("" :: "v"(bal[0]), "v"(bal[1])); }
  STAMP(3);
  *(LAS f32x4*)mw = acc0 * fw; *(LAS f32x4*)(mw + 1024) = acc1 * fw;
  barrier();
  if (w < 2) {
    const f32x4 o = (*(LAS f32x4*)mr + *(LAS f32x4*)(mr + 2048)) + (*(LAS f32x4*)(mr + 4096) + *(LAS f32x4*)(mr + 6144));
    ST128((v4u{cvtpk(o[0], o[1]), t1w, cvtpk(o[2], o[3]), t3w}), pao);
  }
  STAMP(4);
  const int ci = CI();
  if (role == 1 || role == 2) WAIT_UNTIL(role == 1 ? T_WC : T_WB, 2);
  WUPTO(role == 1 ? 24 : role == 2 ? 16 : 0, 40);
  unsigned dm0 = (c & 7) == 0 ? 0xffffu : (c & 7) == 1 ? 0xffff0000u : 0u, dm1 = (c & 7) == 2 ? 0xffffu : (c & 7) == 3 ? 0xffff0000u : 0u,
           dm2 = (c & 7) == 4 ? 0xffffu : (c & 7) == 5 ? 0xffff0000u : 0u, dm3 = (c & 7) == 6 ? 0xffffu : (c & 7) == 7 ? 0xffff0000u : 0u;
  unsigned tb0 = tagh & 0xffu, tb1 = (tagh >> 8) & 0xffu, tb2 = (tagh >> 16) & 0xffu, tb3 = tagh >> 24;
#define PUBLISH_ACC(acc, off) ST128((v4u{PK24(acc[0], tb0), PK24(acc[1], tb1), PK24(acc[2], tb2), PK24(acc[3], tb3)}), (off))

  if (role == 2) {
    unsigned wra = lds0 + L_W + w * WREG + lane * 16;
    PIN(wra);
    const int head = bi >> 1;
    const int po = OFF_PART + (((lane >> 5) * HQ + head) * NSPLIT + ((lane >> 2) & 7)) * 512 + (bi & 1) * 256 + w * 64 + (lane & 3) * 16;
    unsigned ow = lds0 + L_S + L_OB + ((g >> 1) * 4 + (lane & 3)) * 64 + (8 * (w >> 1) + 2 * ((g & 1) + ((lane & 4) >> 1)) + (w & 1)) * 4, orr = lds0 + L_S + L_OB + ((c >> 3) * 4 + g) * 64;
    int so_ = OFF_P1 + bi * 4096 + tid * 16;
    f32x4 acc = {0.f, 0.f, 0.f, 0.f};
    PIN(ow); PIN(orr); PIN(so_); PIN(dm0); PIN(dm1); PIN(dm2); PIN(dm3); PIN(tb0); PIN(tb1); PIN(tb2); PIN(tb3); PIN(acc);
    v4u a0, a1;
    poll2<PD1, 16>(ws, po, po + 8 * 512, tagh, a0, a1);
    STAMP(5);
    v4u wr[16];
#pragma unroll
    for (int i = 0; i < 16; ++i) wr[i] = *(LAS v4u*)(wra + i * 1024);
    SB();
    const float l0 = u2f(__builtin_amdgcn_perm(a0[3], a0[1], 0x05040100u)), l1 = u2f(__builtin_amdgcn_perm(a1[3], a1[1], 0x05040100u));
    float xm = vmax(l0, l1);
    xm = vmax(vmax(xm, dpp<ROR4>(xm)), vmax(dpp<ROR8>(xm), dpp<ROR12>(xm)));
    xm = sw16_max(xm, xm);
    const float w0 = __builtin_amdgcn_exp2f(l0 - xm), w1 = __builtin_amdgcn_exp2f(l1 - xm);
    float u0 = sw16_sum(fmaf(w1, bflo(a1[0]), w0 * bflo(a0[0])), fmaf(w1, bfhi(a1[0]), w0 * bfhi(a0[0])));
    float u1 = sw16_sum(fmaf(w1, bflo(a1[2]), w0 * bflo(a0[2])), fmaf(w1, bfhi(a1[2]), w0 * bfhi(a0[2])));
    float ws_ = w0 + w1;
    ws_ = sw16_sum(ws_, ws_);
    u0 += dpp<ROR8>(u0); u1 += dpp<ROR8>(u1); ws_ += dpp<ROR8>(ws_);
    u0 += dpp<ROR4>(u0); u1 += dpp<ROR4>(u1); ws_ += dpp<ROR4>(ws_);
    const float uo = ((lane & 4) ? u1 : u0) * __builtin_amdgcn_rcpf(ws_);
    *(LAS unsigned*)ow = cvtpk(uo, uo);
    barrier();
    const v4u pv0 = *(LAS v4u*)orr, pv1 = *(LAS v4u*)(orr + 16), pv2 = *(LAS v4u*)(orr + 32), pv3 = *(LAS v4u*)(orr + 48);
    diag_chain_regs(acc, wr, pv0, pv1, dm0, dm1, dm2, dm3);
    diag_chain_regs(acc, wr + 8, pv2, pv3, dm0, dm1, dm2, dm3);
    mfma_drain(acc);
    PUBLISH_ACC(acc, so_);
    STAMP(6);
    poll_done(a0, a1);
  } else if (role == 3) {
    const int rb = rj & 1, col0 = (rj >> 1) * 32;
    unsigned xp = *(const GAS unsigned*)(x + rb * D + col0 + 2 * c);
    asm volatile("" : "+v"(xp));
    const int pp = lane >> 3;
    float h0, h1;
    unsigned rw_ = lds0 + L_S + L_RR + w * 128 + (8 * g + (lane & 7)) * 4, rr_ = lds0 + L_S + L_RR + c * 8;
    int hbo = OFF_HB + ((w & 1) * 4 + g) * 4096 + rb * 2048 + col0 * 4 + c * 8;
    GAS unsigned* op = (GAS unsigned*)(out + rb * D + col0 + 2 * c);
    PIN(rw_); PIN(rr_); PIN(hbo); PIN(op);
    {
      const int po = OFF_P1 + (16 * w + pp) * 4096 + rj * 128 + (lane & 7) * 16;
      v4u a0, a1;
      poll2<PD2, -1>(ws, po, po + 8 * 4096, tagh, a0, a1);
      STAMP(5);
      float u = sw16_sum(sw32_sum(UP24(a0[0]) + UP24(a1[0]), UP24(a0[2]) + UP24(a1[2])), sw32_sum(UP24(a0[1]) + UP24(a1[1]), UP24(a0[3]) + UP24(a1[3])));
      u += dpp<ROR8>(u);
      *(LAS float*)rw_ = u;
      barrier();
      const f32x2 s = (*(LAS f32x2*)rr_ + *(LAS f32x2*)(rr_ + 128)) + (*(LAS f32x2*)(rr_ + 256) + *(LAS f32x2*)(rr_ + 384));
      const unsigned at = cvtpk(s[0], s[1]);
      const unsigned hp = cvtpk(bflo(xp) + bflo(at), bfhi(xp) + bfhi(at));
      h0 = bflo(hp); h1 = bfhi(hp);
      if (w < 2) __builtin_amdgcn_raw_buffer_store_b64(v2u{hp, tag}, rw, hbo, 0, AUX_SC1);
      STAMP(6);
      poll_done(a0, a1);
    }
    {
      const int po = OFF_P2 + (14 * w + pp) * 4096 + rj * 128 + (lane & 7) * 16;
      v4u a0, b1;
      poll2<PD4, -1>(ws, po, po + (pp < 6 ? 8 * 4096 : 0), tagh, a0, b1);
      STAMP(7);
      const unsigned km = pp < 6 ? 0xffffff00u : 0u;
      float u = sw16_sum(sw32_sum(UP24(a0[0]) + u2f(b1[0] & km), UP24(a0[2]) + u2f(b1[2] & km)), sw32_sum(UP24(a0[1]) + u2f(b1[1] & km), UP24(a0[3]) + u2f(b1[3] & km)));
      u += dpp<ROR8>(u);
      *(LAS float*)(rw_ + 512) = u;
      barrier();
      if (tid < 16) {
        const f32x2 s = (*(LAS f32x2*)(rr_ + 512) + *(LAS f32x2*)(rr_ + 640)) + (*(LAS f32x2*)(rr_ + 768) + *(LAS f32x2*)(rr_ + 896));
        const unsigned dn = cvtpk(s[0], s[1]);
        *op = cvtpk(h0 + bflo(dn), h1 + bfhi(dn));
        if (rj == 0 && tid == 0) __builtin_amdgcn_raw_buffer_store_b32(tag, rw, OFF_EPOCH, 0, AUX_SC1);
      }
      STAMP(8);
      poll_done(a0, b1);
    }
  } else if (role == 1) {
    v4u fg[8], fu[8], wd[8];
    LAS unsigned char* fa = L + L_W + w * WREG + (8 * g + q4) * 64 + (p4 & 1) * 8;
    VMWAIT(16);
#pragma unroll
    for (int ks = 0; ks < 4; ++ks)
#pragma unroll
      for (int t = 0; t < 2; ++t) { LAS unsigned char* a = fa + 2048 * ks + (((2 * t + (p4 >> 1)) ^ ((g & 1) << 1)) * 16); fg[ks * 2 + t] = tr_frag(a, a + 256); }
    VMWAIT(8);
    STAMP(10);
#pragma unroll
    for (int ks = 0; ks < 4; ++ks)
#pragma unroll
      for (int t = 0; t < 2; ++t) { LAS unsigned char* a = fa + 8192 + 2048 * ks + (((2 * t + (p4 >> 1)) ^ ((g & 1) << 1)) * 16); fu[ks * 2 + t] = tr_frag(a, a + 256); }
    f32x4 ag[2], au[2];
    unsigned ro = lds0 + L_S + L_RED + ((c >> 2) & 1) * 1024 + ((c & 1) * 32 + (c & 2) * 8 + 4 * g) * 16 + w * 4;
    unsigned fo = lds0 + L_S + L_RED + ((c >> 3) * 32 + 4 * (c & 3) + g) * 16;
#define GU_MFMA(XB) do { _Pragma("unroll") for (int t = 0; t < 2; ++t) { ag[t] = f32x4{0.f, 0.f, 0.f, 0.f}; au[t] = f32x4{0.f, 0.f, 0.f, 0.f}; } \
      _Pragma("unroll") for (int ks = 0; ks < 4; ++ks) { \
        _Pragma("unroll") for (int t = 0; t < 2; ++t) { ag[t] = mfma32(fg[ks * 2 + t], XB, ag[t]); au[t] = mfma32(fu[ks * 2 + t], XB, au[t]); } } } while (0)
#define GU_STORE() do { const f32x4 sel = (c & 4) ? ((c & 2) ? au[1] : au[0]) : ((c & 2) ? ag[1] : ag[0]); \
      _Pragma("unroll") for (int i = 0; i < 4; ++i) *(LAS float*)(ro + i * 16) = sel[i]; } while (0)
#define SUM4(p_) ({ const f32x2 t_ = f32x2{p_[0], p_[1]} + f32x2{p_[2], p_[3]}; float r_ = t_[0] + t_[1]; PIN(r_); r_; })
    GU_MFMA((v4u{0x3f803f80u, 0x3f803f80u, 0x3f803f80u, 0x3f803f80u}));
    GU_STORE();
    barrier();
    float cg0, cg1, cu0, cu1;
    { const f32x4 p0 = *(LAS f32x4*)fo, p1 = *(LAS f32x4*)(fo + 256), p2 = *(LAS f32x4*)(fo + 1024), p3 = *(LAS f32x4*)(fo + 1280); cg0 = SUM4(p0); cg1 = SUM4(p1); cu0 = SUM4(p2); cu1 = SUM4(p3); }
    unsigned xw = lds0 + L_S + L_XN + (w & 1) * 1024 + lane * 16, xr = lds0 + L_S + L_XN + (c & 1) * 1024 + (128 * w + 8 * g) * 2, sw_ = lds0 + L_S + L_ST + w * 8, sr = lds0 + L_S + L_ST + (c >> 3) * 8;
    int so_ = OFF_P2 + ci * 4096 + tid * 16;
    f32x4 acc = {0.f, 0.f, 0.f, 0.f};
    unsigned wda = lds0 + L_W + w * WREG + 16384 + lane * 16;
    PIN(wda); PIN(ro); PIN(fo); PIN(xw); PIN(xr); PIN(sw_); PIN(sr); PIN(so_); PIN(dm0); PIN(dm1); PIN(dm2); PIN(dm3); PIN(tb0); PIN(tb1); PIN(tb2); PIN(tb3); PIN(acc);
    v4u ha = {0u, 0u, 0u, 0u}, hb = ha;
    if (w < 2) {
      const int ho = OFF_HB + xcd * 4096 + w * 2048 + lane * 32;
      poll2<PD3, 0>(ws, ho, ho + 16, tag, ha, hb);
      STAMP(5);
      *(LAS v4u*)xw = v4u{ha[0], ha[2], hb[0], hb[2]};
    } else VMWAIT(0);
    barrier();
    v4u xb[4];
#pragma unroll
    for (int ks = 0; ks < 4; ++ks) xb[ks] = *(LAS v4u*)(xr + 64 * ks);
#pragma unroll
    for (int i = 0; i < 8; ++i) wd[i] = *(LAS v4u*)(wda + i * 1024);
    __builtin_amdgcn_sched_barrier(0);
    const float pv = bflo(__builtin_amdgcn_readfirstlane(ha[0]));
    const float d0 = bflo(ha[0]) - pv, d1 = bfhi(ha[0]) - pv, d2 = bflo(ha[2]) - pv, d3 = bfhi(ha[2]) - pv, d4 = bflo(hb[0]) - pv, d5 = bfhi(hb[0]) - pv, d6 = bflo(hb[2]) - pv, d7 = bfhi(hb[2]) - pv;
    float s1 = ((d0 + d1) + (d2 + d3)) + ((d4 + d5) + (d6 + d7));
    float s2 = (fmaf(d1, d1, d0 * d0) + fmaf(d3, d3, d2 * d2)) + (fmaf(d5, d5, d4 * d4) + fmaf(d7, d7, d6 * d6));
    __builtin_amdgcn_sched_barrier(0);
    GU_MFMA(xb[ks]);
    {
      float t = sw32_sum(s1, s2);
      t = sw16_sum(t, t); t += dpp<ROR8>(t); t += dpp<ROR4>(t); t += dpp<XOR2>(t); t += dpp<XOR1>(t);
      const float md = u2f(__builtin_amdgcn_readlane(f2u(t), 0)) * (1.0f / D), q2 = u2f(__builtin_amdgcn_readlane(f2u(t), 32)) * (1.0f / D);
      const float var = vmax(fmaf(-md, md, q2), 0.f);
      const float rs = __builtin_amdgcn_rcpf(rbf(__builtin_amdgcn_sqrtf(rbf(rbf(var) + 1e-5f))));
      *(LAS f32x2*)sw_ = f32x2{rbf(pv + md), rs};
    }
    GU_STORE();
    barrier();
    const f32x2 st = *(LAS f32x2*)sr;
    const f32x4 p0 = *(LAS f32x4*)fo, p1 = *(LAS f32x4*)(fo + 256), p2 = *(LAS f32x4*)(fo + 1024), p3 = *(LAS f32x4*)(fo + 1280);
    __builtin_amdgcn_sched_barrier(0);
    const float G0 = SUM4(p0), G1 = SUM4(p1), U0 = SUM4(p2), U1 = SUM4(p3);
    const unsigned gp = cvtpk(fmaf(-st[0], cg0, G0) * st[1], fmaf(-st[0], cg1, G1) * st[1]);
    const unsigned up = cvtpk(fmaf(-st[0], cu0, U0) * st[1], fmaf(-st[0], cu1, U1) * st[1]);
    const float g0 = bflo(gp), g1 = bfhi(gp);
    const unsigned sg = cvtpk(g0 * __builtin_amdgcn_rcpf(1.0f + __builtin_amdgcn_exp2f(-g0 * LOG2E)), g1 * __builtin_amdgcn_rcpf(1.0f + __builtin_amdgcn_exp2f(-g1 * LOG2E)));
    const float av0 = bflo(sg) * bflo(up), av1 = bfhi(sg) * bfhi(up);
    diag_chain_quad(acc, wd, cvtpk(av0, av0), cvtpk(av1, av1), dm0, dm1, dm2, dm3);
    mfma_drain(acc);
    PUBLISH_ACC(acc, so_);
    STAMP(6);
    poll_done(ha, hb);
  }
  DUMP_STAMPS();
}

extern "C" void kernel_launch(void* const* d_in, const int* in_sizes, int n_in, void* d_out, int out_size, void* d_ws, size_t ws_size, hipStream_t stream) {
  fused_block<<<NWG, NTHR, 0, stream>>>((const u16*)d_in[0], (const u16*)d_in[1], (const u16*)d_in[2], (const u16*)d_in[3], (unsigned char*)d_ws,
                                        (const u16*)d_in[4], (const u16*)d_in[6], (const u16*)d_in[7], (const u16*)d_in[8], (u16*)d_out, make_plan() TUNE_ARG);
}
```

```cpp
#pragma clang diagnostic ignored "-Wint-to-pointer-cast"
#include <hip/hip_runtime.h>
#include <hip/hip_bf16.h>
#include <cstdint>

typedef unsigned short u16;
typedef unsigned v4u __attribute__((ext_vector_type(4)));
typedef unsigned v2u __attribute__((ext_vector_type(2)));
typedef float f32x4 __attribute__((ext_vector_type(4)));
typedef float f32x2 __attribute__((ext_vector_type(2)));
typedef short s16x4 __attribute__((ext_vector_type(4)));
typedef __bf16 bf16x8_t __attribute__((ext_vector_type(8)));
typedef __bf16 bf16x2_t __attribute__((ext_vector_type(2)));
#define LAS __attribute__((address_space(3)))
#define GAS __attribute__((address_space(1)))

constexpr int D = 512, HQ = 32, HKV = 8, SK = 32768, DH = 128, F = 1792;
constexpr int NWG = 256, NTHR = 320, NSPLIT = 16;
struct Plan { unsigned e[32]; };
#define TUNE_PARAM
#define TUNE_ARG
#define ABL 0
#define TL_C 130
#define TL_CS 129
#define TL_B 128
#define TL_Q 124
#define TL_QS 120
#define T_WC 0
#define T_WB 0
#define T_L 3500
#define T_PD 150
#define T_P 580
static Plan make_plan() {
  Plan p;
  for (int b = 0; b < 2; ++b) {
    const int nc = 4 - b;
    int n[16], role[16], ix[16], used = 0, nn = 0;
    for (int sp = 0; sp < 16; ++sp) {
      if (sp < nc) { role[sp] = 1; ix[sp] = 2 * sp + b; n[sp] = sp == 0 ? TL_CS : TL_C; }
      else if (sp >= 4 && sp < 8) { role[sp] = 2; ix[sp] = 2 * (sp - 4) + b; n[sp] = TL_B; }
      else if (sp >= 12) { role[sp] = 4; ix[sp] = 2 * (sp - 12) + b; n[sp] = ix[sp] == 3 ? TL_QS : TL_Q; }
      else { role[sp] = (sp == 8 || sp == 9) ? 3 : 0; ix[sp] = role[sp] ? 2 * (sp - 8) + b : 0; n[sp] = 0; ++nn; }
      used += n[sp];
    }
    int rest = 2048 - used, t0 = 0;
    for (int sp = 0; sp < 16; ++sp) {
      if (n[sp] == 0) { n[sp] = (rest + nn - 1) / nn; rest -= n[sp]; --nn; }
      p.e[2 * sp + b] = (unsigned)t0 | ((unsigned)n[sp] << 11) | ((unsigned)role[sp] << 19) | ((unsigned)ix[sp] << 22);
      t0 += n[sp];
    }
  }
  return p;
}
#define HMASK 0x0F0F
#define HEAVY(d, s) ((HMASK >> (4 * ((d) & 3) + (s))) & 1)
#define NINST(d) (4 + __builtin_popcount((HMASK >> (4 * ((d) & 3))) & 15))
#define NPRE 13
#define TOUCH_EARLY 1
#define NLR 2
#define PD1 2
#define PD2 3
#define PD3 1
#define PD4 3
#define PSL 1
#define PACE_N 4
#define CODE_BYTES 31744
constexpr int NB = 64, NC = 56, NR = 64, NREP = 8;
constexpr int OFF_EPOCH = 0;
constexpr int OFF_HB = 0x190000;
constexpr int OFF_PART = 0x10000;
constexpr int OFF_P1 = 0x90000;
constexpr int OFF_P2 = 0xD0000;
constexpr int OFF_Q = 0x180000;
constexpr int WS_BYTES = 0x1A0000, OFF_DBG = 0x200000;
constexpr int AUX_NT = 2, AUX_SC1 = 16;
constexpr unsigned SPIN_CAP = 1u << 16;
constexpr int L_W = 0;
constexpr int WREG = 24576;
constexpr int L_QF = 4 * WREG;
constexpr int L_SC = L_QF + 1024;
constexpr int L_TAG = L_SC + 4 * 40 * 128;
constexpr int L_S = L_TAG + 16;
constexpr int L_XN = 0;
constexpr int L_RED = L_XN + 2048;
constexpr int L_MRG = L_RED + 2048;
constexpr int L_OB = L_MRG + 4 * 4 * 132 * 4;
constexpr int L_RR = L_OB + 512;
constexpr int L_ST = L_RR + 1024;
constexpr int S_BYTES = L_ST + 32;
constexpr int L_PF = L_S + S_BYTES;
constexpr int L_DBG = L_PF + 256;
constexpr int L_BYTES = L_DBG + 128;

__device__ __forceinline__ float bflo(unsigned v) { return __uint_as_float(v << 16); }
__device__ __forceinline__ float bfhi(unsigned v) { return __uint_as_float(v & 0xffff0000u); }
__device__ __forceinline__ unsigned cvtpk(float lo, float hi) { return __builtin_bit_cast(unsigned, __builtin_convertvector(f32x2{lo, hi}, bf16x2_t)); }
__device__ __forceinline__ float rbf(float x) { return bflo(cvtpk(x, x)); }
__device__ __forceinline__ bool tagged(v4u v, unsigned tag) { return (v[1] == tag) & (v[3] == tag); }
__device__ __forceinline__ float u2f(unsigned v) { return __uint_as_float(v); }
__device__ __forceinline__ unsigned f2u(float v) { return __float_as_uint(v); }

__device__ __forceinline__ float vmax(float a, float b) { return __builtin_elementwise_maximum(a, b); }
template <int CTRL> __device__ __forceinline__ float dpp(float x) { return __builtin_bit_cast(float, __builtin_amdgcn_mov_dpp(__builtin_bit_cast(int, x), CTRL, 0xf, 0xf, true)); }
template <int CTRL> __device__ __forceinline__ unsigned dppu(unsigned x) { return (unsigned)__builtin_amdgcn_mov_dpp((int)x, CTRL, 0xf, 0xf, true); }
constexpr int XOR1 = 0xB1, XOR2 = 0x4E, ROR8 = 0x128, ROR4 = 0x124, ROR12 = 0x12C;
__device__ __forceinline__ float sw32_sum(float a, float b) { auto r = __builtin_amdgcn_permlane32_swap(f2u(a), f2u(b), false, false); return u2f(r[0]) + u2f(r[1]); }
__device__ __forceinline__ float sw16_sum(float a, float b) { auto r = __builtin_amdgcn_permlane16_swap(f2u(a), f2u(b), false, false); return u2f(r[0]) + u2f(r[1]); }
__device__ __forceinline__ float sw32_max(float a, float b) { auto r = __builtin_amdgcn_permlane32_swap(f2u(a), f2u(b), false, false); return vmax(u2f(r[0]), u2f(r[1])); }
__device__ __forceinline__ float sw16_max(float a, float b) { auto r = __builtin_amdgcn_permlane16_swap(f2u(a), f2u(b), false, false); return vmax(u2f(r[0]), u2f(r[1])); }
__device__ __forceinline__ float wave_sum(float x) {
  x += dpp<XOR1>(x); x += dpp<XOR2>(x); x += dpp<ROR4>(x); x += dpp<ROR8>(x);
  x = sw16_sum(x, x); return sw32_sum(x, x);
}
__device__ __forceinline__ f32x4 mfma32(v4u a, v4u b, f32x4 c) { return __builtin_amdgcn_mfma_f32_16x16x32_bf16(__builtin_bit_cast(bf16x8_t, a), __builtin_bit_cast(bf16x8_t, b), c, 0, 0, 0); }
__device__ __forceinline__ f32x4 mfma16(v2u a, v2u b, f32x4 c) { return __builtin_amdgcn_mfma_f32_16x16x16bf16_1k(__builtin_bit_cast(s16x4, a), __builtin_bit_cast(s16x4, b), c, 0, 0, 0); }
__device__ __forceinline__ void barrier() { asm volatile("s_waitcnt lgkmcnt(0)\n\ts_barrier" ::: "memory"); }
__device__ __forceinline__ v4u tr_frag(LAS unsigned char* lo, LAS unsigned char* hi) {
  const v2u a = __builtin_bit_cast(v2u, __builtin_amdgcn_ds_read_tr16_b64_v4i16((LAS s16x4*)lo));
  const v2u b = __builtin_bit_cast(v2u, __builtin_amdgcn_ds_read_tr16_b64_v4i16((LAS s16x4*)hi));
  return v4u{a[0], a[1], b[0], b[1]};
}

#define AND4(T0, T1, T2, T3, SRC, MOD, SFX) "v_and_b32" SFX " " T0 ", " SRC ", %[m0]" MOD "\n\tv_and_b32" SFX " " T1 ", " SRC ", %[m1]" MOD "\n\tv_and_b32" SFX " " T2 ", " SRC ", %[m2]" MOD "\n\tv_and_b32" SFX " " T3 ", " SRC ", %[m3]" MOD "\n\t"
#define ANDA(SRC, MOD, SFX) AND4("v240", "v241", "v242", "v243", SRC, MOD, SFX)
#define ANDB(SRC, MOD, SFX) AND4("v244", "v245", "v246", "v247", SRC, MOD, SFX)
#define MFA(W) "v_mfma_f32_16x16x32_bf16 %[acc], " W ", v[240:243], %[acc]\n\t"
#define MFB(W) "v_mfma_f32_16x16x32_bf16 %[acc], " W ", v[244:247], %[acc]\n\t"
#define CHAIN_CLOB "v240", "v241", "v242", "v243", "v244", "v245", "v246", "v247"
#define CHAIN_W(W) [w0] "v"(W[0]), [w1] "v"(W[1]), [w2] "v"(W[2]), [w3] "v"(W[3]), [w4] "v"(W[4]), [w5] "v"(W[5]), [w6] "v"(W[6]), [w7] "v"(W[7])
__device__ __forceinline__ void diag_chain_regs(f32x4& acc, const v4u* W, v4u pa, v4u pb, unsigned m0, unsigned m1, unsigned m2, unsigned m3) {
  asm volatile(ANDA("%[p0]", "", "") ANDB("%[p1]", "", "") MFA("%[w0]") ANDA("%[p2]", "", "") MFB("%[w1]") ANDB("%[p3]", "", "") MFA("%[w2]") ANDA("%[p4]", "", "") MFB("%[w3]")
               ANDB("%[p5]", "", "") MFA("%[w4]") ANDA("%[p6]", "", "") MFB("%[w5]") ANDB("%[p7]", "", "") MFA("%[w6]") "s_nop 0\n\t" MFB("%[w7]")
               : [acc] "+v"(acc)
               : CHAIN_W(W), [p0] "v"(pa[0]), [p1] "v"(pa[1]), [p2] "v"(pa[2]), [p3] "v"(pa[3]), [p4] "v"(pb[0]), [p5] "v"(pb[1]), [p6] "v"(pb[2]), [p7] "v"(pb[3]),
                 [m0] "v"(m0), [m1] "v"(m1), [m2] "v"(m2), [m3] "v"(m3)
               : CHAIN_CLOB);
}
#define QP(t) " quad_perm:[" #t "," #t "," #t "," #t "] row_mask:0xf bank_mask:0xf"
__device__ __forceinline__ void diag_chain_quad(f32x4& acc, const v4u* W, unsigned pa, unsigned pb, unsigned m0, unsigned m1, unsigned m2, unsigned m3) {
  asm volatile("s_nop 1\n\t" ANDA("%[pa]", QP(0), "_dpp") ANDB("%[pa]", QP(1), "_dpp") MFA("%[w0]") ANDA("%[pa]", QP(2), "_dpp") MFB("%[w1]") ANDB("%[pa]", QP(3), "_dpp") MFA("%[w2]")
               ANDA("%[pb]", QP(0), "_dpp") MFB("%[w3]") ANDB("%[pb]", QP(1), "_dpp") MFA("%[w4]") ANDA("%[pb]", QP(2), "_dpp") MFB("%[w5]") ANDB("%[pb]", QP(3), "_dpp") MFA("%[w6]")
               "s_nop 0\n\t" MFB("%[w7]")
               : [acc] "+v"(acc)
               : CHAIN_W(W), [pa] "v"(pa), [pb] "v"(pb), [m0] "v"(m0), [m1] "v"(m1), [m2] "v"(m2), [m3] "v"(m3)
               : CHAIN_CLOB);
}
__device__ __forceinline__ void mfma_drain(f32x4& acc) { asm volatile("s_nop 7" : "+v"(acc)); }

__device__ __forceinline__ v4u ln_row(v4u xv) {
  float x[8];
#pragma unroll
  for (int i = 0; i < 4; ++i) { x[2 * i] = bflo(xv[i]); x[2 * i + 1] = bfhi(xv[i]); }
  const float mean = wave_sum(((x[0] + x[1]) + (x[2] + x[3])) + ((x[4] + x[5]) + (x[6] + x[7]))) * (1.0f / D);
  float vs = 0.f;
#pragma unroll
  for (int i = 0; i < 8; ++i) { const float d = x[i] - mean; vs = fmaf(d, d, vs); }
  const float var = wave_sum(vs) * (1.0f / D);
  const float mb = rbf(mean);
  const float rs = __builtin_amdgcn_rcpf(rbf(__builtin_amdgcn_sqrtf(rbf(rbf(var) + 1e-5f))));
  v4u r;
#pragma unroll
  for (int i = 0; i < 4; ++i) { const unsigned d = cvtpk(x[2 * i] - mb, x[2 * i + 1] - mb); r[i] = cvtpk(bflo(d) * rs, bfhi(d) * rs); }
  return r;
}
__device__ __forceinline__ void glds16(const u16* gsrc, unsigned lds_dst) {
  unsigned keep;
  asm volatile("s_mov_b32 %0, m0\n\ts_mov_b32 m0, %2\n\ts_nop 0\n\tglobal_load_lds_dwordx4 %1, off nt\n\ts_mov_b32 m0, %0" : "=&s"(keep) : "v"(gsrc), "s"(lds_dst) : "memory");
}

__device__ __forceinline__ unsigned mixtag(unsigned t) { t *= 0x9E3779B1u; return t ^ (t >> 15); }
#define PK24(x, tb) (((f2u(x) + 0x80u) & 0xffffff00u) | (tb))
#define UP24(x) u2f((x) & 0xffffff00u)
__device__ __forceinline__ unsigned lowbytes(v4u v) { return __builtin_amdgcn_perm(v[1], v[0], 0x0c0c0400u) | __builtin_amdgcn_perm(v[3], v[2], 0x04000c0cu); }
template <int PD_, int TSH>
__device__ __forceinline__ void poll2(const unsigned char* ws, int o0, int o1, unsigned tag, v4u& r0, v4u& r1) {
  r0 = v4u{~tag, ~tag, ~tag, ~tag}; r1 = r0;
#define PASS() asm volatile("global_load_dwordx4 %0, %2, %4 sc1\n\tglobal_load_dwordx4 %1, %3, %4 sc1" : "+v"(r0), "+v"(r1) : "v"(o0), "v"(o1), "s"(ws) : "memory")
#pragma unroll
  for (int k = 0; k < PD_ - 1; ++k) { PASS(); __builtin_amdgcn_s_sleep(PSL); }
#pragma unroll 1
  for (unsigned k = 0; k < SPIN_CAP; ++k) {
    PASS();
    asm volatile("s_waitcnt vmcnt(%2)" : "+v"(r0), "+v"(r1) : "i"((PD_ - 1) * 2) : "memory");
    if (TSH == 16) { if (__all((__builtin_amdgcn_perm(r0[3], r0[1], 0x07060302u) == tag) & (__builtin_amdgcn_perm(r1[3], r1[1], 0x07060302u) == tag))) break; }
    else if (TSH < 0) { if (__all((lowbytes(r0) == tag) & (lowbytes(r1) == tag))) break; }
    else if (__all((((r0[1] ^ tag) | (r0[3] ^ tag) | (r1[1] ^ tag) | (r1[3] ^ tag)) >> (TSH < 0 ? 0 : TSH)) == 0u)) break;
    if (PD_ > 1) __builtin_amdgcn_s_sleep(PSL);
  }
#undef PASS
  asm volatile("s_nop 3 ; POLL_END" : "+v"(r0), "+v"(r1));
}
__device__ __forceinline__ void poll_done(v4u& r0, v4u& r1) { asm volatile("s_waitcnt vmcnt(0)" : "+v"(r0), "+v"(r1) :: "memory"); }

#define STAMP(i) do { } while (0)
#define DUMP_STAMPS() do { } while (0)
#define LD128(off) __builtin_amdgcn_raw_buffer_load_b128(rw, (off), 0, AUX_SC1)
#define ST128(v, off) __builtin_amdgcn_raw_buffer_store_b128((v), rw, (off), 0, AUX_SC1)
#define SPIN_LOOP _Pragma("unroll 1") for (unsigned spins = 0; spins < SPIN_CAP; ++spins)
#define NOHOIST() asm volatile("" ::: "memory")

__global__ void __launch_bounds__(NTHR, 2) fused_block(const u16* x_, const u16* kc_, const u16* vc_, const u16* wq_, unsigned char* ws_,
                                                       const u16* wo_, const u16* wgate_, const u16* wup_, const u16* wdown_, u16* out_, Plan plan_ TUNE_PARAM) {
  typedef unsigned long long u64x8 __attribute__((ext_vector_type(8)));
  typedef unsigned long long u64x2 __attribute__((ext_vector_type(2)));
  u64x8 ka; u64x2 kb2; unsigned pe;
  asm volatile("s_load_dwordx16 %0, %3, 0x0\n\ts_load_dwordx4 %1, %3, 0x40\n\ts_load_dword %2, %3, %4 offset:0x50"
               : "=&s"(ka), "=&s"(kb2), "=&s"(pe) : "s"(__builtin_amdgcn_kernarg_segment_ptr()), "s"((blockIdx.x >> 3) * 4));
  __shared__ __attribute__((aligned(16))) unsigned char smem[L_BYTES];
  LAS unsigned char* L = (LAS unsigned char*)smem;
  STAMP(0);
  const unsigned tstart = (unsigned)__builtin_amdgcn_s_memrealtime();
  const int wg = blockIdx.x, xcd = wg & 7, idx = wg >> 3;
  const int kvh = xcd, b = idx & 1, sp = idx >> 1;
  int lane = threadIdx.x & 63;
  const int w = __builtin_amdgcn_readfirstlane(threadIdx.x >> 6);
  int c = lane & 15, g = lane >> 4;
  if (w == 4 && !(ABL & 32)) {
    unsigned long long pc;
    asm volatile("s_getpc_b64 %0" : "=s"(pc));
    if ((lane * 32 + idx) * 128 < CODE_BYTES) {
      unsigned keep;
      asm volatile("s_mov_b32 %0, m0\n\ts_mov_b32 m0, %3\n\ts_nop 0\n\tglobal_load_lds_dword %1, %2\n\ts_mov_b32 m0, %0"
                   : "=&s"(keep) : "v"((lane * 32 + idx) * 128), "s"(pc & ~255ull), "s"((unsigned)(uintptr_t)L + L_PF) : "memory");
    }
  }
  int koff = (c >> 2) * 1024 + g * 64 + (c & 3) * 16;
  unsigned pbase = (unsigned)(b * HKV + kvh) * (SK * DH * 2);
  asm volatile("s_waitcnt lgkmcnt(0)" : "+s"(ka), "+s"(kb2), "+s"(pe), "+v"(koff), "+s"(pbase));
  const u16 *x = (const u16*)ka[0], *wq = (const u16*)ka[3], *wo = (const u16*)ka[5], *wgate = (const u16*)ka[6], *wup = (const u16*)ka[7], *wdown = (const u16*)kb2[0];
  unsigned char* ws = (unsigned char*)ka[4];
  u16* out = (u16*)kb2[1];
  const int tile0 = pe & 2047, ntile = (pe >> 11) & 255;
  const __amdgpu_buffer_rsrc_t rk = __builtin_amdgcn_make_buffer_rsrc((void*)(ka[1] + pbase), 0, SK * DH * 2, 0x00020000);
  const unsigned sbase = (unsigned)(tile0 + w) * 4096u;
  v4u kb[NPRE + 3][4];
#define SB() __builtin_amdgcn_sched_barrier(0)
#define ISSUEK(d, j) do { _Pragma("unroll") for (int s = 0; s < 4; ++s) { \
      if (!HEAVY(d, s)) kb[d][s] = __builtin_amdgcn_raw_buffer_load_b128(rk, koff + s * 256, sbase + (unsigned)(j) * 16384u, AUX_NT); \
      else { const v2u lo_ = __builtin_amdgcn_raw_buffer_load_b64(rk, koff + s * 256, sbase + (unsigned)(j) * 16384u, AUX_NT); NOHOIST(); SB(); \
             const v2u hi_ = __builtin_amdgcn_raw_buffer_load_b64(rk, koff + s * 256 + 8, sbase + (unsigned)(j) * 16384u, AUX_NT); NOHOIST(); \
             kb[d][s] = v4u{lo_[0], lo_[1], hi_[0], hi_[1]}; } } SB(); } while (0)
  const int role = (pe >> 19) & 7, rix = pe >> 22;
  const bool qrole = role == 4;
#define CI() ({ int r_ = rix; asm volatile("" : "+s"(r_)); const int cn_ = xcd * 5 + r_ - (xcd < 7 ? 2 : 0); (xcd < 7 && r_ < 2) ? 8 * xcd + 6 + r_ : cn_ + 2 * ((cn_ * 43) >> 8); })
  const int bi = xcd * 8 + rix, rj = xcd * 4 + rix, qi = bi;
  const __amdgpu_buffer_rsrc_t rw = __builtin_amdgcn_make_buffer_rsrc((void*)ws, 0, WS_BYTES, 0x00020000);
  const unsigned lds0 = (unsigned)(uintptr_t)L;
  LAS unsigned char* S = L + L_S;
#define WAIT_UNTIL(t, slp) do { while ((int)((unsigned)__builtin_amdgcn_s_memrealtime() - (tstart + (unsigned)(t))) < 0) __builtin_amdgcn_s_sleep(slp); } while (0)
  int q4 = (lane >> 2) & 3, p4 = lane & 3;
  constexpr float SCALE = 0.088388347648318440f, LOG2E = 1.4426950408889634f;
  unsigned tag;

  if (w == 4) {
    __builtin_amdgcn_s_setprio(3);
#define TOUCH() \
    {     \
      const int ci = CI(); \
      const unsigned long long vb_ = ka[2] + pbase + (unsigned)tile0 * 4096u, ke_ = ka[1] + pbase + (unsigned)(tile0 + ntile) * 4096u - 128u; \
      const unsigned long long wa_ = role == 1 ? ka[6] + ci * 64 : role == 2 ? ka[5] + bi * 65536 : kb2[1]; \
      const unsigned long long ad = lane == 0 ? vb_ : lane == 1 ? vb_ + (unsigned)ntile * 4096u - 128u : lane == 2 ? ke_ : lane == 3 ? wa_ \
                                  : lane == 4 ? wa_ + 511 * F * 2 : lane == 5 ? ka[7] + ci * 64 : lane == 6 ? ka[7] + ci * 64 + 511 * F * 2 : kb2[0] + ci * 32768; \
      if (lane < (role == 1 ? 8 : 4)) { \
        unsigned keep; \
        asm volatile("s_mov_b32 %0, m0\n\ts_mov_b32 m0, %2\n\ts_nop 0\n\tglobal_load_lds_dword %1, off nt\n\ts_mov_b32 m0, %0" : "=&s"(keep) : "v"(ad), "s"(lds0 + L_PF) : "memory"); \
      } \
    }
    if (qrole) {
      const v4u x0 = *(const GAS v4u*)(x + lane * 8), x1 = *(const GAS v4u*)(x + D + lane * 8);
      tag = __builtin_amdgcn_raw_buffer_load_b32(rw, OFF_EPOCH, 0, AUX_SC1) + 1u;
      const v4u n0 = ln_row(x0), n1 = ln_row(x1);
      *(LAS v4u*)(S + L_XN + lane * 16) = n0; *(LAS v4u*)(S + L_XN + 1024 + lane * 16) = n1;
      STAMP(9);
      barrier();
      if (TOUCH_EARLY) TOUCH();
      barrier();
      const f32x2 s = (*(LAS f32x2*)(S + L_RED + lane * 8) + *(LAS f32x2*)(S + L_RED + 512 + lane * 8)) + (*(LAS f32x2*)(S + L_RED + 1024 + lane * 8) + *(LAS f32x2*)(S + L_RED + 1536 + lane * 8));
      __builtin_amdgcn_raw_buffer_store_b64(v2u{cvtpk(s[0], s[1]), tag}, rw, OFF_Q + ((lane >> 5) * 2048 + qi * 32 + (lane & 31)) * 8, 0, AUX_SC1);
    } else {
      if (TOUCH_EARLY) TOUCH();
      tag = __builtin_amdgcn_raw_buffer_load_b32(rw, OFF_EPOCH, 0, AUX_SC1) + 1u;
    }
    STAMP(1);
    if (lane == 0) *(LAS unsigned*)(L + L_TAG) = tag;
    {
      const int qo = OFF_Q + (b * 2048 + kvh * 256) * 8 + lane * 16;
      v4u q0, q1;
      bool ok0 = false, ok1 = false;
      WAIT_UNTIL(T_P, 2);
      SPIN_LOOP {
        NOHOIST();
        if (!ok0) q0 = LD128(qo);
        if (!ok1) q1 = LD128(qo + 1024);
        ok0 = tagged(q0, tag); ok1 = tagged(q1, tag);
        if (__all(ok0 & ok1)) break;
      }
      *(LAS v2u*)(L + L_QF + lane * 8) = v2u{q0[0], q0[2]};
      *(LAS v2u*)(L + L_QF + 512 + lane * 8) = v2u{q1[0], q1[2]};
      STAMP(2);
      barrier();
    }
    if (!TOUCH_EARLY) TOUCH();
    asm volatile("s_waitcnt vmcnt(0)" ::: "memory");
    return;
  }
  const int nstep = (ntile - w + 3) >> 2;
  const __amdgpu_buffer_rsrc_t rv = __builtin_amdgcn_make_buffer_rsrc((void*)(ka[2] + pbase), 0, SK * DH * 2, 0x00020000);
#define VMWAIT(n) do { asm volatile("s_waitcnt vmcnt(%0)" :: "i"(n) : "memory"); SB(); } while (0)
#define RELANE() do { asm volatile("v_mbcnt_lo_u32_b32 %0, -1, 0\n\tv_mbcnt_hi_u32_b32 %0, -1, %0" : "=v"(lane)); c = lane & 15; g = lane >> 4; } while (0)
  int wp = 0;
#define WUPTO(n, pace) do { _Pragma("unroll 1") while (wp < (n)) { \
      const u16* src_ = role == 2 ? wo + (size_t)(bi * 64 + 4 * wp + g) * D + 128 * w + 8 * c \
                      : wp < 16 ? (wp < 8 ? wgate : wup) + (size_t)(128 * w + 16 * (wp & 7) + (lane >> 2)) * F + ci * 32 + (((lane & 3) ^ ((lane >> 4) & 2)) * 8) \
                                : wdown + (size_t)(ci * 32 + 4 * (wp & 7) + g) * D + 128 * w + 8 * c; \
      VMWAIT(pace); glds16(src_, lds0 + L_W + w * WREG + wp * 1024); ++wp; } } while (0)
  if (qrole) {
    const int r8 = lane >> 3;
    const u16* p = wq + (size_t)(128 * w + r8) * (HQ * DH) + qi * 64;
    const int ch0 = ((lane & 7) ^ (r8 & 2)) * 8;
#pragma unroll 1
    for (int t = 0; t < 16; t += 2) {
      glds16(p + ch0, lds0 + L_W + w * WREG + t * 1024);
      glds16(p + 8 * (HQ * DH) + (ch0 ^ 32), lds0 + L_W + w * WREG + t * 1024 + 1024);
      p += 16 * (HQ * DH);
    }
    STAMP(7);
  }
  STAMP(11);
  if (!qrole) { ISSUEK(0, 0); ISSUEK(1, 1); }
  else {
    const int sw = (q4 & 2) | ((g & 1) << 2);
    v4u fr[16];
#define QFRAGS(ks) do { _Pragma("unroll") for (int t = 0; t < 4; ++t) { \
        LAS unsigned char* a = L + L_W + w * WREG + (ks) * 4096 + g * 1024 + q4 * 128 + (p4 & 1) * 8 + (((2 * t + (p4 >> 1)) ^ sw) * 16); fr[(ks) * 4 + t] = tr_frag(a, a + 512); } } while (0)
    VMWAIT(8); QFRAGS(0); QFRAGS(1); ISSUEK(0, 0);
    VMWAIT(4 + NINST(0)); QFRAGS(2); ISSUEK(1, 1);
    barrier();
    f32x4 qa[4];
    v4u xb[4];
#pragma unroll
    for (int ks = 0; ks < 4; ++ks) xb[ks] = *(LAS v4u*)(S + L_XN + (c & 1) * 1024 + (128 * w + 32 * ks + 8 * g) * 2);
#pragma unroll
    for (int t = 0; t < 4; ++t) qa[t] = f32x4{0.f, 0.f, 0.f, 0.f};
#pragma unroll
    for (int ks = 0; ks < 3; ++ks)
#pragma unroll
      for (int t = 0; t < 4; ++t) qa[t] = mfma32(fr[ks * 4 + t], xb[ks], qa[t]);
    VMWAIT(NINST(0) + NINST(1)); STAMP(8); QFRAGS(3);
#pragma unroll
    for (int t = 0; t < 4; ++t) qa[t] = mfma32(fr[12 + t], xb[3], qa[t]);
    if (c < 2) {
#pragma unroll
      for (int t = 0; t < 4; ++t) *(LAS f32x4*)(S + L_RED + w * 512 + c * 256 + t * 64 + g * 16) = qa[t];
    }
    STAMP(1);
    barrier();
  }
  const int rem = nstep & 3, remk = (nstep - NPRE) & 3;
#pragma unroll
  for (int d = 2; d < NPRE; ++d) { VMWAIT(NINST(d - 1)); ISSUEK(d, d); }
  STAMP(10);
  barrier();
  STAMP(2);
  const v4u qf = *(LAS v4u*)(L + L_QF + (c >> 2) * 256 + g * 64 + (c & 3) * 16);
  tag = __builtin_amdgcn_readfirstlane(*(LAS unsigned*)(L + L_TAG));

  LAS unsigned char* scp = L + L_SC + w * 5120 + g * 32 + (c >> 2) * 8;
  float m = -INFINITY;
  f32x4 sc;
#define QK(d) do { \
    _Pragma("unroll") for (int s = 0; s < 4; ++s) { const f32x4 dd = mfma32(kb[d][s], qf, f32x4{0.f, 0.f, 0.f, 0.f}); sc[s] = (c & 2) ? ((c & 1) ? dd[3] : dd[2]) : ((c & 1) ? dd[1] : dd[0]); } \
    asm volatile("" : "+v"(sc) :: "memory"); SB(); } while (0)
#define SCORE(d, j) do { \
    _Pragma("unroll") for (int s = 0; s < 4; ++s) { float t_ = sc[s]; t_ += dpp<XOR1>(t_); t_ += dpp<XOR2>(t_); sc[s] = t_; } \
    const unsigned r01 = cvtpk(sc[0], sc[1]), r23 = cvtpk(sc[2], sc[3]); \
    const unsigned t01 = cvtpk(bflo(r01) * SCALE, bfhi(r01) * SCALE), t23 = cvtpk(bflo(r23) * SCALE, bfhi(r23) * SCALE); \
    m = vmax(vmax(m, vmax(bflo(t01), bfhi(t01))), vmax(bflo(t23), bfhi(t23))); \
    *(LAS v2u*)(scp + (j) * 128) = v2u{t01, t23}; asm volatile("" : "+v"(m)); SB(); } while (0)
#pragma unroll
  for (int d = 0; d < 4; ++d) { QK(d); ISSUEK(d, NPRE + remk + d); SCORE(d, d); }
#pragma unroll
  for (int d = 4; d < NPRE; ++d) { QK(d); SCORE(d, d); }
#pragma unroll
  for (int d = NPRE; d < NPRE + 3; ++d) if (d - NPRE < remk) ISSUEK(d, d);
  int j = NPRE + remk;
#pragma unroll 1
  do {
#pragma unroll
    for (int d = 0; d < 4; ++d) { QK(d); ISSUEK(d, j + 4 + d); SCORE(d, j + d); }
    j += 4;
  } while (j + 4 < nstep);
#pragma unroll
  for (int d = NPRE; d < NPRE + 3; ++d) if (d - NPRE < remk) { QK(d); SCORE(d, d); }
  const int voff = g * 1024 + c * 16;
  v2u vl[7][4], vh[7][4];
#define ISSUEV(d, j) do { _Pragma("unroll") for (int s = 0; s < 4; ++s) { \
      if (!HEAVY(d, s)) { const v4u t_ = __builtin_amdgcn_raw_buffer_load_b128(rv, voff + s * 256, sbase + (unsigned)(j) * 16384u, AUX_NT); vl[d][s] = v2u{t_[0], t_[1]}; vh[d][s] = v2u{t_[2], t_[3]}; } \
      else { vl[d][s] = __builtin_amdgcn_raw_buffer_load_b64(rv, voff + s * 256, sbase + (unsigned)(j) * 16384u, AUX_NT); NOHOIST(); SB(); \
             vh[d][s] = __builtin_amdgcn_raw_buffer_load_b64(rv, voff + s * 256 + 8, sbase + (unsigned)(j) * 16384u, AUX_NT); NOHOIST(); } } SB(); } while (0)
#pragma unroll
  for (int d = 0; d < 4; ++d) { QK(d); ISSUEV(d, d); SCORE(d, j + d); }
#pragma unroll
  for (int d = 4; d < 7; ++d) if (d - 4 < rem) ISSUEV(d, d);
  STAMP(9);
  m = sw16_max(m, m); m = sw32_max(m, m);
  const float mlog = m * LOG2E;
  const unsigned m0 = (c & 3) == 0 ? 0xffffu : (c & 3) == 1 ? 0xffff0000u : 0u;
  const unsigned m1 = (c & 3) == 2 ? 0xffffu : (c & 3) == 3 ? 0xffff0000u : 0u;
  f32x4 acc0 = {0.f, 0.f, 0.f, 0.f}, acc1 = {0.f, 0.f, 0.f, 0.f};
  float l = 0.f;
#define PCALC(bb, j) do { \
    const v2u t_ = *(LAS v2u*)(scp + (j) * 128); \
    const float sv[4] = {bflo(t_[0]), bfhi(t_[0]), bflo(t_[1]), bfhi(t_[1])}; \
    _Pragma("unroll") for (int i = 0; i < 4; ++i) { const float p_ = __builtin_amdgcn_exp2f(fmaf(sv[i], LOG2E, -mlog)); const unsigned pp = cvtpk(p_, p_); l += bflo(pp); bb[i] = v2u{pp & m0, pp & m1}; } } while (0)
#define PMFMA(d, bb) do { SB(); \
    _Pragma("unroll") for (int i = 0; i < 4; ++i) { acc0 = mfma16(vl[d][i], bb[i], acc0); acc1 = mfma16(vh[d][i], bb[i], acc1); } \
    asm volatile("" : "+v"(acc0), "+v"(acc1) :: "memory"); SB(); } while (0)
#define PV(d, j) do { v2u bb_[4]; PCALC(bb_, j); PMFMA(d, bb_); } while (0)
#pragma unroll
  for (int d = 0; d < 4; ++d) { PV(d, d); ISSUEV(d, 4 + rem + d); }
#pragma unroll
  for (int d = 4; d < 7; ++d) if (d - 4 < rem) PV(d, d);
  j = 4 + rem;
#pragma unroll 1
  do {
#pragma unroll
    for (int d = 0; d < 4; ++d) { PV(d, j + d); ISSUEV(d, j + 4 + d); }
    j += 4;
  } while (j + 4 + 4 * NLR < nstep);
  unsigned bal[2] = {0u, 0u};
  const int blo = (lane & 31) * 128;
#pragma unroll
  for (int r = 0; r < NLR; ++r) {
#pragma unroll
    for (int d = 0; d < 4; ++d) {
      if (r > 0 && !(d & 1)) asm volatile("" :: "v"(bal[d >> 1]));
      PV(d, j + d);
#pragma unroll
      for (int s = 0; s < 4; ++s) { const v4u t_ = __builtin_amdgcn_raw_buffer_load_b128(rv, voff + s * 256, sbase + (unsigned)(j + 4 + d) * 16384u, AUX_NT); vl[d][s] = v2u{t_[0], t_[1]}; vh[d][s] = v2u{t_[2], t_[3]}; }
      SB();
      if (!(d & 1)) {
        const bool late = (int)((unsigned)__builtin_amdgcn_s_memrealtime() - (tstart + T_L)) > 0;
        bal[d >> 1] = __builtin_amdgcn_raw_buffer_load_b32(rv, late ? 0 : blo, sbase + (unsigned)(j + 4 + d) * 16384u, AUX_NT); SB();
      }
    }
    j += 4;
  }
  asm volatile("v_mbcnt_lo_u32_b32 %0, -1, 0\n\tv_mbcnt_hi_u32_b32 %0, -1, %0" : "=v"(lane));
  c = lane & 15; g = lane >> 4; q4 = (lane >> 2) & 3; p4 = lane & 3;
  const int tid = w * 64 + lane;
#define PIN(x) asm volatile("" : "+v"(x))
  const unsigned tagh = mixtag(tag);
  v2u lb[4][4];
#pragma unroll
  for (int d = 0; d < 4; ++d) PCALC(lb[d], j + d);
  l = sw16_sum(l, l); l = sw32_sum(l, l);
  *(LAS f32x2*)(S + L_RED + (c >> 2) * 32 + w * 8) = f32x2{mlog, l};
  barrier();
  const int hd = 2 * (w & 1) + (lane >> 5);
  float fw; unsigned t1w, t3w;
  {
#define MERGE_ML(h, M_, L_) do { const f32x4 u0_ = *(LAS f32x4*)(S + L_RED + (h) * 32), u1_ = *(LAS f32x4*)(S + L_RED + (h) * 32 + 16); \
      M_ = vmax(vmax(u0_[0], u0_[2]), vmax(u1_[0], u1_[2])); \
      L_ = fmaf(__builtin_amdgcn_exp2f(u0_[2] - M_), u0_[3], __builtin_amdgcn_exp2f(u0_[0] - M_) * u0_[1]) + fmaf(__builtin_amdgcn_exp2f(u1_[2] - M_), u1_[3], __builtin_amdgcn_exp2f(u1_[0] - M_) * u1_[1]); } while (0)
    float M1, L1, M2, L2;
    MERGE_ML(c >> 2, M1, L1); MERGE_ML(hd, M2, L2);
    fw = __builtin_amdgcn_exp2f(mlog - M1) * __builtin_amdgcn_rcpf(L1);
    const unsigned lse = f2u(M2 + __builtin_amdgcn_logf(L2));
    t1w = __builtin_amdgcn_perm(tagh, lse, 0x05040100u); t3w = __builtin_amdgcn_perm(tagh, lse, 0x07060302u);
  }
  unsigned mw = lds0 + L_S + L_MRG + w * 2048 + lane * 16, mr = lds0 + L_S + L_MRG + ((lane >> 2) & 1) * 1024 + (((lane >> 3) & 3) * 16 + hd * 4 + (lane & 3)) * 16;
  int pao = OFF_PART + ((b * HQ + kvh * 4 + hd) * NSPLIT + sp) * 512 + (lane & 31) * 16;
  PIN(mw); PIN(mr); PIN(pao); PIN(fw); PIN(t1w); PIN(t3w);
#pragma unroll
  for (int d = 0; d < 4; ++d) { PMFMA(d, lb[d]); if (d == 2) asm volatile("" :: "v"(bal[0]), "v"(bal[1])); }
  STAMP(3);
  *(LAS f32x4*)mw = acc0 * fw; *(LAS f32x4*)(mw + 1024) = acc1 * fw;
  barrier();
  if (w < 2) {
    const f32x4 o = (*(LAS f32x4*)mr + *(LAS f32x4*)(mr + 2048)) + (*(LAS f32x4*)(mr + 4096) + *(LAS f32x4*)(mr + 6144));
    ST128((v4u{cvtpk(o[0], o[1]), t1w, cvtpk(o[2], o[3]), t3w}), pao);
  }
  STAMP(4);
  const int ci = CI();
  WUPTO(role == 1 ? 24 : role == 2 ? 16 : 0, 40);
  unsigned dm0 = (c & 7) == 0 ? 0xffffu : (c & 7) == 1 ? 0xffff0000u : 0u, dm1 = (c & 7) == 2 ? 0xffffu : (c & 7) == 3 ? 0xffff0000u : 0u,
           dm2 = (c & 7) == 4 ? 0xffffu : (c & 7) == 5 ? 0xffff0000u : 0u, dm3 = (c & 7) == 6 ? 0xffffu : (c & 7) == 7 ? 0xffff0000u : 0u;
  unsigned tb0 = tagh & 0xffu, tb1 = (tagh >> 8) & 0xffu, tb2 = (tagh >> 16) & 0xffu, tb3 = tagh >> 24;
#define PUBLISH_ACC(acc, off) ST128((v4u{PK24(acc[0], tb0), PK24(acc[1], tb1), PK24(acc[2], tb2), PK24(acc[3], tb3)}), (off))

  if (role == 2) {
    unsigned wra = lds0 + L_W + w * WREG + lane * 16;
    PIN(wra);
    const int head = bi >> 1;
    const int po = OFF_PART + (((lane >> 5) * HQ + head) * NSPLIT + ((lane >> 2) & 7)) * 512 + (bi & 1) * 256 + w * 64 + (lane & 3) * 16;
    unsigned ow = lds0 + L_S + L_OB + ((g >> 1) * 4 + (lane & 3)) * 64 + (8 * (w >> 1) + 2 * ((g & 1) + ((lane & 4) >> 1)) + (w & 1)) * 4, orr = lds0 + L_S + L_OB + ((c >> 3) * 4 + g) * 64;
    int so_ = OFF_P1 + bi * 4096 + tid * 16;
    f32x4 acc = {0.f, 0.f, 0.f, 0.f};
    PIN(ow); PIN(orr); PIN(so_); PIN(dm0); PIN(dm1); PIN(dm2); PIN(dm3); PIN(tb0); PIN(tb1); PIN(tb2); PIN(tb3); PIN(acc);
    v4u a0, a1;
    poll2<PD1, 16>(ws, po, po + 8 * 512, tagh, a0, a1);
    STAMP(5);
    v4u wr[16];
#pragma unroll
    for (int i = 0; i < 16; ++i) wr[i] = *(LAS v4u*)(wra + i * 1024);
    SB();
    const float l0 = u2f(__builtin_amdgcn_perm(a0[3], a0[1], 0x05040100u)), l1 = u2f(__builtin_amdgcn_perm(a1[3], a1[1], 0x05040100u));
    float xm = vmax(l0, l1);
    xm = vmax(vmax(xm, dpp<ROR4>(xm)), vmax(dpp<ROR8>(xm), dpp<ROR12>(xm)));
    xm = sw16_max(xm, xm);
    const float w0 = __builtin_amdgcn_exp2f(l0 - xm), w1 = __builtin_amdgcn_exp2f(l1 - xm);
    float u0 = sw16_sum(fmaf(w1, bflo(a1[0]), w0 * bflo(a0[0])), fmaf(w1, bfhi(a1[0]), w0 * bfhi(a0[0])));
    float u1 = sw16_sum(fmaf(w1, bflo(a1[2]), w0 * bflo(a0[2])), fmaf(w1, bfhi(a1[2]), w0 * bfhi(a0[2])));
    float ws_ = w0 + w1;
    ws_ = sw16_sum(ws_, ws_);
    u0 += dpp<ROR8>(u0); u1 += dpp<ROR8>(u1); ws_ += dpp<ROR8>(ws_);
    u0 += dpp<ROR4>(u0); u1 += dpp<ROR4>(u1); ws_ += dpp<ROR4>(ws_);
    const float uo = ((lane & 4) ? u1 : u0) * __builtin_amdgcn_rcpf(ws_);
    *(LAS unsigned*)ow = cvtpk(uo, uo);
    barrier();
    const v4u pv0 = *(LAS v4u*)orr, pv1 = *(LAS v4u*)(orr + 16), pv2 = *(LAS v4u*)(orr + 32), pv3 = *(LAS v4u*)(orr + 48);
    diag_chain_regs(acc, wr, pv0, pv1, dm0, dm1, dm2, dm3);
    diag_chain_regs(acc, wr + 8, pv2, pv3, dm0, dm1, dm2, dm3);
    mfma_drain(acc);
    PUBLISH_ACC(acc, so_);
    STAMP(6);
    poll_done(a0, a1);
  } else if (role == 3) {
    const int rb = rj & 1, col0 = (rj >> 1) * 32;
    unsigned xp = *(const GAS unsigned*)(x + rb * D + col0 + 2 * c);
    asm volatile("" : "+v"(xp));
    const int pp = lane >> 3;
    float h0, h1;
    unsigned rw_ = lds0 + L_S + L_RR + w * 128 + (8 * g + (lane & 7)) * 4, rr_ = lds0 + L_S + L_RR + c * 8;
    int hbo = OFF_HB + ((w & 1) * 4 + g) * 4096 + rb * 2048 + col0 * 4 + c * 8;
    GAS unsigned* op = (GAS unsigned*)(out + rb * D + col0 + 2 * c);
    PIN(rw_); PIN(rr_); PIN(hbo); PIN(op);
    {
      const int po = OFF_P1 + (16 * w + pp) * 4096 + rj * 128 + (lane & 7) * 16;
      v4u a0, a1;
      poll2<PD2, -1>(ws, po, po + 8 * 4096, tagh, a0, a1);
      STAMP(5);
      float u = sw16_sum(sw32_sum(UP24(a0[0]) + UP24(a1[0]), UP24(a0[2]) + UP24(a1[2])), sw32_sum(UP24(a0[1]) + UP24(a1[1]), UP24(a0[3]) + UP24(a1[3])));
      u += dpp<ROR8>(u);
      *(LAS float*)rw_ = u;
      barrier();
      const f32x2 s = (*(LAS f32x2*)rr_ + *(LAS f32x2*)(rr_ + 128)) + (*(LAS f32x2*)(rr_ + 256) + *(LAS f32x2*)(rr_ + 384));
      const unsigned at = cvtpk(s[0], s[1]);
      const unsigned hp = cvtpk(bflo(xp) + bflo(at), bfhi(xp) + bfhi(at));
      h0 = bflo(hp); h1 = bfhi(hp);
      if (w < 2) __builtin_amdgcn_raw_buffer_store_b64(v2u{hp, tag}, rw, hbo, 0, AUX_SC1);
      STAMP(6);
      poll_done(a0, a1);
    }
    {
      const int po = OFF_P2 + (14 * w + pp) * 4096 + rj * 128 + (lane & 7) * 16;
      v4u a0, b1;
      poll2<PD4, -1>(ws, po, po + (pp < 6 ? 8 * 4096 : 0), tagh, a0, b1);
      STAMP(7);
      const unsigned km = pp < 6 ? 0xffffff00u : 0u;
      float u = sw16_sum(sw32_sum(UP24(a0[0]) + u2f(b1[0] & km), UP24(a0[2]) + u2f(b1[2] & km)), sw32_sum(UP24(a0[1]) + u2f(b1[1] & km), UP24(a0[3]) + u2f(b1[3] & km)));
      u += dpp<ROR8>(u);
      *(LAS float*)(rw_ + 512) = u;
      barrier();
      if (tid < 16) {
        const f32x2 s = (*(LAS f32x2*)(rr_ + 512) + *(LAS f32x2*)(rr_ + 640)) + (*(LAS f32x2*)(rr_ + 768) + *(LAS f32x2*)(rr_ + 896));
        const unsigned dn = cvtpk(s[0], s[1]);
        *op = cvtpk(h0 + bflo(dn), h1 + bfhi(dn));
        if (rj == 0 && tid == 0) __builtin_amdgcn_raw_buffer_store_b32(tag, rw, OFF_EPOCH, 0, AUX_SC1);
      }
      STAMP(8);
      poll_done(a0, b1);
    }
  } else if (role == 1) {
    v4u fg[8], fu[8], wd[8];
    LAS unsigned char* fa = L + L_W + w * WREG + (8 * g + q4) * 64 + (p4 & 1) * 8;
    VMWAIT(16);
#pragma unroll
    for (int ks = 0; ks < 4; ++ks)
#pragma unroll
      for (int t = 0; t < 2; ++t) { LAS unsigned char* a = fa + 2048 * ks + (((2 * t + (p4 >> 1)) ^ ((g & 1) << 1)) * 16); fg[ks * 2 + t] = tr_frag(a, a + 256); }
    VMWAIT(8);
    STAMP(10);
#pragma unroll
    for (int ks = 0; ks < 4; ++ks)
#pragma unroll
      for (int t = 0; t < 2; ++t) { LAS unsigned char* a = fa + 8192 + 2048 * ks + (((2 * t + (p4 >> 1)) ^ ((g & 1) << 1)) * 16); fu[ks * 2 + t] = tr_frag(a, a + 256); }
    f32x4 ag[2], au[2];
    unsigned ro = lds0 + L_S + L_RED + ((c >> 2) & 1) * 1024 + ((c & 1) * 32 + (c & 2) * 8 + 4 * g) * 16 + w * 4;
    unsigned fo = lds0 + L_S + L_RED + ((c >> 3) * 32 + 4 * (c & 3) + g) * 16;
#define GU_MFMA(XB) do { _Pragma("unroll") for (int t = 0; t < 2; ++t) { ag[t] = f32x4{0.f, 0.f, 0.f, 0.f}; au[t] = f32x4{0.f, 0.f, 0.f, 0.f}; } \
      _Pragma("unroll") for (int ks = 0; ks < 4; ++ks) { \
        _Pragma("unroll") for (int t = 0; t < 2; ++t) { ag[t] = mfma32(fg[ks * 2 + t], XB, ag[t]); au[t] = mfma32(fu[ks * 2 + t], XB, au[t]); } } } while (0)
#define GU_STORE() do { const f32x4 sel = (c & 4) ? ((c & 2) ? au[1] : au[0]) : ((c & 2) ? ag[1] : ag[0]); \
      _Pragma("unroll") for (int i = 0; i < 4; ++i) *(LAS float*)(ro + i * 16) = sel[i]; } while (0)
#define SUM4(p_) ({ const f32x2 t_ = f32x2{p_[0], p_[1]} + f32x2{p_[2], p_[3]}; float r_ = t_[0] + t_[1]; PIN(r_); r_; })
    GU_MFMA((v4u{0x3f803f80u, 0x3f803f80u, 0x3f803f80u, 0x3f803f80u}));
    GU_STORE();
    barrier();
    float cg0, cg1, cu0, cu1;
    { const f32x4 p0 = *(LAS f32x4*)fo, p1 = *(LAS f32x4*)(fo + 256), p2 = *(LAS f32x4*)(fo + 1024), p3 = *(LAS f32x4*)(fo + 1280); cg0 = SUM4(p0); cg1 = SUM4(p1); cu0 = SUM4(p2); cu1 = SUM4(p3); }
    unsigned xw = lds0 + L_S + L_XN + (w & 1) * 1024 + lane * 16, xr = lds0 + L_S + L_XN + (c & 1) * 1024 + (128 * w + 8 * g) * 2, sw_ = lds0 + L_S + L_ST + w * 8, sr = lds0 + L_S + L_ST + (c >> 3) * 8;
    int so_ = OFF_P2 + ci * 4096 + tid * 16;
    f32x4 acc = {0.f, 0.f, 0.f, 0.f};
    unsigned wda = lds0 + L_W + w * WREG + 16384 + lane * 16;
    PIN(wda); PIN(ro); PIN(fo); PIN(xw); PIN(xr); PIN(sw_); PIN(sr); PIN(so_); PIN(dm0); PIN(dm1); PIN(dm2); PIN(dm3); PIN(tb0); PIN(tb1); PIN(tb2); PIN(tb3); PIN(acc);
    v4u ha = {0u, 0u, 0u, 0u}, hb = ha;
    if (w < 2) {
      const int ho = OFF_HB + xcd * 4096 + w * 2048 + lane * 32;
      poll2<PD3, 0>(ws, ho, ho + 16, tag, ha, hb);
      STAMP(5);
      *(LAS v4u*)xw = v4u{ha[0], ha[2], hb[0], hb[2]};
    } else VMWAIT(0);
    barrier();
    v4u xb[4];
#pragma unroll
    for (int ks = 0; ks < 4; ++ks) xb[ks] = *(LAS v4u*)(xr + 64 * ks);
#pragma unroll
    for (int i = 0; i < 8; ++i) wd[i] = *(LAS v4u*)(wda + i * 1024);
    __builtin_amdgcn_sched_barrier(0);
    const float pv = bflo(__builtin_amdgcn_readfirstlane(ha[0]));
    const float d0 = bflo(ha[0]) - pv, d1 = bfhi(ha[0]) - pv, d2 = bflo(ha[2]) - pv, d3 = bfhi(ha[2]) - pv, d4 = bflo(hb[0]) - pv, d5 = bfhi(hb[0]) - pv, d6 = bflo(hb[2]) - pv, d7 = bfhi(hb[2]) - pv;
    float s1 = ((d0 + d1) + (d2 + d3)) + ((d4 + d5) + (d6 + d7));
    float s2 = (fmaf(d1, d1, d0 * d0) + fmaf(d3, d3, d2 * d2)) + (fmaf(d5, d5, d4 * d4) + fmaf(d7, d7, d6 * d6));
    __builtin_amdgcn_sched_barrier(0);
    GU_MFMA(xb[ks]);
    {
      float t = sw32_sum(s1, s2);
      t = sw16_sum(t, t); t += dpp<ROR8>(t); t += dpp<ROR4>(t); t += dpp<XOR2>(t); t += dpp<XOR1>(t);
      const float md = u2f(__builtin_amdgcn_readlane(f2u(t), 0)) * (1.0f / D), q2 = u2f(__builtin_amdgcn_readlane(f2u(t), 32)) * (1.0f / D);
      const float var = vmax(fmaf(-md, md, q2), 0.f);
      const float rs = __builtin_amdgcn_rcpf(rbf(__builtin_amdgcn_sqrtf(rbf(rbf(var) + 1e-5f))));
      *(LAS f32x2*)sw_ = f32x2{rbf(pv + md), rs};
    }
    GU_STORE();
    barrier();
    const f32x2 st = *(LAS f32x2*)sr;
    const f32x4 p0 = *(LAS f32x4*)fo, p1 = *(LAS f32x4*)(fo + 256), p2 = *(LAS f32x4*)(fo + 1024), p3 = *(LAS f32x4*)(fo + 1280);
    __builtin_amdgcn_sched_barrier(0);
    const float G0 = SUM4(p0), G1 = SUM4(p1), U0 = SUM4(p2), U1 = SUM4(p3);
    const unsigned gp = cvtpk(fmaf(-st[0], cg0, G0) * st[1], fmaf(-st[0], cg1, G1) * st[1]);
    const unsigned up = cvtpk(fmaf(-st[0], cu0, U0) * st[1], fmaf(-st[0], cu1, U1) * st[1]);
    const float g0 = bflo(gp), g1 = bfhi(gp);
    const unsigned sg = cvtpk(g0 * __builtin_amdgcn_rcpf(1.0f + __builtin_amdgcn_exp2f(-g0 * LOG2E)), g1 * __builtin_amdgcn_rcpf(1.0f + __builtin_amdgcn_exp2f(-g1 * LOG2E)));
    const float av0 = bflo(sg) * bflo(up), av1 = bfhi(sg) * bfhi(up);
    diag_chain_quad(acc, wd, cvtpk(av0, av0), cvtpk(av1, av1), dm0, dm1, dm2, dm3);
    mfma_drain(acc);
    PUBLISH_ACC(acc, so_);
    STAMP(6);
    poll_done(ha, hb);
  }
  DUMP_STAMPS();
}

extern "C" void kernel_launch(void* const* d_in, const int* in_sizes, int n_in, void* d_out, int out_size, void* d_ws, size_t ws_size, hipStream_t stream) {
  fused_block<<<NWG, NTHR, 0, stream>>>((const u16*)d_in[0], (const u16*)d_in[1], (const u16*)d_in[2], (const u16*)d_in[3], (unsigned char*)d_ws,
                                        (const u16*)d_in[4], (const u16*)d_in[6], (const u16*)d_in[7], (const u16*)d_in[8], (u16*)d_out, make_plan() TUNE_ARG);
}
```

```cpp
#pragma clang diagnostic ignored "-Wint-to-pointer-cast"
#include <hip/hip_runtime.h>
#include <hip/hip_bf16.h>
#include <cstdint>

typedef unsigned short u16;
typedef unsigned v4u __attribute__((ext_vector_type(4)));
typedef unsigned v2u __attribute__((ext_vector_type(2)));
typedef float f32x4 __attribute__((ext_vector_type(4)));
typedef float f32x2 __attribute__((ext_vector_type(2)));
typedef short s16x4 __attribute__((ext_vector_type(4)));
typedef __bf16 bf16x8_t __attribute__((ext_vector_type(8)));
typedef __bf16 bf16x2_t __attribute__((ext_vector_type(2)));
#define LAS __attribute__((address_space(3)))
#define GAS __attribute__((address_space(1)))

constexpr int D = 512, HQ = 32, HKV = 8, SK = 32768, DH = 128, F = 1792;
constexpr int NWG = 256, NTHR = 320, NSPLIT = 16;
struct Plan { unsigned e[32]; };
#define TUNE_PARAM
#define TUNE_ARG
#define ABL 0
#define TL_C 130
#define TL_CS 129
#define TL_B 128
#define TL_Q 124
#define TL_QS 120
#define T_WC 0
#define T_WB 0
#define T_L 3500
#define T_PD 150
#define T_P 580
static Plan make_plan() {
  Plan p;
  for (int b = 0; b < 2; ++b) {
    const int nc = 4 - b;
    int n[16], role[16], ix[16], used = 0, nn = 0;
    for (int sp = 0; sp < 16; ++sp) {
      if (sp < nc) { role[sp] = 1; ix[sp] = 2 * sp + b; n[sp] = sp == 0 ? TL_CS : TL_C; }
      else if (sp >= 4 && sp < 8) { role[sp] = 2; ix[sp] = 2 * (sp - 4) + b; n[sp] = TL_B; }
      else if (sp >= 12) { role[sp] = 4; ix[sp] = 2 * (sp - 12) + b; n[sp] = ix[sp] == 3 ? TL_QS : TL_Q; }
      else { role[sp] = (sp == 8 || sp == 9) ? 3 : 0; ix[sp] = role[sp] ? 2 * (sp - 8) + b : 0; n[sp] = 0; ++nn; }
      used += n[sp];
    }
    int rest = 2048 - used, t0 = 0;
    for (int sp = 0; sp < 16; ++sp) {
      if (n[sp] == 0) { n[sp] = (rest + nn - 1) / nn; rest -= n[sp]; --nn; }
      p.e[2 * sp + b] = (unsigned)t0 | ((unsigned)n[sp] << 11) | ((unsigned)role[sp] << 19) | ((unsigned)ix[sp] << 22);
      t0 += n[sp];
    }
  }
  return p;
}
#define HMASK 0x0F0F
#define HEAVY(d, s) ((HMASK >> (4 * ((d) & 3) + (s))) & 1)
#define NINST(d) (4 + __builtin_popcount((HMASK >> (4 * ((d) & 3))) & 15))
#define NPRE 13
#define TOUCH_EARLY 1
#define NLR 2
#define PD1 2
#define PD2 3
#define PD3 1
#define PD4 3
#define PSL 1
#define PACE_N 4
#define CODE_BYTES 31744
constexpr int NB = 64, NC = 56, NR = 64, NREP = 8;
constexpr int OFF_EPOCH = 0;
constexpr int OFF_HB = 0x190000;
constexpr int OFF_PART = 0x10000;
constexpr int OFF_P1 = 0x90000;
constexpr int OFF_P2 = 0xD0000;
constexpr int OFF_Q = 0x180000;
constexpr int WS_BYTES = 0x1A0000, OFF_DBG = 0x200000;
constexpr int AUX_NT = 2, AUX_SC1 = 16;
constexpr unsigned SPIN_CAP = 1u << 16;
constexpr int L_W = 0;
constexpr int WREG = 24576;
constexpr int L_QF = 4 * WREG;
constexpr int L_SC = L_QF + 1024;
constexpr int L_TAG = L_SC + 4 * 40 * 128;
constexpr int L_S = L_TAG + 16;
constexpr int L_XN = 0;
constexpr int L_RED = L_XN + 2048;
constexpr int L_MRG = L_RED + 2048;
constexpr int L_OB = L_MRG + 4 * 4 * 132 * 4;
constexpr int L_RR = L_OB + 512;
constexpr int L_ST = L_RR + 1024;
constexpr int S_BYTES = L_ST + 32;
constexpr int L_PF = L_S + S_BYTES;
constexpr int L_DBG = L_PF + 256;
constexpr int L_BYTES = L_DBG + 128;

__device__ __forceinline__ float bflo(unsigned v) { return __uint_as_float(v << 16); }
__device__ __forceinline__ float bfhi(unsigned v) { return __uint_as_float(v & 0xffff0000u); }
__device__ __forceinline__ unsigned cvtpk(float lo, float hi) { return __builtin_bit_cast(unsigned, __builtin_convertvector(f32x2{lo, hi}, bf16x2_t)); }
__device__ __forceinline__ float rbf(float x) { return bflo(cvtpk(x, x)); }
__device__ __forceinline__ bool tagged(v4u v, unsigned tag) { return (v[1] == tag) & (v[3] == tag); }
__device__ __forceinline__ float u2f(unsigned v) { return __uint_as_float(v); }
__device__ __forceinline__ unsigned f2u(float v) { return __float_as_uint(v); }

__device__ __forceinline__ float vmax(float a, float b) { return __builtin_elementwise_maximum(a, b); }
template <int CTRL> __device__ __forceinline__ float dpp(float x) { return __builtin_bit_cast(float, __builtin_amdgcn_mov_dpp(__builtin_bit_cast(int, x), CTRL, 0xf, 0xf, true)); }
template <int CTRL> __device__ __forceinline__ unsigned dppu(unsigned x) { return (unsigned)__builtin_amdgcn_mov_dpp((int)x, CTRL, 0xf, 0xf, true); }
constexpr int XOR1 = 0xB1, XOR2 = 0x4E, ROR8 = 0x128, ROR4 = 0x124, ROR12 = 0x12C;
__device__ __forceinline__ float sw32_sum(float a, float b) { auto r = __builtin_amdgcn_permlane32_swap(f2u(a), f2u(b), false, false); return u2f(r[0]) + u2f(r[1]); }
__device__ __forceinline__ float sw16_sum(float a, float b) { auto r = __builtin_amdgcn_permlane16_swap(f2u(a), f2u(b), false, false); return u2f(r[0]) + u2f(r[1]); }
__device__ __forceinline__ float sw32_max(float a, float b) { auto r = __builtin_amdgcn_permlane32_swap(f2u(a), f2u(b), false, false); return vmax(u2f(r[0]), u2f(r[1])); }
__device__ __forceinline__ float sw16_max(float a, float b) { auto r = __builtin_amdgcn_permlane16_swap(f2u(a), f2u(b), false, false); return vmax(u2f(r[0]), u2f(r[1])); }
__device__ __forceinline__ float wave_sum(float x) {
  x += dpp<XOR1>(x); x += dpp<XOR2>(x); x += dpp<ROR4>(x); x += dpp<ROR8>(x);
  x = sw16_sum(x, x); return sw32_sum(x, x);
}
__device__ __forceinline__ f32x4 mfma32(v4u a, v4u b, f32x4 c) { return __builtin_amdgcn_mfma_f32_16x16x32_bf16(__builtin_bit_cast(bf16x8_t, a), __builtin_bit_cast(bf16x8_t, b), c, 0, 0, 0); }
__device__ __forceinline__ f32x4 mfma16(v2u a, v2u b, f32x4 c) { return __builtin_amdgcn_mfma_f32_16x16x16bf16_1k(__builtin_bit_cast(s16x4, a), __builtin_bit_cast(s16x4, b), c, 0, 0, 0); }
__device__ __forceinline__ void barrier() { asm volatile("s_waitcnt lgkmcnt(0)\n\ts_barrier" ::: "memory"); }
__device__ __forceinline__ v4u tr_frag(LAS unsigned char* lo, LAS unsigned char* hi) {
  const v2u a = __builtin_bit_cast(v2u, __builtin_amdgcn_ds_read_tr16_b64_v4i16((LAS s16x4*)lo));
  const v2u b = __builtin_bit_cast(v2u, __builtin_amdgcn_ds_read_tr16_b64_v4i16((LAS s16x4*)hi));
  return v4u{a[0], a[1], b[0], b[1]};
}

#define AND4(T0, T1, T2, T3, SRC, MOD, SFX) "v_and_b32" SFX " " T0 ", " SRC ", %[m0]" MOD "\n\tv_and_b32" SFX " " T1 ", " SRC ", %[m1]" MOD "\n\tv_and_b32" SFX " " T2 ", " SRC ", %[m2]" MOD "\n\tv_and_b32" SFX " " T3 ", " SRC ", %[m3]" MOD "\n\t"
#define ANDA(SRC, MOD, SFX) AND4("v240", "v241", "v242", "v243", SRC, MOD, SFX)
#define ANDB(SRC, MOD, SFX) AND4("v244", "v245", "v246", "v247", SRC, MOD, SFX)
#define MFA(W) "v_mfma_f32_16x16x32_bf16 %[acc], " W ", v[240:243], %[acc]\n\t"
#define MFB(W) "v_mfma_f32_16x16x32_bf16 %[acc], " W ", v[244:247], %[acc]\n\t"
#define CHAIN_CLOB "v240", "v241", "v242", "v243", "v244", "v245", "v246", "v247"
#define CHAIN_W(W) [w0] "v"(W[0]), [w1] "v"(W[1]), [w2] "v"(W[2]), [w3] "v"(W[3]), [w4] "v"(W[4]), [w5] "v"(W[5]), [w6] "v"(W[6]), [w7] "v"(W[7])
__device__ __forceinline__ void diag_chain_regs(f32x4& acc, const v4u* W, v4u pa, v4u pb, unsigned m0, unsigned m1, unsigned m2, unsigned m3) {
  asm volatile(ANDA("%[p0]", "", "") ANDB("%[p1]", "", "") MFA("%[w0]") ANDA("%[p2]", "", "") MFB("%[w1]") ANDB("%[p3]", "", "") MFA("%[w2]") ANDA("%[p4]", "", "") MFB("%[w3]")
               ANDB("%[p5]", "", "") MFA("%[w4]") ANDA("%[p6]", "", "") MFB("%[w5]") ANDB("%[p7]", "", "") MFA("%[w6]") "s_nop 0\n\t" MFB("%[w7]")
               : [acc] "+v"(acc)
               : CHAIN_W(W), [p0] "v"(pa[0]), [p1] "v"(pa[1]), [p2] "v"(pa[2]), [p3] "v"(pa[3]), [p4] "v"(pb[0]), [p5] "v"(pb[1]), [p6] "v"(pb[2]), [p7] "v"(pb[3]),
                 [m0] "v"(m0), [m1] "v"(m1), [m2] "v"(m2), [m3] "v"(m3)
               : CHAIN_CLOB);
}
#define QP(t) " quad_perm:[" #t "," #t "," #t "," #t "] row_mask:0xf bank_mask:0xf"
__device__ __forceinline__ void diag_chain_quad(f32x4& acc, const v4u* W, unsigned pa, unsigned pb, unsigned m0, unsigned m1, unsigned m2, unsigned m3) {
  asm volatile("s_nop 1\n\t" ANDA("%[pa]", QP(0), "_dpp") ANDB("%[pa]", QP(1), "_dpp") MFA("%[w0]") ANDA("%[pa]", QP(2), "_dpp") MFB("%[w1]") ANDB("%[pa]", QP(3), "_dpp") MFA("%[w2]")
               ANDA("%[pb]", QP(0), "_dpp") MFB("%[w3]") ANDB("%[pb]", QP(1), "_dpp") MFA("%[w4]") ANDA("%[pb]", QP(2), "_dpp") MFB("%[w5]") ANDB("%[pb]", QP(3), "_dpp") MFA("%[w6]")
               "s_nop 0\n\t" MFB("%[w7]")
               : [acc] "+v"(acc)
               : CHAIN_W(W), [pa] "v"(pa), [pb] "v"(pb), [m0] "v"(m0), [m1] "v"(m1), [m2] "v"(m2), [m3] "v"(m3)
               : CHAIN_CLOB);
}
__device__ __forceinline__ void mfma_drain(f32x4& acc) { asm volatile("s_nop 7" : "+v"(acc)); }

__device__ __forceinline__ v4u ln_row(v4u xv) {
  float x[8];
#pragma unroll
  for (int i = 0; i < 4; ++i) { x[2 * i] = bflo(xv[i]); x[2 * i + 1] = bfhi(xv[i]); }
  const float mean = wave_sum(((x[0] + x[1]) + (x[2] + x[3])) + ((x[4] + x[5]) + (x[6] + x[7]))) * (1.0f / D);
  float vs = 0.f;
#pragma unroll
  for (int i = 0; i < 8; ++i) { const float d = x[i] - mean; vs = fmaf(d, d, vs); }
  const float var = wave_sum(vs) * (1.0f / D);
  const float mb = rbf(mean);
  const float rs = __builtin_amdgcn_rcpf(rbf(__builtin_amdgcn_sqrtf(rbf(rbf(var) + 1e-5f))));
  v4u r;
#pragma unroll
  for (int i = 0; i < 4; ++i) { const unsigned d = cvtpk(x[2 * i] - mb, x[2 * i + 1] - mb); r[i] = cvtpk(bflo(d) * rs, bfhi(d) * rs); }
  return r;
}
__device__ __forceinline__ void glds16(const u16* gsrc, unsigned lds_dst) {
  unsigned keep;
  asm volatile("s_mov_b32 %0, m0\n\ts_mov_b32 m0, %2\n\ts_nop 0\n\tglobal_load_lds_dwordx4 %1, off nt\n\ts_mov_b32 m0, %0" : "=&s"(keep) : "v"(gsrc), "s"(lds_dst) : "memory");
}

__device__ __forceinline__ unsigned mixtag(unsigned t) { t *= 0x9E3779B1u; return t ^ (t >> 15); }
#define PK24(x, tb) (((f2u(x) + 0x80u) & 0xffffff00u) | (tb))
#define UP24(x) u2f((x) & 0xffffff00u)
__device__ __forceinline__ unsigned lowbytes(v4u v) { return __builtin_amdgcn_perm(v[1], v[0], 0x0c0c0400u) | __builtin_amdgcn_perm(v[3], v[2], 0x04000c0cu); }
template <int PD_, int TSH>
__device__ __forceinline__ void poll2(const unsigned char* ws, int o0, int o1, unsigned tag, v4u& r0, v4u& r1) {
  r0 = v4u{~tag, ~tag, ~tag, ~tag}; r1 = r0;
#define PASS() asm volatile("global_load_dwordx4 %0, %2, %4 sc1\n\tglobal_load_dwordx4 %1, %3, %4 sc1" : "+v"(r0), "+v"(r1) : "v"(o0), "v"(o1), "s"(ws) : "memory")
#pragma unroll
  for (int k = 0; k < PD_ - 1; ++k) { PASS(); __builtin_amdgcn_s_sleep(PSL); }
#pragma unroll 1
  for (unsigned k = 0; k < SPIN_CAP; ++k) {
    PASS();
    asm volatile("s_waitcnt vmcnt(%2)" : "+v"(r0), "+v"(r1) : "i"((PD_ - 1) * 2) : "memory");
    if (TSH == 16) { if (__all((__builtin_amdgcn_perm(r0[3], r0[1], 0x07060302u) == tag) & (__builtin_amdgcn_perm(r1[3], r1[1], 0x07060302u) == tag))) break; }
    else if (TSH < 0) { if (__all((lowbytes(r0) == tag) & (lowbytes(r1) == tag))) break; }
    else if (__all((((r0[1] ^ tag) | (r0[3] ^ tag) | (r1[1] ^ tag) | (r1[3] ^ tag)) >> (TSH < 0 ? 0 : TSH)) == 0u)) break;
    if (PD_ > 1) __builtin_amdgcn_s_sleep(PSL);
  }
#undef PASS
  asm volatile("s_nop 3 ; POLL_END" : "+v"(r0), "+v"(r1));
}
__device__ __forceinline__ void poll_done(v4u& r0, v4u& r1) { asm volatile("s_waitcnt vmcnt(0)" : "+v"(r0), "+v"(r1) :: "memory"); }

#define STAMP(i) do { } while (0)
#define DUMP_STAMPS() do { } while (0)
#define LD128(off) __builtin_amdgcn_raw_buffer_load_b128(rw, (off), 0, AUX_SC1)
#define ST128(v, off) __builtin_amdgcn_raw_buffer_store_b128((v), rw, (off), 0, AUX_SC1)
#define SPIN_LOOP _Pragma("unroll 1") for (unsigned spins = 0; spins < SPIN_CAP; ++spins)
#define NOHOIST() asm volatile("" ::: "memory")

__global__ void __launch_bounds__(NTHR, 2) fused_block(const u16* x_, const u16* kc_, const u16* vc_, const u16* wq_, unsigned char* ws_,
                                                       const u16* wo_, const u16* wgate_, const u16* wup_, const u16* wdown_, u16* out_, Plan plan_ TUNE_PARAM) {
  typedef unsigned long long u64x8 __attribute__((ext_vector_type(8)));
  typedef unsigned long long u64x2 __attribute__((ext_vector_type(2)));
  u64x8 ka; u64x2 kb2; unsigned pe;
  asm volatile("s_load_dwordx16 %0, %3, 0x0\n\ts_load_dwordx4 %1, %3, 0x40\n\ts_load_dword %2, %3, %4 offset:0x50"
               : "=&s"(ka), "=&s"(kb2), "=&s"(pe) : "s"(__builtin_amdgcn_kernarg_segment_ptr()), "s"((blockIdx.x >> 3) * 4));
  __shared__ __attribute__((aligned(16))) unsigned char smem[L_BYTES];
  LAS unsigned char* L = (LAS unsigned char*)smem;
  STAMP(0);
  const unsigned tstart = (unsigned)__builtin_amdgcn_s_memrealtime();
  const int wg = blockIdx.x, xcd = wg & 7, idx = wg >> 3;
  const int kvh = xcd, b = idx & 1, sp = idx >> 1;
  int lane = threadIdx.x & 63;
  const int w = __builtin_amdgcn_readfirstlane(threadIdx.x >> 6);
  int c = lane & 15, g = lane >> 4;
  if (w == 4 && !(ABL & 32)) {
    unsigned long long pc;
    asm volatile("s_getpc_b64 %0" : "=s"(pc));
    if ((lane * 32 + idx) * 128 < CODE_BYTES) {
      unsigned keep;
      asm volatile("s_mov_b32 %0, m0\n\ts_mov_b32 m0, %3\n\ts_nop 0\n\tglobal_load_lds_dword %1, %2\n\ts_mov_b32 m0, %0"
                   : "=&s"(keep) : "v"((lane * 32 + idx) * 128), "s"(pc & ~255ull), "s"((unsigned)(uintptr_t)L + L_PF) : "memory");
    }
  }
  int koff = (c >> 2) * 1024 + g * 64 + (c & 3) * 16;
  unsigned pbase = (unsigned)(b * HKV + kvh) * (SK * DH * 2);
  asm volatile("s_waitcnt lgkmcnt(0)" : "+s"(ka), "+s"(kb2), "+s"(pe), "+v"(koff), "+s"(pbase));
  const u16 *x = (const u16*)ka[0], *wq = (const u16*)ka[3], *wo = (const u16*)ka[5], *wgate = (const u16*)ka[6], *wup = (const u16*)ka[7], *wdown = (const u16*)kb2[0];
  unsigned char* ws = (unsigned char*)ka[4];
  u16* out = (u16*)kb2[1];
  const int tile0 = pe & 2047, ntile = (pe >> 11) & 255;
  const __amdgpu_buffer_rsrc_t rk = __builtin_amdgcn_make_buffer_rsrc((void*)(ka[1] + pbase), 0, SK * DH * 2, 0x00020000);
  const unsigned sbase = (unsigned)(tile0 + w) * 4096u;
  v4u kb[NPRE + 3][4];
#define SB() __builtin_amdgcn_sched_barrier(0)
#define ISSUEK(d, j) do { _Pragma("unroll") for (int s = 0; s < 4; ++s) { \
      if (!HEAVY(d, s)) kb[d][s] = __builtin_amdgcn_raw_buffer_load_b128(rk, koff + s * 256, sbase + (unsigned)(j) * 16384u, AUX_NT); \
      else { const v2u lo_ = __builtin_amdgcn_raw_buffer_load_b64(rk, koff + s * 256, sbase + (unsigned)(j) * 16384u, AUX_NT); NOHOIST(); SB(); \
             const v2u hi_ = __builtin_amdgcn_raw_buffer_load_b64(rk, koff + s * 256 + 8, sbase + (unsigned)(j) * 16384u, AUX_NT); NOHOIST(); \
             kb[d][s] = v4u{lo_[0], lo_[1], hi_[0], hi_[1]}; } } SB(); } while (0)
  const int role = (pe >> 19) & 7, rix = pe >> 22;
  const bool qrole = role == 4;
#define CI() ({ int r_ = rix; asm volatile("" : "+s"(r_)); const int cn_ = xcd * 5 + r_ - (xcd < 7 ? 2 : 0); (xcd < 7 && r_ < 2) ? 8 * xcd + 6 + r_ : cn_ + 2 * ((cn_ * 43) >> 8); })
  const int bi = xcd * 8 + rix, rj = xcd * 4 + rix, qi = bi;
  const __amdgpu_buffer_rsrc_t rw = __builtin_amdgcn_make_buffer_rsrc((void*)ws, 0, WS_BYTES, 0x00020000);
  const unsigned lds0 = (unsigned)(uintptr_t)L;
  LAS unsigned char* S = L + L_S;
#define WAIT_UNTIL(t, slp) do { while ((int)((unsigned)__builtin_amdgcn_s_memrealtime() - (tstart + (unsigned)(t))) < 0) __builtin_amdgcn_s_sleep(slp); } while (0)
  int q4 = (lane >> 2) & 3, p4 = lane & 3;
  constexpr float SCALE = 0.088388347648318440f, LOG2E = 1.4426950408889634f;
  unsigned tag;

  if (w == 4) {
    __builtin_amdgcn_s_setprio(3);
#define TOUCH() \
    {     \
      const int ci = CI(); \
      const unsigned long long vb_ = ka[2] + pbase + (unsigned)tile0 * 4096u, ke_ = ka[1] + pbase + (unsigned)(tile0 + ntile) * 4096u - 128u; \
      const unsigned long long wa_ = role == 1 ? ka[6] + ci * 64 : role == 2 ? ka[5] + bi * 65536 : kb2[1]; \
      const unsigned long long ad = lane == 0 ? vb_ : lane == 1 ? vb_ + (unsigned)ntile * 4096u - 128u : lane == 2 ? ke_ : lane == 3 ? wa_ \
                                  : lane == 4 ? wa_ + 511 * F * 2 : lane == 5 ? ka[7] + ci * 64 : lane == 6 ? ka[7] + ci * 64 + 511 * F * 2 : kb2[0] + ci * 32768; \
      if (lane < (role == 1 ? 8 : 4)) { \
        unsigned keep; \
        asm volatile("s_mov_b32 %0, m0\n\ts_mov_b32 m0, %2\n\ts_nop 0\n\tglobal_load_lds_dword %1, off nt\n\ts_mov_b32 m0, %0" : "=&s"(keep) : "v"(ad), "s"(lds0 + L_PF) : "memory"); \
      } \
    }
    if (qrole) {
      const v4u x0 = *(const GAS v4u*)(x + lane * 8), x1 = *(const GAS v4u*)(x + D + lane * 8);
      tag = __builtin_amdgcn_raw_buffer_load_b32(rw, OFF_EPOCH, 0, AUX_SC1) + 1u;
      const v4u n0 = ln_row(x0), n1 = ln_row(x1);
      *(LAS v4u*)(S + L_XN + lane * 16) = n0; *(LAS v4u*)(S + L_XN + 1024 + lane * 16) = n1;
      STAMP(9);
      barrier();
      if (TOUCH_EARLY) TOUCH();
      barrier();
      const f32x2 s = (*(LAS f32x2*)(S + L_RED + lane * 8) + *(LAS f32x2*)(S + L_RED + 512 + lane * 8)) + (*(LAS f32x2*)(S + L_RED + 1024 + lane * 8) + *(LAS f32x2*)(S + L_RED + 1536 + lane * 8));
      __builtin_amdgcn_raw_buffer_store_b64(v2u{cvtpk(s[0], s[1]), tag}, rw, OFF_Q + ((lane >> 5) * 2048 + qi * 32 + (lane & 31)) * 8, 0, AUX_SC1);
    } else {
      if (TOUCH_EARLY) TOUCH();
      tag = __builtin_amdgcn_raw_buffer_load_b32(rw, OFF_EPOCH, 0, AUX_SC1) + 1u;
    }
    STAMP(1);
    if (lane == 0) *(LAS v2u*)(L + L_TAG) = v2u{tag, qrole ? (unsigned)__builtin_amdgcn_s_memrealtime() : tstart};
    {
      const int qo = OFF_Q + (b * 2048 + kvh * 256) * 8 + lane * 16;
      v4u q0, q1;
      bool ok0 = false, ok1 = false;
      WAIT_UNTIL(T_P, 2);
      SPIN_LOOP {
        NOHOIST();
        if (!ok0) q0 = LD128(qo);
        if (!ok1) q1 = LD128(qo + 1024);
        ok0 = tagged(q0, tag); ok1 = tagged(q1, tag);
        if (__all(ok0 & ok1)) break;
      }
      *(LAS v2u*)(L + L_QF + lane * 8) = v2u{q0[0], q0[2]};
      *(LAS v2u*)(L + L_QF + 512 + lane * 8) = v2u{q1[0], q1[2]};
      STAMP(2);
      barrier();
    }
    if (!TOUCH_EARLY) TOUCH();
    asm volatile("s_waitcnt vmcnt(0)" ::: "memory");
    return;
  }
  const int nstep = (ntile - w + 3) >> 2;
  const __amdgpu_buffer_rsrc_t rv = __builtin_amdgcn_make_buffer_rsrc((void*)(ka[2] + pbase), 0, SK * DH * 2, 0x00020000);
#define VMWAIT(n) do { asm volatile("s_waitcnt vmcnt(%0)" :: "i"(n) : "memory"); SB(); } while (0)
#define RELANE() do { asm volatile("v_mbcnt_lo_u32_b32 %0, -1, 0\n\tv_mbcnt_hi_u32_b32 %0, -1, %0" : "=v"(lane)); c = lane & 15; g = lane >> 4; } while (0)
  int wp = 0;
#define WUPTO(n, pace) do { _Pragma("unroll 1") while (wp < (n)) { \
      const u16* src_ = role == 2 ? wo + (size_t)(bi * 64 + 4 * wp + g) * D + 128 * w + 8 * c \
                      : wp < 16 ? (wp < 8 ? wgate : wup) + (size_t)(128 * w + 16 * (wp & 7) + (lane >> 2)) * F + ci * 32 + (((lane & 3) ^ ((lane >> 4) & 2)) * 8) \
                                : wdown + (size_t)(ci * 32 + 4 * (wp & 7) + g) * D + 128 * w + 8 * c; \
      VMWAIT(pace); glds16(src_, lds0 + L_W + w * WREG + wp * 1024); ++wp; } } while (0)
  if (qrole) {
    const int r8 = lane >> 3;
    const u16* p = wq + (size_t)(128 * w + r8) * (HQ * DH) + qi * 64;
    const int ch0 = ((lane & 7) ^ (r8 & 2)) * 8;
#pragma unroll 1
    for (int t = 0; t < 16; t += 2) {
      glds16(p + ch0, lds0 + L_W + w * WREG + t * 1024);
      glds16(p + 8 * (HQ * DH) + (ch0 ^ 32), lds0 + L_W + w * WREG + t * 1024 + 1024);
      p += 16 * (HQ * DH);
    }
    STAMP(7);
  }
  STAMP(11);
  if (!qrole) { ISSUEK(0, 0); ISSUEK(1, 1); }
  else {
    const int sw = (q4 & 2) | ((g & 1) << 2);
    v4u fr[16];
#define QFRAGS(ks) do { _Pragma("unroll") for (int t = 0; t < 4; ++t) { \
        LAS unsigned char* a = L + L_W + w * WREG + (ks) * 4096 + g * 1024 + q4 * 128 + (p4 & 1) * 8 + (((2 * t + (p4 >> 1)) ^ sw) * 16); fr[(ks) * 4 + t] = tr_frag(a, a + 512); } } while (0)
    VMWAIT(8); QFRAGS(0); QFRAGS(1); ISSUEK(0, 0);
    VMWAIT(4 + NINST(0)); QFRAGS(2); ISSUEK(1, 1);
    barrier();
    f32x4 qa[4];
    v4u xb[4];
#pragma unroll
    for (int ks = 0; ks < 4; ++ks) xb[ks] = *(LAS v4u*)(S + L_XN + (c & 1) * 1024 + (128 * w + 32 * ks + 8 * g) * 2);
#pragma unroll
    for (int t = 0; t < 4; ++t) qa[t] = f32x4{0.f, 0.f, 0.f, 0.f};
#pragma unroll
    for (int ks = 0; ks < 3; ++ks)
#pragma unroll
      for (int t = 0; t < 4; ++t) qa[t] = mfma32(fr[ks * 4 + t], xb[ks], qa[t]);
    VMWAIT(NINST(0) + NINST(1)); STAMP(8); QFRAGS(3);
#pragma unroll
    for (int t = 0; t < 4; ++t) qa[t] = mfma32(fr[12 + t], xb[3], qa[t]);
    if (c < 2) {
#pragma unroll
      for (int t = 0; t < 4; ++t) *(LAS f32x4*)(S + L_RED + w * 512 + c * 256 + t * 64 + g * 16) = qa[t];
    }
    STAMP(1);
    barrier();
  }
  const int rem = nstep & 3, remk = (nstep - NPRE) & 3;
#pragma unroll
  for (int d = 2; d < NPRE; ++d) { VMWAIT(NINST(d - 1)); ISSUEK(d, d); }
  STAMP(10);
  barrier();
  STAMP(2);
  const v4u qf = *(LAS v4u*)(L + L_QF + (c >> 2) * 256 + g * 64 + (c & 3) * 16);
  tag = __builtin_amdgcn_readfirstlane(*(LAS unsigned*)(L + L_TAG));

  LAS unsigned char* scp = L + L_SC + w * 5120 + g * 32 + (c >> 2) * 8;
  float m = -INFINITY;
  f32x4 sc;
#define QK(d) do { \
    _Pragma("unroll") for (int s = 0; s < 4; ++s) { const f32x4 dd = mfma32(kb[d][s], qf, f32x4{0.f, 0.f, 0.f, 0.f}); sc[s] = (c & 2) ? ((c & 1) ? dd[3] : dd[2]) : ((c & 1) ? dd[1] : dd[0]); } \
    asm volatile("" : "+v"(sc) :: "memory"); SB(); } while (0)
#define SCORE(d, j) do { \
    _Pragma("unroll") for (int s = 0; s < 4; ++s) { float t_ = sc[s]; t_ += dpp<XOR1>(t_); t_ += dpp<XOR2>(t_); sc[s] = t_; } \
    const unsigned r01 = cvtpk(sc[0], sc[1]), r23 = cvtpk(sc[2], sc[3]); \
    const unsigned t01 = cvtpk(bflo(r01) * SCALE, bfhi(r01) * SCALE), t23 = cvtpk(bflo(r23) * SCALE, bfhi(r23) * SCALE); \
    m = vmax(vmax(m, vmax(bflo(t01), bfhi(t01))), vmax(bflo(t23), bfhi(t23))); \
    *(LAS v2u*)(scp + (j) * 128) = v2u{t01, t23}; asm volatile("" : "+v"(m)); SB(); } while (0)
#pragma unroll
  for (int d = 0; d < 4; ++d) { QK(d); ISSUEK(d, NPRE + remk + d); SCORE(d, d); }
#pragma unroll
  for (int d = 4; d < NPRE; ++d) { QK(d); SCORE(d, d); }
#pragma unroll
  for (int d = NPRE; d < NPRE + 3; ++d) if (d - NPRE < remk) ISSUEK(d, d);
  int j = NPRE + remk;
#pragma unroll 1
  do {
#pragma unroll
    for (int d = 0; d < 4; ++d) { QK(d); ISSUEK(d, j + 4 + d); SCORE(d, j + d); }
    j += 4;
  } while (j + 4 < nstep);
#pragma unroll
  for (int d = NPRE; d < NPRE + 3; ++d) if (d - NPRE < remk) { QK(d); SCORE(d, d); }
  const int voff = g * 1024 + c * 16;
  v2u vl[7][4], vh[7][4];
#define ISSUEV(d, j) do { _Pragma("unroll") for (int s = 0; s < 4; ++s) { \
      if (!HEAVY(d, s)) { const v4u t_ = __builtin_amdgcn_raw_buffer_load_b128(rv, voff + s * 256, sbase + (unsigned)(j) * 16384u, AUX_NT); vl[d][s] = v2u{t_[0], t_[1]}; vh[d][s] = v2u{t_[2], t_[3]}; } \
      else { vl[d][s] = __builtin_amdgcn_raw_buffer_load_b64(rv, voff + s * 256, sbase + (unsigned)(j) * 16384u, AUX_NT); NOHOIST(); SB(); \
             vh[d][s] = __builtin_amdgcn_raw_buffer_load_b64(rv, voff + s * 256 + 8, sbase + (unsigned)(j) * 16384u, AUX_NT); NOHOIST(); } } SB(); } while (0)
#pragma unroll
  for (int d = 0; d < 4; ++d) { QK(d); ISSUEV(d, d); SCORE(d, j + d); }
#pragma unroll
  for (int d = 4; d < 7; ++d) if (d - 4 < rem) ISSUEV(d, d);
  STAMP(9);
  m = sw16_max(m, m); m = sw32_max(m, m);
  const float mlog = m * LOG2E;
  const unsigned m0 = (c & 3) == 0 ? 0xffffu : (c & 3) == 1 ? 0xffff0000u : 0u;
  const unsigned m1 = (c & 3) == 2 ? 0xffffu : (c & 3) == 3 ? 0xffff0000u : 0u;
  f32x4 acc0 = {0.f, 0.f, 0.f, 0.f}, acc1 = {0.f, 0.f, 0.f, 0.f};
  float l = 0.f;
#define PCALC(bb, j) do { \
    const v2u t_ = *(LAS v2u*)(scp + (j) * 128); \
    const float sv[4] = {bflo(t_[0]), bfhi(t_[0]), bflo(t_[1]), bfhi(t_[1])}; \
    _Pragma("unroll") for (int i = 0; i < 4; ++i) { const float p_ = __builtin_amdgcn_exp2f(fmaf(sv[i], LOG2E, -mlog)); const unsigned pp = cvtpk(p_, p_); l += bflo(pp); bb[i] = v2u{pp & m0, pp & m1}; } } while (0)
#define PMFMA(d, bb) do { SB(); \
    _Pragma("unroll") for (int i = 0; i < 4; ++i) { acc0 = mfma16(vl[d][i], bb[i], acc0); acc1 = mfma16(vh[d][i], bb[i], acc1); } \
    asm volatile("" : "+v"(acc0), "+v"(acc1) :: "memory"); SB(); } while (0)
#define PV(d, j) do { v2u bb_[4]; PCALC(bb_, j); PMFMA(d, bb_); } while (0)
#pragma unroll
  for (int d = 0; d < 4; ++d) { PV(d, d); ISSUEV(d, 4 + rem + d); }
#pragma unroll
  for (int d = 4; d < 7; ++d) if (d - 4 < rem) PV(d, d);
  j = 4 + rem;
#pragma unroll 1
  do {
#pragma unroll
    for (int d = 0; d < 4; ++d) { PV(d, j + d); ISSUEV(d, j + 4 + d); }
    j += 4;
  } while (j + 4 + 4 * NLR < nstep);
  unsigned bal[2] = {0u, 0u};
  const int blo = (lane & 31) * 128;
#pragma unroll
  for (int r = 0; r < NLR; ++r) {
#pragma unroll
    for (int d = 0; d < 4; ++d) {
      if (r > 0 && !(d & 1)) asm volatile("" :: "v"(bal[d >> 1]));
      PV(d, j + d);
#pragma unroll
      for (int s = 0; s < 4; ++s) { const v4u t_ = __builtin_amdgcn_raw_buffer_load_b128(rv, voff + s * 256, sbase + (unsigned)(j + 4 + d) * 16384u, AUX_NT); vl[d][s] = v2u{t_[0], t_[1]}; vh[d][s] = v2u{t_[2], t_[3]}; }
      SB();
      if (!(d & 1)) {
        const bool late = (int)((unsigned)__builtin_amdgcn_s_memrealtime() - (tstart + T_L)) > 0;
        bal[d >> 1] = __builtin_amdgcn_raw_buffer_load_b32(rv, late ? 0 : blo, sbase + (unsigned)(j + 4 + d) * 16384u, AUX_NT); SB();
      }
    }
    j += 4;
  }
  asm volatile("v_mbcnt_lo_u32_b32 %0, -1, 0\n\tv_mbcnt_hi_u32_b32 %0, -1, %0" : "=v"(lane));
  c = lane & 15; g = lane >> 4; q4 = (lane >> 2) & 3; p4 = lane & 3;
  const int tid = w * 64 + lane;
#define PIN(x) asm volatile("" : "+v"(x))
  const unsigned tagh = mixtag(tag);
  v2u lb[4][4];
#pragma unroll
  for (int d = 0; d < 4; ++d) PCALC(lb[d], j + d);
  l = sw16_sum(l, l); l = sw32_sum(l, l);
  *(LAS f32x2*)(S + L_RED + (c >> 2) * 32 + w * 8) = f32x2{mlog, l};
  barrier();
  const int hd = 2 * (w & 1) + (lane >> 5);
  float fw; unsigned t1w, t3w;
  {
#define MERGE_ML(h, M_, L_) do { const f32x4 u0_ = *(LAS f32x4*)(S + L_RED + (h) * 32), u1_ = *(LAS f32x4*)(S + L_RED + (h) * 32 + 16); \
      M_ = vmax(vmax(u0_[0], u0_[2]), vmax(u1_[0], u1_[2])); \
      L_ = fmaf(__builtin_amdgcn_exp2f(u0_[2] - M_), u0_[3], __builtin_amdgcn_exp2f(u0_[0] - M_) * u0_[1]) + fmaf(__builtin_amdgcn_exp2f(u1_[2] - M_), u1_[3], __builtin_amdgcn_exp2f(u1_[0] - M_) * u1_[1]); } while (0)
    float M1, L1, M2, L2;
    MERGE_ML(c >> 2, M1, L1); MERGE_ML(hd, M2, L2);
    fw = __builtin_amdgcn_exp2f(mlog - M1) * __builtin_amdgcn_rcpf(L1);
    const unsigned lse = f2u(M2 + __builtin_amdgcn_logf(L2));
    t1w = __builtin_amdgcn_perm(tagh, lse, 0x05040100u); t3w = __builtin_amdgcn_perm(tagh, lse, 0x07060302u);
  }
  unsigned mw = lds0 + L_S + L_MRG + w * 2048 + lane * 16, mr = lds0 + L_S + L_MRG + ((lane >> 2) & 1) * 1024 + (((lane >> 3) & 3) * 16 + hd * 4 + (lane & 3)) * 16;
  int pao = OFF_PART + ((b * HQ + kvh * 4 + hd) * NSPLIT + sp) * 512 + (lane & 31) * 16;
  PIN(mw); PIN(mr); PIN(pao); PIN(fw); PIN(t1w); PIN(t3w);
#pragma unroll
  for (int d = 0; d < 4; ++d) { PMFMA(d, lb[d]); if (d == 2) asm volatile("" :: "v"(bal[0]), "v"(bal[1])); }
  STAMP(3);
  *(LAS f32x4*)mw = acc0 * fw; *(LAS f32x4*)(mw + 1024) = acc1 * fw;
  barrier();
  if (w < 2) {
    const f32x4 o = (*(LAS f32x4*)mr + *(LAS f32x4*)(mr + 2048)) + (*(LAS f32x4*)(mr + 4096) + *(LAS f32x4*)(mr + 6144));
    ST128((v4u{cvtpk(o[0], o[1]), t1w, cvtpk(o[2], o[3]), t3w}), pao);
  }
  STAMP(4);
  const unsigned tend = (unsigned)__builtin_amdgcn_s_memrealtime();
  const int ci = CI();
  WUPTO(role == 1 ? 24 : role == 2 ? 16 : 0, 40);
  unsigned dm0 = (c & 7) == 0 ? 0xffffu : (c & 7) == 1 ? 0xffff0000u : 0u, dm1 = (c & 7) == 2 ? 0xffffu : (c & 7) == 3 ? 0xffff0000u : 0u,
           dm2 = (c & 7) == 4 ? 0xffffu : (c & 7) == 5 ? 0xffff0000u : 0u, dm3 = (c & 7) == 6 ? 0xffffu : (c & 7) == 7 ? 0xffff0000u : 0u;
  unsigned tb0 = tagh & 0xffu, tb1 = (tagh >> 8) & 0xffu, tb2 = (tagh >> 16) & 0xffu, tb3 = tagh >> 24;
#define PUBLISH_ACC(acc, off) ST128((v4u{PK24(acc[0], tb0), PK24(acc[1], tb1), PK24(acc[2], tb2), PK24(acc[3], tb3)}), (off))

  if (role == 2) {
    unsigned wra = lds0 + L_W + w * WREG + lane * 16;
    PIN(wra);
    const int head = bi >> 1;
    const int po = OFF_PART + (((lane >> 5) * HQ + head) * NSPLIT + ((lane >> 2) & 7)) * 512 + (bi & 1) * 256 + w * 64 + (lane & 3) * 16;
    unsigned ow = lds0 + L_S + L_OB + ((g >> 1) * 4 + (lane & 3)) * 64 + (8 * (w >> 1) + 2 * ((g & 1) + ((lane & 4) >> 1)) + (w & 1)) * 4, orr = lds0 + L_S + L_OB + ((c >> 3) * 4 + g) * 64;
    int so_ = OFF_P1 + bi * 4096 + tid * 16;
    f32x4 acc = {0.f, 0.f, 0.f, 0.f};
    PIN(ow); PIN(orr); PIN(so_); PIN(dm0); PIN(dm1); PIN(dm2); PIN(dm3); PIN(tb0); PIN(tb1); PIN(tb2); PIN(tb3); PIN(acc);
    v4u a0, a1;
    poll2<PD1, 16>(ws, po, po + 8 * 512, tagh, a0, a1);
    STAMP(5);
    v4u wr[16];
#pragma unroll
    for (int i = 0; i < 16; ++i) wr[i] = *(LAS v4u*)(wra + i * 1024);
    SB();
    const float l0 = u2f(__builtin_amdgcn_perm(a0[3], a0[1], 0x05040100u)), l1 = u2f(__builtin_amdgcn_perm(a1[3], a1[1], 0x05040100u));
    float xm = vmax(l0, l1);
    xm = vmax(vmax(xm, dpp<ROR4>(xm)), vmax(dpp<ROR8>(xm), dpp<ROR12>(xm)));
    xm = sw16_max(xm, xm);
    const float w0 = __builtin_amdgcn_exp2f(l0 - xm), w1 = __builtin_amdgcn_exp2f(l1 - xm);
    float u0 = sw16_sum(fmaf(w1, bflo(a1[0]), w0 * bflo(a0[0])), fmaf(w1, bfhi(a1[0]), w0 * bfhi(a0[0])));
    float u1 = sw16_sum(fmaf(w1, bflo(a1[2]), w0 * bflo(a0[2])), fmaf(w1, bfhi(a1[2]), w0 * bfhi(a0[2])));
    float ws_ = w0 + w1;
    ws_ = sw16_sum(ws_, ws_);
    u0 += dpp<ROR8>(u0); u1 += dpp<ROR8>(u1); ws_ += dpp<ROR8>(ws_);
    u0 += dpp<ROR4>(u0); u1 += dpp<ROR4>(u1); ws_ += dpp<ROR4>(ws_);
    const float uo = ((lane & 4) ? u1 : u0) * __builtin_amdgcn_rcpf(ws_);
    *(LAS unsigned*)ow = cvtpk(uo, uo);
    barrier();
    const v4u pv0 = *(LAS v4u*)orr, pv1 = *(LAS v4u*)(orr + 16), pv2 = *(LAS v4u*)(orr + 32), pv3 = *(LAS v4u*)(orr + 48);
    diag_chain_regs(acc, wr, pv0, pv1, dm0, dm1, dm2, dm3);
    diag_chain_regs(acc, wr + 8, pv2, pv3, dm0, dm1, dm2, dm3);
    mfma_drain(acc);
    PUBLISH_ACC(acc, so_);
    STAMP(6);
    poll_done(a0, a1);
  } else if (role == 3) {
    const int rb = rj & 1, col0 = (rj >> 1) * 32;
    unsigned xp = *(const GAS unsigned*)(x + rb * D + col0 + 2 * c);
    asm volatile("" : "+v"(xp));
    const int pp = lane >> 3;
    float h0, h1;
    unsigned rw_ = lds0 + L_S + L_RR + w * 128 + (8 * g + (lane & 7)) * 4, rr_ = lds0 + L_S + L_RR + c * 8;
    int hbo = OFF_HB + ((w & 1) * 4 + g) * 4096 + rb * 2048 + col0 * 4 + c * 8;
    GAS unsigned* op = (GAS unsigned*)(out + rb * D + col0 + 2 * c);
    PIN(rw_); PIN(rr_); PIN(hbo); PIN(op);
    {
      const int po = OFF_P1 + (16 * w + pp) * 4096 + rj * 128 + (lane & 7) * 16;
      v4u a0, a1;
      poll2<PD2, -1>(ws, po, po + 8 * 4096, tagh, a0, a1);
      STAMP(5);
      float u = sw16_sum(sw32_sum(UP24(a0[0]) + UP24(a1[0]), UP24(a0[2]) + UP24(a1[2])), sw32_sum(UP24(a0[1]) + UP24(a1[1]), UP24(a0[3]) + UP24(a1[3])));
      u += dpp<ROR8>(u);
      *(LAS float*)rw_ = u;
      barrier();
      const f32x2 s = (*(LAS f32x2*)rr_ + *(LAS f32x2*)(rr_ + 128)) + (*(LAS f32x2*)(rr_ + 256) + *(LAS f32x2*)(rr_ + 384));
      const unsigned at = cvtpk(s[0], s[1]);
      const unsigned hp = cvtpk(bflo(xp) + bflo(at), bfhi(xp) + bfhi(at));
      h0 = bflo(hp); h1 = bfhi(hp);
      if (w < 2) __builtin_amdgcn_raw_buffer_store_b64(v2u{hp, tag}, rw, hbo, 0, AUX_SC1);
      STAMP(6);
      poll_done(a0, a1);
    }
    {
      const int po = OFF_P2 + (14 * w + pp) * 4096 + rj * 128 + (lane & 7) * 16;
      v4u a0, b1;
      poll2<PD4, -1>(ws, po, po + (pp < 6 ? 8 * 4096 : 0), tagh, a0, b1);
      STAMP(7);
      const unsigned km = pp < 6 ? 0xffffff00u : 0u;
      float u = sw16_sum(sw32_sum(UP24(a0[0]) + u2f(b1[0] & km), UP24(a0[2]) + u2f(b1[2] & km)), sw32_sum(UP24(a0[1]) + u2f(b1[1] & km), UP24(a0[3]) + u2f(b1[3] & km)));
      u += dpp<ROR8>(u);
      *(LAS float*)(rw_ + 512) = u;
      barrier();
      if (tid < 16) {
        const f32x2 s = (*(LAS f32x2*)(rr_ + 512) + *(LAS f32x2*)(rr_ + 640)) + (*(LAS f32x2*)(rr_ + 768) + *(LAS f32x2*)(rr_ + 896));
        const unsigned dn = cvtpk(s[0], s[1]);
        *op = cvtpk(h0 + bflo(dn), h1 + bfhi(dn));
        if (rj == 0 && tid == 0) __builtin_amdgcn_raw_buffer_store_b32(tag, rw, OFF_EPOCH, 0, AUX_SC1);
      }
      STAMP(8);
      poll_done(a0, b1);
    }
  } else if (role == 1) {
    v4u fg[8], fu[8], wd[8];
    LAS unsigned char* fa = L + L_W + w * WREG + (8 * g + q4) * 64 + (p4 & 1) * 8;
    VMWAIT(16);
#pragma unroll
    for (int ks = 0; ks < 4; ++ks)
#pragma unroll
      for (int t = 0; t < 2; ++t) { LAS unsigned char* a = fa + 2048 * ks + (((2 * t + (p4 >> 1)) ^ ((g & 1) << 1)) * 16); fg[ks * 2 + t] = tr_frag(a, a + 256); }
    VMWAIT(8);
    STAMP(10);
#pragma unroll
    for (int ks = 0; ks < 4; ++ks)
#pragma unroll
      for (int t = 0; t < 2; ++t) { LAS unsigned char* a = fa + 8192 + 2048 * ks + (((2 * t + (p4 >> 1)) ^ ((g & 1) << 1)) * 16); fu[ks * 2 + t] = tr_frag(a, a + 256); }
    f32x4 ag[2], au[2];
    unsigned ro = lds0 + L_S + L_RED + ((c >> 2) & 1) * 1024 + ((c & 1) * 32 + (c & 2) * 8 + 4 * g) * 16 + w * 4;
    unsigned fo = lds0 + L_S + L_RED + ((c >> 3) * 32 + 4 * (c & 3) + g) * 16;
#define GU_MFMA(XB) do { _Pragma("unroll") for (int t = 0; t < 2; ++t) { ag[t] = f32x4{0.f, 0.f, 0.f, 0.f}; au[t] = f32x4{0.f, 0.f, 0.f, 0.f}; } \
      _Pragma("unroll") for (int ks = 0; ks < 4; ++ks) { \
        _Pragma("unroll") for (int t = 0; t < 2; ++t) { ag[t] = mfma32(fg[ks * 2 + t], XB, ag[t]); au[t] = mfma32(fu[ks * 2 + t], XB, au[t]); } } } while (0)
#define GU_STORE() do { const f32x4 sel = (c & 4) ? ((c & 2) ? au[1] : au[0]) : ((c & 2) ? ag[1] : ag[0]); \
      _Pragma("unroll") for (int i = 0; i < 4; ++i) *(LAS float*)(ro + i * 16) = sel[i]; } while (0)
#define SUM4(p_) ({ const f32x2 t_ = f32x2{p_[0], p_[1]} + f32x2{p_[2], p_[3]}; float r_ = t_[0] + t_[1]; PIN(r_); r_; })
    GU_MFMA((v4u{0x3f803f80u, 0x3f803f80u, 0x3f803f80u, 0x3f803f80u}));
    GU_STORE();
    barrier();
    float cg0, cg1, cu0, cu1;
    { const f32x4 p0 = *(LAS f32x4*)fo, p1 = *(LAS f32x4*)(fo + 256), p2 = *(LAS f32x4*)(fo + 1024), p3 = *(LAS f32x4*)(fo + 1280); cg0 = SUM4(p0); cg1 = SUM4(p1); cu0 = SUM4(p2); cu1 = SUM4(p3); }
    unsigned xw = lds0 + L_S + L_XN + (w & 1) * 1024 + lane * 16, xr = lds0 + L_S + L_XN + (c & 1) * 1024 + (128 * w + 8 * g) * 2, sw_ = lds0 + L_S + L_ST + w * 8, sr = lds0 + L_S + L_ST + (c >> 3) * 8;
    int so_ = OFF_P2 + ci * 4096 + tid * 16;
    f32x4 acc = {0.f, 0.f, 0.f, 0.f};
    unsigned wda = lds0 + L_W + w * WREG + 16384 + lane * 16;
    PIN(wda); PIN(ro); PIN(fo); PIN(xw); PIN(xr); PIN(sw_); PIN(sr); PIN(so_); PIN(dm0); PIN(dm1); PIN(dm2); PIN(dm3); PIN(tb0); PIN(tb1); PIN(tb2); PIN(tb3); PIN(acc);
    v4u ha = {0u, 0u, 0u, 0u}, hb = ha;
    if (w < 2) {
      const int ho = OFF_HB + xcd * 4096 + w * 2048 + lane * 32;
      poll2<PD3, 0>(ws, ho, ho + 16, tag, ha, hb);
      STAMP(5);
      *(LAS v4u*)xw = v4u{ha[0], ha[2], hb[0], hb[2]};
    } else VMWAIT(0);
    barrier();
    v4u xb[4];
#pragma unroll
    for (int ks = 0; ks < 4; ++ks) xb[ks] = *(LAS v4u*)(xr + 64 * ks);
#pragma unroll
    for (int i = 0; i < 8; ++i) wd[i] = *(LAS v4u*)(wda + i * 1024);
    __builtin_amdgcn_sched_barrier(0);
    const float pv = bflo(__builtin_amdgcn_readfirstlane(ha[0]));
    const float d0 = bflo(ha[0]) - pv, d1 = bfhi(ha[0]) - pv, d2 = bflo(ha[2]) - pv, d3 = bfhi(ha[2]) - pv, d4 = bflo(hb[0]) - pv, d5 = bfhi(hb[0]) - pv, d6 = bflo(hb[2]) - pv, d7 = bfhi(hb[2]) - pv;
    float s1 = ((d0 + d1) + (d2 + d3)) + ((d4 + d5) + (d6 + d7));
    float s2 = (fmaf(d1, d1, d0 * d0) + fmaf(d3, d3, d2 * d2)) + (fmaf(d5, d5, d4 * d4) + fmaf(d7, d7, d6 * d6));
    __builtin_amdgcn_sched_barrier(0);
    GU_MFMA(xb[ks]);
    {
      float t = sw32_sum(s1, s2);
      t = sw16_sum(t, t); t += dpp<ROR8>(t); t += dpp<ROR4>(t); t += dpp<XOR2>(t); t += dpp<XOR1>(t);
      const float md = u2f(__builtin_amdgcn_readlane(f2u(t), 0)) * (1.0f / D), q2 = u2f(__builtin_amdgcn_readlane(f2u(t), 32)) * (1.0f / D);
      const float var = vmax(fmaf(-md, md, q2), 0.f);
      const float rs = __builtin_amdgcn_rcpf(rbf(__builtin_amdgcn_sqrtf(rbf(rbf(var) + 1e-5f))));
      *(LAS f32x2*)sw_ = f32x2{rbf(pv + md), rs};
    }
    GU_STORE();
    barrier();
    const f32x2 st = *(LAS f32x2*)sr;
    const f32x4 p0 = *(LAS f32x4*)fo, p1 = *(LAS f32x4*)(fo + 256), p2 = *(LAS f32x4*)(fo + 1024), p3 = *(LAS f32x4*)(fo + 1280);
    __builtin_amdgcn_sched_barrier(0);
    const float G0 = SUM4(p0), G1 = SUM4(p1), U0 = SUM4(p2), U1 = SUM4(p3);
    const unsigned gp = cvtpk(fmaf(-st[0], cg0, G0) * st[1], fmaf(-st[0], cg1, G1) * st[1]);
    const unsigned up = cvtpk(fmaf(-st[0], cu0, U0) * st[1], fmaf(-st[0], cu1, U1) * st[1]);
    const float g0 = bflo(gp), g1 = bfhi(gp);
    const unsigned sg = cvtpk(g0 * __builtin_amdgcn_rcpf(1.0f + __builtin_amdgcn_exp2f(-g0 * LOG2E)), g1 * __builtin_amdgcn_rcpf(1.0f + __builtin_amdgcn_exp2f(-g1 * LOG2E)));
    const float av0 = bflo(sg) * bflo(up), av1 = bfhi(sg) * bfhi(up);
    diag_chain_quad(acc, wd, cvtpk(av0, av0), cvtpk(av1, av1), dm0, dm1, dm2, dm3);
    mfma_drain(acc);
    PUBLISH_ACC(acc, so_);
    STAMP(6);
    poll_done(ha, hb);
  }
  WAIT_UNTIL(5500, 16);
  if (tid == 0) __builtin_amdgcn_raw_buffer_store_b64(v2u{tstart, *(LAS unsigned*)(L + L_TAG + 4)}, rw, 0x2000 + wg * 8, 0, AUX_SC1);
  if (role == 3 && rj == 0 && w == 0) {
    WAIT_UNTIL(6200, 16);
    int mn = 0x7fffffff, mx = -0x7fffffff;
    for (int k = 0; k < 2; ++k) { const v4u ts = LD128(0x2000 + k * 1024 + lane * 16);
      for (int i = 0; i < 4; i += 2) { const int d = (int)(ts[i] - tstart), e = (int)(ts[i + 1] - tstart); mn = d < mn ? d : mn; mx = e > mx ? e : mx; } }
    for (int o = 32; o >= 1; o >>= 1) { const int a = __shfl_xor(mn, o), bb = __shfl_xor(mx, o); mn = a < mn ? a : mn; mx = bb > mx ? bb : mx; }
    mn = __builtin_amdgcn_readfirstlane(mn); mx = __builtin_amdgcn_readfirstlane(mx);
    int dl = mx - mn; dl = dl < 0 ? 0 : dl > 6000 ? 6000 : dl;
    WAIT_UNTIL(7000 + mn + 4 * dl, 1);
  }
}

extern "C" void kernel_launch(void* const* d_in, const int* in_sizes, int n_in, void* d_out, int out_size, void* d_ws, size_t ws_size, hipStream_t stream) {
  fused_block<<<NWG, NTHR, 0, stream>>>((const u16*)d_in[0], (const u16*)d_in[1], (const u16*)d_in[2], (const u16*)d_in[3], (unsigned char*)d_ws,
                                        (const u16*)d_in[4], (const u16*)d_in[6], (const u16*)d_in[7], (const u16*)d_in[8], (u16*)d_out, make_plan() TUNE_ARG);
}
```

```cpp
#pragma clang diagnostic ignored "-Wint-to-pointer-cast"
#include <hip/hip_runtime.h>
#include <hip/hip_bf16.h>
#include <cstdint>

typedef unsigned short u16;
typedef unsigned v4u __attribute__((ext_vector_type(4)));
typedef unsigned v2u __attribute__((ext_vector_type(2)));
typedef float f32x4 __attribute__((ext_vector_type(4)));
typedef float f32x2 __attribute__((ext_vector_type(2)));
typedef short s16x4 __attribute__((ext_vector_type(4)));
typedef __bf16 bf16x8_t __attribute__((ext_vector_type(8)));
typedef __bf16 bf16x2_t __attribute__((ext_vector_type(2)));
#define LAS __attribute__((address_space(3)))
#define GAS __attribute__((address_space(1)))

constexpr int D = 512, HQ = 32, HKV = 8, SK = 32768, DH = 128, F = 1792;
constexpr int NWG = 256, NTHR = 320, NSPLIT = 16;
struct Plan { unsigned e[32]; };
#define TUNE_PARAM
#define TUNE_ARG
#define ABL 0
#define TL_C 130
#define TL_CS 129
#define TL_B 128
#define TL_Q 124
#define TL_QS 120
#define T_WC 0
#define T_WB 0
#define T_L 3500
#define T_PD 150
#define T_P 680
static Plan make_plan() {
  Plan p;
  for (int b = 0; b < 2; ++b) {
    const int nc = 4 - b;
    int n[16], role[16], ix[16], used = 0, nn = 0;
    for (int sp = 0; sp < 16; ++sp) {
      if (sp < nc) { role[sp] = 1; ix[sp] = 2 * sp + b; n[sp] = sp == 0 ? TL_CS : TL_C; }
      else if (sp >= 4 && sp < 8) { role[sp] = 2; ix[sp] = 2 * (sp - 4) + b; n[sp] = TL_B; }
      else if (sp >= 12) { role[sp] = 4; ix[sp] = 2 * (sp - 12) + b; n[sp] = ix[sp] == 3 ? TL_QS : TL_Q; }
      else { role[sp] = (sp == 8 || sp == 9) ? 3 : 0; ix[sp] = role[sp] ? 2 * (sp - 8) + b : 0; n[sp] = 0; ++nn; }
      used += n[sp];
    }
    int rest = 2048 - used, t0 = 0;
    for (int sp = 0; sp < 16; ++sp) {
      if (n[sp] == 0) { n[sp] = (rest + nn - 1) / nn; rest -= n[sp]; --nn; }
      p.e[2 * sp + b] = (unsigned)t0 | ((unsigned)n[sp] << 11) | ((unsigned)role[sp] << 19) | ((unsigned)ix[sp] << 22);
      t0 += n[sp];
    }
  }
  return p;
}
#define HMASK 0x0F0F
#define HEAVY(d, s) ((HMASK >> (4 * ((d) & 3) + (s))) & 1)
#define NINST(d) (4 + __builtin_popcount((HMASK >> (4 * ((d) & 3))) & 15))
#define NPRE 10
#define NLT 6
#define TOUCH_EARLY 1
#define NLR 2
#define PD1 2
#define PD2 3
#define PD3 1
#define PD4 3
#define PSL 1
#define PACE_N 4
#define CODE_BYTES 32768
constexpr int NB = 64, NC = 56, NR = 64, NREP = 8;
constexpr int OFF_EPOCH = 0;
constexpr int OFF_HB = 0x190000;
constexpr int OFF_PART = 0x10000;
constexpr int OFF_P1 = 0x90000;
constexpr int OFF_P2 = 0xD0000;
constexpr int OFF_Q = 0x180000;
constexpr int WS_BYTES = 0x1A0000, OFF_DBG = 0x200000;
constexpr int AUX_NT = 2, AUX_SC1 = 16;
constexpr unsigned SPIN_CAP = 1u << 16;
constexpr int L_W = 0;
constexpr int WREG = 24576;
constexpr int L_QF = 4 * WREG;
constexpr int L_SC = L_QF + 1024;
constexpr int L_TAG = L_SC + 4 * 40 * 128;
constexpr int L_S = L_TAG + 16;
constexpr int L_XN = 0;
constexpr int L_RED = L_XN + 2048;
constexpr int L_MRG = L_RED + 2048;
constexpr int L_OB = L_MRG + 4 * 4 * 132 * 4;
constexpr int L_RR = L_OB + 512;
constexpr int L_ST = L_RR + 1024;
constexpr int S_BYTES = L_ST + 32;
constexpr int L_PF = L_S + S_BYTES;
constexpr int L_DBG = L_PF + 256;
constexpr int L_BYTES = L_DBG + 128;

__device__ __forceinline__ float bflo(unsigned v) { return __uint_as_float(v << 16); }
__device__ __forceinline__ float bfhi(unsigned v) { return __uint_as_float(v & 0xffff0000u); }
__device__ __forceinline__ unsigned cvtpk(float lo, float hi) { return __builtin_bit_cast(unsigned, __builtin_convertvector(f32x2{lo, hi}, bf16x2_t)); }
__device__ __forceinline__ float rbf(float x) { return bflo(cvtpk(x, x)); }
__device__ __forceinline__ bool tagged(v4u v, unsigned tag) { return (v[1] == tag) & (v[3] == tag); }
__device__ __forceinline__ float u2f(unsigned v) { return __uint_as_float(v); }
__device__ __forceinline__ unsigned f2u(float v) { return __float_as_uint(v); }

__device__ __forceinline__ float vmax(float a, float b) { return __builtin_elementwise_maximum(a, b); }
template <int CTRL> __device__ __forceinline__ float dpp(float x) { return __builtin_bit_cast(float, __builtin_amdgcn_mov_dpp(__builtin_bit_cast(int, x), CTRL, 0xf, 0xf, true)); }
template <int CTRL> __device__ __forceinline__ unsigned dppu(unsigned x) { return (unsigned)__builtin_amdgcn_mov_dpp((int)x, CTRL, 0xf, 0xf, true); }
constexpr int XOR1 = 0xB1, XOR2 = 0x4E, ROR8 = 0x128, ROR4 = 0x124, ROR12 = 0x12C;
__device__ __forceinline__ float sw32_sum(float a, float b) { auto r = __builtin_amdgcn_permlane32_swap(f2u(a), f2u(b), false, false); return u2f(r[0]) + u2f(r[1]); }
__device__ __forceinline__ float sw16_sum(float a, float b) { auto r = __builtin_amdgcn_permlane16_swap(f2u(a), f2u(b), false, false); return u2f(r[0]) + u2f(r[1]); }
__device__ __forceinline__ float sw32_max(float a, float b) { auto r = __builtin_amdgcn_permlane32_swap(f2u(a), f2u(b), false, false); return vmax(u2f(r[0]), u2f(r[1])); }
__device__ __forceinline__ float sw16_max(float a, float b) { auto r = __builtin_amdgcn_permlane16_swap(f2u(a), f2u(b), false, false); return vmax(u2f(r[0]), u2f(r[1])); }
__device__ __forceinline__ float wave_sum(float x) {
  x += dpp<XOR1>(x); x += dpp<XOR2>(x); x += dpp<ROR4>(x); x += dpp<ROR8>(x);
  x = sw16_sum(x, x); return sw32_sum(x, x);
}
__device__ __forceinline__ f32x4 mfma32(v4u a, v4u b, f32x4 c) { return __builtin_amdgcn_mfma_f32_16x16x32_bf16(__builtin_bit_cast(bf16x8_t, a), __builtin_bit_cast(bf16x8_t, b), c, 0, 0, 0); }
__device__ __forceinline__ f32x4 mfma16(v2u a, v2u b, f32x4 c) { return __builtin_amdgcn_mfma_f32_16x16x16bf16_1k(__builtin_bit_cast(s16x4, a), __builtin_bit_cast(s16x4, b), c, 0, 0, 0); }
__device__ __forceinline__ void barrier() { asm volatile("s_waitcnt lgkmcnt(0)\n\ts_barrier" ::: "memory"); }
__device__ __forceinline__ v4u tr_frag(LAS unsigned char* lo, LAS unsigned char* hi) {
  const v2u a = __builtin_bit_cast(v2u, __builtin_amdgcn_ds_read_tr16_b64_v4i16((LAS s16x4*)lo));
  const v2u b = __builtin_bit_cast(v2u, __builtin_amdgcn_ds_read_tr16_b64_v4i16((LAS s16x4*)hi));
  return v4u{a[0], a[1], b[0], b[1]};
}

#define AND4(T0, T1, T2, T3, SRC, MOD, SFX) "v_and_b32" SFX " " T0 ", " SRC ", %[m0]" MOD "\n\tv_and_b32" SFX " " T1 ", " SRC ", %[m1]" MOD "\n\tv_and_b32" SFX " " T2 ", " SRC ", %[m2]" MOD "\n\tv_and_b32" SFX " " T3 ", " SRC ", %[m3]" MOD "\n\t"
#define ANDA(SRC, MOD, SFX) AND4("v240", "v241", "v242", "v243", SRC, MOD, SFX)
#define ANDB(SRC, MOD, SFX) AND4("v244", "v245", "v246", "v247", SRC, MOD, SFX)
#define MFA(W) "v_mfma_f32_16x16x32_bf16 %[acc], " W ", v[240:243], %[acc]\n\t"
#define MFB(W) "v_mfma_f32_16x16x32_bf16 %[acc], " W ", v[244:247], %[acc]\n\t"
#define CHAIN_CLOB "v240", "v241", "v242", "v243", "v244", "v245", "v246", "v247"
#define CHAIN_W(W) [w0] "v"(W[0]), [w1] "v"(W[1]), [w2] "v"(W[2]), [w3] "v"(W[3]), [w4] "v"(W[4]), [w5] "v"(W[5]), [w6] "v"(W[6]), [w7] "v"(W[7])
__device__ __forceinline__ void diag_chain_regs(f32x4& acc, const v4u* W, v4u pa, v4u pb, unsigned m0, unsigned m1, unsigned m2, unsigned m3) {
  asm volatile(ANDA("%[p0]", "", "") ANDB("%[p1]", "", "") MFA("%[w0]") ANDA("%[p2]", "", "") MFB("%[w1]") ANDB("%[p3]", "", "") MFA("%[w2]") ANDA("%[p4]", "", "") MFB("%[w3]")
               ANDB("%[p5]", "", "") MFA("%[w4]") ANDA("%[p6]", "", "") MFB("%[w5]") ANDB("%[p7]", "", "") MFA("%[w6]") "s_nop 0\n\t" MFB("%[w7]")
               : [acc] "+v"(acc)
               : CHAIN_W(W), [p0] "v"(pa[0]), [p1] "v"(pa[1]), [p2] "v"(pa[2]), [p3] "v"(pa[3]), [p4] "v"(pb[0]), [p5] "v"(pb[1]), [p6] "v"(pb[2]), [p7] "v"(pb[3]),
                 [m0] "v"(m0), [m1] "v"(m1), [m2] "v"(m2), [m3] "v"(m3)
               : CHAIN_CLOB);
}
#define QP(t) " quad_perm:[" #t "," #t "," #t "," #t "] row_mask:0xf bank_mask:0xf"
__device__ __forceinline__ void diag_chain_quad(f32x4& acc, const v4u* W, unsigned pa, unsigned pb, unsigned m0, unsigned m1, unsigned m2, unsigned m3) {
  asm volatile("s_nop 1\n\t" ANDA("%[pa]", QP(0), "_dpp") ANDB("%[pa]", QP(1), "_dpp") MFA("%[w0]") ANDA("%[pa]", QP(2), "_dpp") MFB("%[w1]") ANDB("%[pa]", QP(3), "_dpp") MFA("%[w2]")
               ANDA("%[pb]", QP(0), "_dpp") MFB("%[w3]") ANDB("%[pb]", QP(1), "_dpp") MFA("%[w4]") ANDA("%[pb]", QP(2), "_dpp") MFB("%[w5]") ANDB("%[pb]", QP(3), "_dpp") MFA("%[w6]")
               "s_nop 0\n\t" MFB("%[w7]")
               : [acc] "+v"(acc)
               : CHAIN_W(W), [pa] "v"(pa), [pb] "v"(pb), [m0] "v"(m0), [m1] "v"(m1), [m2] "v"(m2), [m3] "v"(m3)
               : CHAIN_CLOB);
}
__device__ __forceinline__ void mfma_drain(f32x4& acc) { asm volatile("s_nop 7" : "+v"(acc)); }

__device__ __forceinline__ v4u ln_row(v4u xv) {
  float x[8];
#pragma unroll
  for (int i = 0; i < 4; ++i) { x[2 * i] = bflo(xv[i]); x[2 * i + 1] = bfhi(xv[i]); }
  const float mean = wave_sum(((x[0] + x[1]) + (x[2] + x[3])) + ((x[4] + x[5]) + (x[6] + x[7]))) * (1.0f / D);
  float vs = 0.f;
#pragma unroll
  for (int i = 0; i < 8; ++i) { const float d = x[i] - mean; vs = fmaf(d, d, vs); }
  const float var = wave_sum(vs) * (1.0f / D);
  const float mb = rbf(mean);
  const float rs = __builtin_amdgcn_rcpf(rbf(__builtin_amdgcn_sqrtf(rbf(rbf(var) + 1e-5f))));
  v4u r;
#pragma unroll
  for (int i = 0; i < 4; ++i) { const unsigned d = cvtpk(x[2 * i] - mb, x[2 * i + 1] - mb); r[i] = cvtpk(bflo(d) * rs, bfhi(d) * rs); }
  return r;
}
__device__ __forceinline__ void glds16(const u16* gsrc, unsigned lds_dst) {
  unsigned keep;
  asm volatile("s_mov_b32 %0, m0\n\ts_mov_b32 m0, %2\n\ts_nop 0\n\tglobal_load_lds_dwordx4 %1, off nt\n\ts_mov_b32 m0, %0" : "=&s"(keep) : "v"(gsrc), "s"(lds_dst) : "memory");
}

__device__ __forceinline__ unsigned mixtag(unsigned t) { t *= 0x9E3779B1u; return t ^ (t >> 15); }
#define PK24(x, tb) (((f2u(x) + 0x80u) & 0xffffff00u) | (tb))
#define UP24(x) u2f((x) & 0xffffff00u)
__device__ __forceinline__ unsigned lowbytes(v4u v) { return __builtin_amdgcn_perm(v[1], v[0], 0x0c0c0400u) | __builtin_amdgcn_perm(v[3], v[2], 0x04000c0cu); }
template <int PD_, int TSH>
__device__ __forceinline__ void poll2(const unsigned char* ws, int o0, int o1, unsigned tag, v4u& r0, v4u& r1) {
  r0 = v4u{~tag, ~tag, ~tag, ~tag}; r1 = r0;
#define PASS() asm volatile("global_load_dwordx4 %0, %2, %4 sc1\n\tglobal_load_dwordx4 %1, %3, %4 sc1" : "+v"(r0), "+v"(r1) : "v"(o0), "v"(o1), "s"(ws) : "memory")
#pragma unroll
  for (int k = 0; k < PD_ - 1; ++k) { PASS(); __builtin_amdgcn_s_sleep(PSL); }
#pragma unroll 1
  for (unsigned k = 0; k < SPIN_CAP; ++k) {
    PASS();
    asm volatile("s_waitcnt vmcnt(%2)" : "+v"(r0), "+v"(r1) : "i"((PD_ - 1) * 2) : "memory");
    if (TSH == 16) { if (__all((__builtin_amdgcn_perm(r0[3], r0[1], 0x07060302u) == tag) & (__builtin_amdgcn_perm(r1[3], r1[1], 0x07060302u) == tag))) break; }
    else if (TSH < 0) { if (__all((lowbytes(r0) == tag) & (lowbytes(r1) == tag))) break; }
    else if (__all((((r0[1] ^ tag) | (r0[3] ^ tag) | (r1[1] ^ tag) | (r1[3] ^ tag)) >> (TSH < 0 ? 0 : TSH)) == 0u)) break;
    if (PD_ > 1) __builtin_amdgcn_s_sleep(PSL);
  }
#undef PASS
  asm volatile("s_nop 3 ; POLL_END" : "+v"(r0), "+v"(r1));
}
__device__ __forceinline__ void poll_done(v4u& r0, v4u& r1) { asm volatile("s_waitcnt vmcnt(0)" : "+v"(r0), "+v"(r1) :: "memory"); }

#define STAMP(i) do { } while (0)
#define DUMP_STAMPS() do { } while (0)
#define LD128(off) __builtin_amdgcn_raw_buffer_load_b128(rw, (off), 0, AUX_SC1)
#define ST128(v, off) __builtin_amdgcn_raw_buffer_store_b128((v), rw, (off), 0, AUX_SC1)
#define SPIN_LOOP _Pragma("unroll 1") for (unsigned spins = 0; spins < SPIN_CAP; ++spins)
#define NOHOIST() asm volatile("" ::: "memory")

__global__ void __launch_bounds__(NTHR, 2) fused_block(const u16* x_, const u16* kc_, const u16* vc_, const u16* wq_, unsigned char* ws_,
                                                       const u16* wo_, const u16* wgate_, const u16* wup_, const u16* wdown_, u16* out_, Plan plan_ TUNE_PARAM) {
  typedef unsigned long long u64x8 __attribute__((ext_vector_type(8)));
  typedef unsigned long long u64x2 __attribute__((ext_vector_type(2)));
  u64x8 ka; u64x2 kb2; unsigned pe;
  asm volatile("s_load_dwordx16 %0, %3, 0x0\n\ts_load_dwordx4 %1, %3, 0x40\n\ts_load_dword %2, %3, %4 offset:0x50"
               : "=&s"(ka), "=&s"(kb2), "=&s"(pe) : "s"(__builtin_amdgcn_kernarg_segment_ptr()), "s"((blockIdx.x >> 3) * 4));
  __shared__ __attribute__((aligned(16))) unsigned char smem[L_BYTES];
  LAS unsigned char* L = (LAS unsigned char*)smem;
  STAMP(0);
  const unsigned tstart = (unsigned)__builtin_amdgcn_s_memrealtime();
  const int wg = blockIdx.x, xcd = wg & 7, idx = wg >> 3;
  const int kvh = xcd, b = idx & 1, sp = idx >> 1;
  int lane = threadIdx.x & 63;
  const int w = __builtin_amdgcn_readfirstlane(threadIdx.x >> 6);
  int c = lane & 15, g = lane >> 4;
  if (w == 4 && !(ABL & 32)) {
    unsigned long long pc;
    asm volatile("s_getpc_b64 %0" : "=s"(pc));
    if ((lane * 32 + idx) * 128 < CODE_BYTES) {
      unsigned keep;
      asm volatile("s_mov_b32 %0, m0\n\ts_mov_b32 m0, %3\n\ts_nop 0\n\tglobal_load_lds_dword %1, %2\n\ts_mov_b32 m0, %0"
                   : "=&s"(keep) : "v"((lane * 32 + idx) * 128), "s"(pc & ~255ull), "s"((unsigned)(uintptr_t)L + L_PF) : "memory");
    }
  }
  int koff = (c >> 2) * 1024 + g * 64 + (c & 3) * 16;
  unsigned pbase = (unsigned)(b * HKV + kvh) * (SK * DH * 2);
  asm volatile("s_waitcnt lgkmcnt(0)" : "+s"(ka), "+s"(kb2), "+s"(pe), "+v"(koff), "+s"(pbase));
  const unsigned tval = (unsigned)__builtin_amdgcn_s_memrealtime();
  const u16 *x = (const u16*)ka[0], *wq = (const u16*)ka[3], *wo = (const u16*)ka[5], *wgate = (const u16*)ka[6], *wup = (const u16*)ka[7], *wdown = (const u16*)kb2[0];
  unsigned char* ws = (unsigned char*)ka[4];
  u16* out = (u16*)kb2[1];
  const int tile0 = pe & 2047, ntile = (pe >> 11) & 255;
  const __amdgpu_buffer_rsrc_t rk = __builtin_amdgcn_make_buffer_rsrc((void*)(ka[1] + pbase), 0, SK * DH * 2, 0x00020000);
  const unsigned sbase = (unsigned)(tile0 + w) * 4096u;
  v4u kb[NPRE + 3][4];
#define SB() __builtin_amdgcn_sched_barrier(0)
#define ISSUEK(d, j) do { _Pragma("unroll") for (int s = 0; s < 4; ++s) { \
      if (!HEAVY(d, s)) kb[d][s] = __builtin_amdgcn_raw_buffer_load_b128(rk, koff + s * 256, sbase + (unsigned)(j) * 16384u, AUX_NT); \
      else { const v2u lo_ = __builtin_amdgcn_raw_buffer_load_b64(rk, koff + s * 256, sbase + (unsigned)(j) * 16384u, AUX_NT); NOHOIST(); SB(); \
             const v2u hi_ = __builtin_amdgcn_raw_buffer_load_b64(rk, koff + s * 256 + 8, sbase + (unsigned)(j) * 16384u, AUX_NT); NOHOIST(); \
             kb[d][s] = v4u{lo_[0], lo_[1], hi_[0], hi_[1]}; } } SB(); } while (0)
  const int role = (pe >> 19) & 7, rix = pe >> 22;
  const bool qrole = role == 4;
#define CI() ({ int r_ = rix; asm volatile("" : "+s"(r_)); const int cn_ = xcd * 5 + r_ - (xcd < 7 ? 2 : 0); (xcd < 7 && r_ < 2) ? 8 * xcd + 6 + r_ : cn_ + 2 * ((cn_ * 43) >> 8); })
  const int bi = xcd * 8 + rix, rj = xcd * 4 + rix, qi = bi;
  const __amdgpu_buffer_rsrc_t rw = __builtin_amdgcn_make_buffer_rsrc((void*)ws, 0, WS_BYTES, 0x00020000);
  const unsigned lds0 = (unsigned)(uintptr_t)L;
  LAS unsigned char* S = L + L_S;
#define WAIT_UNTIL(t, slp) do { while ((int)((unsigned)__builtin_amdgcn_s_memrealtime() - (tstart + (unsigned)(t))) < 0) __builtin_amdgcn_s_sleep(slp); } while (0)
  int q4 = (lane >> 2) & 3, p4 = lane & 3;
  constexpr float SCALE = 0.088388347648318440f, LOG2E = 1.4426950408889634f;
  unsigned tag;

  if (w == 4) {
    __builtin_amdgcn_s_setprio(3);
#define TOUCH() \
    {     \
      const int ci = CI(); \
      const unsigned long long vb_ = ka[2] + pbase + (unsigned)tile0 * 4096u, ke_ = ka[1] + pbase + (unsigned)(tile0 + ntile) * 4096u - 128u; \
      const unsigned long long wa_ = role == 1 ? ka[6] + ci * 64 : role == 2 ? ka[5] + bi * 65536 : kb2[1]; \
      const unsigned long long ad = lane == 0 ? vb_ : lane == 1 ? vb_ + (unsigned)ntile * 4096u - 128u : lane == 2 ? ke_ : lane == 3 ? wa_ \
                                  : lane == 4 ? wa_ + 511 * F * 2 : lane == 5 ? ka[7] + ci * 64 : lane == 6 ? ka[7] + ci * 64 + 511 * F * 2 : kb2[0] + ci * 32768; \
      if (lane < (role == 1 ? 8 : 4)) { \
        unsigned keep; \
        asm volatile("s_mov_b32 %0, m0\n\ts_mov_b32 m0, %2\n\ts_nop 0\n\tglobal_load_lds_dword %1, off nt\n\ts_mov_b32 m0, %0" : "=&s"(keep) : "v"(ad), "s"(lds0 + L_PF) : "memory"); \
      } \
    }
    if (qrole) {
      const v4u x0 = *(const GAS v4u*)(x + lane * 8), x1 = *(const GAS v4u*)(x + D + lane * 8);
      tag = __builtin_amdgcn_raw_buffer_load_b32(rw, OFF_EPOCH, 0, AUX_SC1) + 1u;
      const v4u n0 = ln_row(x0), n1 = ln_row(x1);
      *(LAS v4u*)(S + L_XN + lane * 16) = n0; *(LAS v4u*)(S + L_XN + 1024 + lane * 16) = n1;
      STAMP(9);
      barrier();
      if (TOUCH_EARLY) TOUCH();
      barrier();
      const f32x2 s = (*(LAS f32x2*)(S + L_RED + lane * 8) + *(LAS f32x2*)(S + L_RED + 512 + lane * 8)) + (*(LAS f32x2*)(S + L_RED + 1024 + lane * 8) + *(LAS f32x2*)(S + L_RED + 1536 + lane * 8));
      __builtin_amdgcn_raw_buffer_store_b64(v2u{cvtpk(s[0], s[1]), tag}, rw, OFF_Q + ((lane >> 5) * 2048 + qi * 32 + (lane & 31)) * 8, 0, AUX_SC1);
    } else {
      if (TOUCH_EARLY) TOUCH();
      tag = __builtin_amdgcn_raw_buffer_load_b32(rw, OFF_EPOCH, 0, AUX_SC1) + 1u;
    }
    STAMP(1);
    if (lane == 0) *(LAS unsigned*)(L + L_TAG) = tag;
    {
      const int qo = OFF_Q + (b * 2048 + kvh * 256) * 8 + lane * 16;
      v4u q0, q1;
      bool ok0 = false, ok1 = false;
      WAIT_UNTIL(T_P, 2);
      SPIN_LOOP {
        NOHOIST();
        if (!ok0) q0 = LD128(qo);
        if (!ok1) q1 = LD128(qo + 1024);
        ok0 = tagged(q0, tag); ok1 = tagged(q1, tag);
        if (__all(ok0 & ok1)) break;
      }
      *(LAS v2u*)(L + L_QF + lane * 8) = v2u{q0[0], q0[2]};
      *(LAS v2u*)(L + L_QF + 512 + lane * 8) = v2u{q1[0], q1[2]};
      STAMP(2);
      barrier();
    }
    if (!TOUCH_EARLY) TOUCH();
    asm volatile("s_waitcnt vmcnt(0)" ::: "memory");
    return;
  }
  const int nstep = (ntile - w + 3) >> 2;
  const __amdgpu_buffer_rsrc_t rv = __builtin_amdgcn_make_buffer_rsrc((void*)(ka[2] + pbase), 0, SK * DH * 2, 0x00020000);
#define VMWAIT(n) do { asm volatile("s_waitcnt vmcnt(%0)" :: "i"(n) : "memory"); SB(); } while (0)
#define RELANE() do { asm volatile("v_mbcnt_lo_u32_b32 %0, -1, 0\n\tv_mbcnt_hi_u32_b32 %0, -1, %0" : "=v"(lane)); c = lane & 15; g = lane >> 4; } while (0)
  int wp = 0;
#define WUPTO(n, pace) do { _Pragma("unroll 1") while (wp < (n)) { \
      const u16* src_ = role == 2 ? wo + (size_t)(bi * 64 + 4 * wp + g) * D + 128 * w + 8 * c \
                      : wp < 16 ? (wp < 8 ? wgate : wup) + (size_t)(128 * w + 16 * (wp & 7) + (lane >> 2)) * F + ci * 32 + (((lane & 3) ^ ((lane >> 4) & 2)) * 8) \
                                : wdown + (size_t)(ci * 32 + 4 * (wp & 7) + g) * D + 128 * w + 8 * c; \
      VMWAIT(pace); glds16(src_, lds0 + L_W + w * WREG + wp * 1024); ++wp; } } while (0)
  if (qrole) {
    const int r8 = lane >> 3;
    const u16* p = wq + (size_t)(128 * w + r8) * (HQ * DH) + qi * 64;
    const int ch0 = ((lane & 7) ^ (r8 & 2)) * 8;
#pragma unroll 1
    for (int t = 0; t < 16; t += 2) {
      glds16(p + ch0, lds0 + L_W + w * WREG + t * 1024);
      glds16(p + 8 * (HQ * DH) + (ch0 ^ 32), lds0 + L_W + w * WREG + t * 1024 + 1024);
      p += 16 * (HQ * DH);
    }
    STAMP(7);
  }
  STAMP(11);
  if (!qrole) { ISSUEK(0, 0); ISSUEK(1, 1); }
  else {
    const int sw = (q4 & 2) | ((g & 1) << 2);
    v4u fr[16];
#define QFRAGS(ks) do { _Pragma("unroll") for (int t = 0; t < 4; ++t) { \
        LAS unsigned char* a = L + L_W + w * WREG + (ks) * 4096 + g * 1024 + q4 * 128 + (p4 & 1) * 8 + (((2 * t + (p4 >> 1)) ^ sw) * 16); fr[(ks) * 4 + t] = tr_frag(a, a + 512); } } while (0)
    VMWAIT(8); QFRAGS(0); QFRAGS(1); ISSUEK(0, 0);
    VMWAIT(4 + NINST(0)); QFRAGS(2); ISSUEK(1, 1);
    barrier();
    f32x4 qa[4];
    v4u xb[4];
#pragma unroll
    for (int ks = 0; ks < 4; ++ks) xb[ks] = *(LAS v4u*)(S + L_XN + (c & 1) * 1024 + (128 * w + 32 * ks + 8 * g) * 2);
#pragma unroll
    for (int t = 0; t < 4; ++t) qa[t] = f32x4{0.f, 0.f, 0.f, 0.f};
#pragma unroll
    for (int ks = 0; ks < 3; ++ks)
#pragma unroll
      for (int t = 0; t < 4; ++t) qa[t] = mfma32(fr[ks * 4 + t], xb[ks], qa[t]);
    VMWAIT(NINST(0) + NINST(1)); STAMP(8); QFRAGS(3);
#pragma unroll
    for (int t = 0; t < 4; ++t) qa[t] = mfma32(fr[12 + t], xb[3], qa[t]);
    if (c < 2) {
#pragma unroll
      for (int t = 0; t < 4; ++t) *(LAS f32x4*)(S + L_RED + w * 512 + c * 256 + t * 64 + g * 16) = qa[t];
    }
    STAMP(1);
    barrier();
  }
  const int rem = nstep & 3, remk = (nstep - NPRE - NLT) & 3;
#pragma unroll
  for (int d = 2; d < NPRE; ++d) { VMWAIT(NINST(d - 1)); ISSUEK(d, d); }
  {
    const u16* kt = (const u16*)(ka[1] + pbase + sbase + NPRE * 16384u);
    VMWAIT(NINST(NPRE - 1));
#pragma unroll 1
    for (int t = 0; t < NLT; t += 2) {
#pragma unroll
      for (int s = 0; s < 4; ++s) glds16(kt + (c * 256 + g * 16 + s * 64) / 2, lds0 + L_W + w * WREG + t * 4096 + s * 1024);
      VMWAIT(4);
#pragma unroll
      for (int s = 0; s < 4; ++s) glds16(kt + (16384 + koff + s * 256) / 2, lds0 + L_W + w * WREG + t * 4096 + 4096 + s * 1024);
      if (t + 2 < NLT) VMWAIT(4);
      kt += 16384;
    }
  }
  STAMP(10);
  barrier();
  STAMP(2);
  const v4u qf = *(LAS v4u*)(L + L_QF + (c >> 2) * 256 + g * 64 + (c & 3) * 16);
  tag = __builtin_amdgcn_readfirstlane(*(LAS unsigned*)(L + L_TAG));

  LAS unsigned char* scp = L + L_SC + w * 5120 + g * 32 + (c >> 2) * 8;
  float m = -INFINITY;
  f32x4 sc;
#define QK(d) do { \
    _Pragma("unroll") for (int s = 0; s < 4; ++s) { const f32x4 dd = mfma32(kb[d][s], qf, f32x4{0.f, 0.f, 0.f, 0.f}); sc[s] = (c & 2) ? ((c & 1) ? dd[3] : dd[2]) : ((c & 1) ? dd[1] : dd[0]); } \
    asm volatile("" : "+v"(sc) :: "memory"); SB(); } while (0)
#define SCORE(d, j) do { \
    _Pragma("unroll") for (int s = 0; s < 4; ++s) { float t_ = sc[s]; t_ += dpp<XOR1>(t_); t_ += dpp<XOR2>(t_); sc[s] = t_; } \
    const unsigned r01 = cvtpk(sc[0], sc[1]), r23 = cvtpk(sc[2], sc[3]); \
    const unsigned t01 = cvtpk(bflo(r01) * SCALE, bfhi(r01) * SCALE), t23 = cvtpk(bflo(r23) * SCALE, bfhi(r23) * SCALE); \
    m = vmax(vmax(m, vmax(bflo(t01), bfhi(t01))), vmax(bflo(t23), bfhi(t23))); \
    *(LAS v2u*)(scp + (j) * 128) = v2u{t01, t23}; asm volatile("" : "+v"(m)); SB(); } while (0)
#pragma unroll
  for (int d = 0; d < 4; ++d) { QK(d); ISSUEK(d, NPRE + NLT + remk + d); SCORE(d, d); }
#pragma unroll
  for (int d = 4; d < NPRE; ++d) { QK(d); SCORE(d, d); }
#pragma unroll
  for (int d = NPRE; d < NPRE + 3; ++d) if (d - NPRE < remk) ISSUEK(d, d + NLT);
  int j = NPRE + NLT + remk;
#pragma unroll 1
  do {
#pragma unroll
    for (int d = 0; d < 4; ++d) { QK(d); ISSUEK(d, j + 4 + d); SCORE(d, j + d); }
    j += 4;
  } while (j + 4 < nstep);
#pragma unroll
  for (int d = NPRE; d < NPRE + 3; ++d) if (d - NPRE < remk) { QK(d); SCORE(d, d + NLT); }
  const int voff = g * 1024 + c * 16;
  v2u vl[7][4], vh[7][4];
#define ISSUEV(d, j) do { _Pragma("unroll") for (int s = 0; s < 4; ++s) { \
      if (!HEAVY(d, s)) { const v4u t_ = __builtin_amdgcn_raw_buffer_load_b128(rv, voff + s * 256, sbase + (unsigned)(j) * 16384u, AUX_NT); vl[d][s] = v2u{t_[0], t_[1]}; vh[d][s] = v2u{t_[2], t_[3]}; } \
      else { vl[d][s] = __builtin_amdgcn_raw_buffer_load_b64(rv, voff + s * 256, sbase + (unsigned)(j) * 16384u, AUX_NT); NOHOIST(); SB(); \
             vh[d][s] = __builtin_amdgcn_raw_buffer_load_b64(rv, voff + s * 256 + 8, sbase + (unsigned)(j) * 16384u, AUX_NT); NOHOIST(); } } SB(); } while (0)
#pragma unroll
  for (int d = 0; d < 4; ++d) { QK(d); ISSUEV(d, d); SCORE(d, j + d); }
#pragma unroll
  for (int d = 4; d < 7; ++d) if (d - 4 < rem) ISSUEV(d, d);
  {
    const int he = (g * 64 + (c & 3) * 16 + (c >> 2) * 4) * 16;
#pragma unroll 1
    for (int t = 0; t < NLT; t += 2) {
#pragma unroll
      for (int s = 0; s < 4; ++s) kb[4][s] = *(LAS v4u*)(L + L_W + w * WREG + t * 4096 + he + s * 16);
#pragma unroll
      for (int s = 0; s < 4; ++s) kb[5][s] = *(LAS v4u*)(L + L_W + w * WREG + t * 4096 + 4096 + s * 1024 + lane * 16);
      QK(4); SCORE(4, NPRE + t); QK(5); SCORE(5, NPRE + t + 1);
    }
  }
  STAMP(9);
  m = sw16_max(m, m); m = sw32_max(m, m);
  const float mlog = m * LOG2E;
  const unsigned m0 = (c & 3) == 0 ? 0xffffu : (c & 3) == 1 ? 0xffff0000u : 0u;
  const unsigned m1 = (c & 3) == 2 ? 0xffffu : (c & 3) == 3 ? 0xffff0000u : 0u;
  f32x4 acc0 = {0.f, 0.f, 0.f, 0.f}, acc1 = {0.f, 0.f, 0.f, 0.f};
  float l = 0.f;
#define PCALC(bb, j) do { \
    const v2u t_ = *(LAS v2u*)(scp + (j) * 128); \
    const float sv[4] = {bflo(t_[0]), bfhi(t_[0]), bflo(t_[1]), bfhi(t_[1])}; \
    _Pragma("unroll") for (int i = 0; i < 4; ++i) { const float p_ = __builtin_amdgcn_exp2f(fmaf(sv[i], LOG2E, -mlog)); const unsigned pp = cvtpk(p_, p_); l += bflo(pp); bb[i] = v2u{pp & m0, pp & m1}; } } while (0)
#define PMFMA(d, bb) do { SB(); \
    _Pragma("unroll") for (int i = 0; i < 4; ++i) { acc0 = mfma16(vl[d][i], bb[i], acc0); acc1 = mfma16(vh[d][i], bb[i], acc1); } \
    asm volatile("" : "+v"(acc0), "+v"(acc1) :: "memory"); SB(); } while (0)
#define PV(d, j) do { v2u bb_[4]; PCALC(bb_, j); PMFMA(d, bb_); } while (0)
#pragma unroll
  for (int d = 0; d < 4; ++d) { PV(d, d); ISSUEV(d, 4 + rem + d); }
#pragma unroll
  for (int d = 4; d < 7; ++d) if (d - 4 < rem) PV(d, d);
  j = 4 + rem;
#pragma unroll 1
  do {
#pragma unroll
    for (int d = 0; d < 4; ++d) { PV(d, j + d); ISSUEV(d, j + 4 + d); }
    j += 4;
  } while (j + 4 + 4 * NLR < nstep);
  unsigned bal[2] = {0u, 0u};
  const int blo = (lane & 31) * 128;
#pragma unroll
  for (int r = 0; r < NLR; ++r) {
#pragma unroll
    for (int d = 0; d < 4; ++d) {
      if (r > 0 && !(d & 1)) asm volatile("" :: "v"(bal[d >> 1]));
      PV(d, j + d);
#pragma unroll
      for (int s = 0; s < 4; ++s) { const v4u t_ = __builtin_amdgcn_raw_buffer_load_b128(rv, voff + s * 256, sbase + (unsigned)(j + 4 + d) * 16384u, AUX_NT); vl[d][s] = v2u{t_[0], t_[1]}; vh[d][s] = v2u{t_[2], t_[3]}; }
      SB();
      if (!(d & 1)) {
        const bool late = (int)((unsigned)__builtin_amdgcn_s_memrealtime() - (tstart + T_L)) > 0;
        bal[d >> 1] = __builtin_amdgcn_raw_buffer_load_b32(rv, late ? 0 : blo, sbase + (unsigned)(j + 4 + d) * 16384u, AUX_NT); SB();
      }
    }
    j += 4;
  }
  asm volatile("v_mbcnt_lo_u32_b32 %0, -1, 0\n\tv_mbcnt_hi_u32_b32 %0, -1, %0" : "=v"(lane));
  c = lane & 15; g = lane >> 4; q4 = (lane >> 2) & 3; p4 = lane & 3;
  const int tid = w * 64 + lane;
#define PIN(x) asm volatile("" : "+v"(x))
  const unsigned tagh = mixtag(tag);
  v2u lb[4][4];
#pragma unroll
  for (int d = 0; d < 4; ++d) PCALC(lb[d], j + d);
  l = sw16_sum(l, l); l = sw32_sum(l, l);
  *(LAS f32x2*)(S + L_RED + (c >> 2) * 32 + w * 8) = f32x2{mlog, l};
  barrier();
  const int hd = 2 * (w & 1) + (lane >> 5);
  float fw; unsigned t1w, t3w;
  {
#define MERGE_ML(h, M_, L_) do { const f32x4 u0_ = *(LAS f32x4*)(S + L_RED + (h) * 32), u1_ = *(LAS f32x4*)(S + L_RED + (h) * 32 + 16); \
      M_ = vmax(vmax(u0_[0], u0_[2]), vmax(u1_[0], u1_[2])); \
      L_ = fmaf(__builtin_amdgcn_exp2f(u0_[2] - M_), u0_[3], __builtin_amdgcn_exp2f(u0_[0] - M_) * u0_[1]) + fmaf(__builtin_amdgcn_exp2f(u1_[2] - M_), u1_[3], __builtin_amdgcn_exp2f(u1_[0] - M_) * u1_[1]); } while (0)
    float M1, L1, M2, L2;
    MERGE_ML(c >> 2, M1, L1); MERGE_ML(hd, M2, L2);
    fw = __builtin_amdgcn_exp2f(mlog - M1) * __builtin_amdgcn_rcpf(L1);
    const unsigned lse = f2u(M2 + __builtin_amdgcn_logf(L2));
    t1w = __builtin_amdgcn_perm(tagh, lse, 0x05040100u); t3w = __builtin_amdgcn_perm(tagh, lse, 0x07060302u);
  }
  unsigned mw = lds0 + L_S + L_MRG + w * 2048 + lane * 16, mr = lds0 + L_S + L_MRG + ((lane >> 2) & 1) * 1024 + (((lane >> 3) & 3) * 16 + hd * 4 + (lane & 3)) * 16;
  int pao = OFF_PART + ((b * HQ + kvh * 4 + hd) * NSPLIT + sp) * 512 + (lane & 31) * 16;
  PIN(mw); PIN(mr); PIN(pao); PIN(fw); PIN(t1w); PIN(t3w);
#pragma unroll
  for (int d = 0; d < 4; ++d) { PMFMA(d, lb[d]); if (d == 2) asm volatile("" :: "v"(bal[0]), "v"(bal[1])); }
  STAMP(3);
  *(LAS f32x4*)mw = acc0 * fw; *(LAS f32x4*)(mw + 1024) = acc1 * fw;
  barrier();
  if (w < 2) {
    const f32x4 o = (*(LAS f32x4*)mr + *(LAS f32x4*)(mr + 2048)) + (*(LAS f32x4*)(mr + 4096) + *(LAS f32x4*)(mr + 6144));
    ST128((v4u{cvtpk(o[0], o[1]), t1w, cvtpk(o[2], o[3]), t3w}), pao);
  }
  STAMP(4);
  const int ci = CI();
  WUPTO(role == 1 ? 24 : role == 2 ? 16 : 0, 40);
  unsigned dm0 = (c & 7) == 0 ? 0xffffu : (c & 7) == 1 ? 0xffff0000u : 0u, dm1 = (c & 7) == 2 ? 0xffffu : (c & 7) == 3 ? 0xffff0000u : 0u,
           dm2 = (c & 7) == 4 ? 0xffffu : (c & 7) == 5 ? 0xffff0000u : 0u, dm3 = (c & 7) == 6 ? 0xffffu : (c & 7) == 7 ? 0xffff0000u : 0u;
  unsigned tb0 = tagh & 0xffu, tb1 = (tagh >> 8) & 0xffu, tb2 = (tagh >> 16) & 0xffu, tb3 = tagh >> 24;
#define PUBLISH_ACC(acc, off) ST128((v4u{PK24(acc[0], tb0), PK24(acc[1], tb1), PK24(acc[2], tb2), PK24(acc[3], tb3)}), (off))

  if (role == 2) {
    unsigned wra = lds0 + L_W + w * WREG + lane * 16;
    PIN(wra);
    const int head = bi >> 1;
    const int po = OFF_PART + (((lane >> 5) * HQ + head) * NSPLIT + ((lane >> 2) & 7)) * 512 + (bi & 1) * 256 + w * 64 + (lane & 3) * 16;
    unsigned ow = lds0 + L_S + L_OB + ((g >> 1) * 4 + (lane & 3)) * 64 + (8 * (w >> 1) + 2 * ((g & 1) + ((lane & 4) >> 1)) + (w & 1)) * 4, orr = lds0 + L_S + L_OB + ((c >> 3) * 4 + g) * 64;
    int so_ = OFF_P1 + bi * 4096 + tid * 16;
    f32x4 acc = {0.f, 0.f, 0.f, 0.f};
    PIN(ow); PIN(orr); PIN(so_); PIN(dm0); PIN(dm1); PIN(dm2); PIN(dm3); PIN(tb0); PIN(tb1); PIN(tb2); PIN(tb3); PIN(acc);
    v4u a0, a1;
    poll2<PD1, 16>(ws, po, po + 8 * 512, tagh, a0, a1);
    STAMP(5);
    v4u wr[16];
#pragma unroll
    for (int i = 0; i < 16; ++i) wr[i] = *(LAS v4u*)(wra + i * 1024);
    SB();
    const float l0 = u2f(__builtin_amdgcn_perm(a0[3], a0[1], 0x05040100u)), l1 = u2f(__builtin_amdgcn_perm(a1[3], a1[1], 0x05040100u));
    float xm = vmax(l0, l1);
    xm = vmax(vmax(xm, dpp<ROR4>(xm)), vmax(dpp<ROR8>(xm), dpp<ROR12>(xm)));
    xm = sw16_max(xm, xm);
    const float w0 = __builtin_amdgcn_exp2f(l0 - xm), w1 = __builtin_amdgcn_exp2f(l1 - xm);
    float u0 = sw16_sum(fmaf(w1, bflo(a1[0]), w0 * bflo(a0[0])), fmaf(w1, bfhi(a1[0]), w0 * bfhi(a0[0])));
    float u1 = sw16_sum(fmaf(w1, bflo(a1[2]), w0 * bflo(a0[2])), fmaf(w1, bfhi(a1[2]), w0 * bfhi(a0[2])));
    float ws_ = w0 + w1;
    ws_ = sw16_sum(ws_, ws_);
    u0 += dpp<ROR8>(u0); u1 += dpp<ROR8>(u1); ws_ += dpp<ROR8>(ws_);
    u0 += dpp<ROR4>(u0); u1 += dpp<ROR4>(u1); ws_ += dpp<ROR4>(ws_);
    const float uo = ((lane & 4) ? u1 : u0) * __builtin_amdgcn_rcpf(ws_);
    *(LAS unsigned*)ow = cvtpk(uo, uo);
    barrier();
    const v4u pv0 = *(LAS v4u*)orr, pv1 = *(LAS v4u*)(orr + 16), pv2 = *(LAS v4u*)(orr + 32), pv3 = *(LAS v4u*)(orr + 48);
    diag_chain_regs(acc, wr, pv0, pv1, dm0, dm1, dm2, dm3);
    diag_chain_regs(acc, wr + 8, pv2, pv3, dm0, dm1, dm2, dm3);
    mfma_drain(acc);
    PUBLISH_ACC(acc, so_);
    STAMP(6);
    poll_done(a0, a1);
  } else if (role == 3) {
    const int rb = rj & 1, col0 = (rj >> 1) * 32;
    unsigned xp = *(const GAS unsigned*)(x + rb * D + col0 + 2 * c);
    asm volatile("" : "+v"(xp));
    const int pp = lane >> 3;
    float h0, h1;
    unsigned rw_ = lds0 + L_S + L_RR + w * 128 + (8 * g + (lane & 7)) * 4, rr_ = lds0 + L_S + L_RR + c * 8;
    int hbo = OFF_HB + ((w & 1) * 4 + g) * 4096 + rb * 2048 + col0 * 4 + c * 8;
    GAS unsigned* op = (GAS unsigned*)(out + rb * D + col0 + 2 * c);
    PIN(rw_); PIN(rr_); PIN(hbo); PIN(op);
    {
      const int po = OFF_P1 + (16 * w + pp) * 4096 + rj * 128 + (lane & 7) * 16;
      v4u a0, a1;
      poll2<PD2, -1>(ws, po, po + 8 * 4096, tagh, a0, a1);
      STAMP(5);
      float u = sw16_sum(sw32_sum(UP24(a0[0]) + UP24(a1[0]), UP24(a0[2]) + UP24(a1[2])), sw32_sum(UP24(a0[1]) + UP24(a1[1]), UP24(a0[3]) + UP24(a1[3])));
      u += dpp<ROR8>(u);
      *(LAS float*)rw_ = u;
      barrier();
      const f32x2 s = (*(LAS f32x2*)rr_ + *(LAS f32x2*)(rr_ + 128)) + (*(LAS f32x2*)(rr_ + 256) + *(LAS f32x2*)(rr_ + 384));
      const unsigned at = cvtpk(s[0], s[1]);
      const unsigned hp = cvtpk(bflo(xp) + bflo(at), bfhi(xp) + bfhi(at));
      h0 = bflo(hp); h1 = bfhi(hp);
      if (w < 2) __builtin_amdgcn_raw_buffer_store_b64(v2u{hp, tag}, rw, hbo, 0, AUX_SC1);
      STAMP(6);
      poll_done(a0, a1);
    }
    {
      const int po = OFF_P2 + (14 * w + pp) * 4096 + rj * 128 + (lane & 7) * 16;
      v4u a0, b1;
      poll2<PD4, -1>(ws, po, po + (pp < 6 ? 8 * 4096 : 0), tagh, a0, b1);
      STAMP(7);
      const unsigned km = pp < 6 ? 0xffffff00u : 0u;
      float u = sw16_sum(sw32_sum(UP24(a0[0]) + u2f(b1[0] & km), UP24(a0[2]) + u2f(b1[2] & km)), sw32_sum(UP24(a0[1]) + u2f(b1[1] & km), UP24(a0[3]) + u2f(b1[3] & km)));
      u += dpp<ROR8>(u);
      *(LAS float*)(rw_ + 512) = u;
      barrier();
      if (tid < 16) {
        const f32x2 s = (*(LAS f32x2*)(rr_ + 512) + *(LAS f32x2*)(rr_ + 640)) + (*(LAS f32x2*)(rr_ + 768) + *(LAS f32x2*)(rr_ + 896));
        const unsigned dn = cvtpk(s[0], s[1]);
        *op = cvtpk(h0 + bflo(dn), h1 + bfhi(dn));
        if (rj == 0 && tid == 0) __builtin_amdgcn_raw_buffer_store_b32(tag, rw, OFF_EPOCH, 0, AUX_SC1);
      }
      STAMP(8);
      poll_done(a0, b1);
    }
  } else if (role == 1) {
    v4u fg[8], fu[8], wd[8];
    LAS unsigned char* fa = L + L_W + w * WREG + (8 * g + q4) * 64 + (p4 & 1) * 8;
    VMWAIT(16);
#pragma unroll
    for (int ks = 0; ks < 4; ++ks)
#pragma unroll
      for (int t = 0; t < 2; ++t) { LAS unsigned char* a = fa + 2048 * ks + (((2 * t + (p4 >> 1)) ^ ((g & 1) << 1)) * 16); fg[ks * 2 + t] = tr_frag(a, a + 256); }
    VMWAIT(8);
    STAMP(10);
#pragma unroll
    for (int ks = 0; ks < 4; ++ks)
#pragma unroll
      for (int t = 0; t < 2; ++t) { LAS unsigned char* a = fa + 8192 + 2048 * ks + (((2 * t + (p4 >> 1)) ^ ((g & 1) << 1)) * 16); fu[ks * 2 + t] = tr_frag(a, a + 256); }
    f32x4 ag[2], au[2];
    unsigned ro = lds0 + L_S + L_RED + ((c >> 2) & 1) * 1024 + ((c & 1) * 32 + (c & 2) * 8 + 4 * g) * 16 + w * 4;
    unsigned fo = lds0 + L_S + L_RED + ((c >> 3) * 32 + 4 * (c & 3) + g) * 16;
#define GU_MFMA(XB) do { _Pragma("unroll") for (int t = 0; t < 2; ++t) { ag[t] = f32x4{0.f, 0.f, 0.f, 0.f}; au[t] = f32x4{0.f, 0.f, 0.f, 0.f}; } \
      _Pragma("unroll") for (int ks = 0; ks < 4; ++ks) { \
        _Pragma("unroll") for (int t = 0; t < 2; ++t) { ag[t] = mfma32(fg[ks * 2 + t], XB, ag[t]); au[t] = mfma32(fu[ks * 2 + t], XB, au[t]); } } } while (0)
#define GU_STORE() do { const f32x4 sel = (c & 4) ? ((c & 2) ? au[1] : au[0]) : ((c & 2) ? ag[1] : ag[0]); \
      _Pragma("unroll") for (int i = 0; i < 4; ++i) *(LAS float*)(ro + i * 16) = sel[i]; } while (0)
#define SUM4(p_) ({ const f32x2 t_ = f32x2{p_[0], p_[1]} + f32x2{p_[2], p_[3]}; float r_ = t_[0] + t_[1]; PIN(r_); r_; })
    GU_MFMA((v4u{0x3f803f80u, 0x3f803f80u, 0x3f803f80u, 0x3f803f80u}));
    GU_STORE();
    barrier();
    float cg0, cg1, cu0, cu1;
    { const f32x4 p0 = *(LAS f32x4*)fo, p1 = *(LAS f32x4*)(fo + 256), p2 = *(LAS f32x4*)(fo + 1024), p3 = *(LAS f32x4*)(fo + 1280); cg0 = SUM4(p0); cg1 = SUM4(p1); cu0 = SUM4(p2); cu1 = SUM4(p3); }
    unsigned xw = lds0 + L_S + L_XN + (w & 1) * 1024 + lane * 16, xr = lds0 + L_S + L_XN + (c & 1) * 1024 + (128 * w + 8 * g) * 2, sw_ = lds0 + L_S + L_ST + w * 8, sr = lds0 + L_S + L_ST + (c >> 3) * 8;
    int so_ = OFF_P2 + ci * 4096 + tid * 16;
    f32x4 acc = {0.f, 0.f, 0.f, 0.f};
    unsigned wda = lds0 + L_W + w * WREG + 16384 + lane * 16;
    PIN(wda); PIN(ro); PIN(fo); PIN(xw); PIN(xr); PIN(sw_); PIN(sr); PIN(so_); PIN(dm0); PIN(dm1); PIN(dm2); PIN(dm3); PIN(tb0); PIN(tb1); PIN(tb2); PIN(tb3); PIN(acc);
    v4u ha = {0u, 0u, 0u, 0u}, hb = ha;
    if (w < 2) {
      const int ho = OFF_HB + xcd * 4096 + w * 2048 + lane * 32;
      poll2<PD3, 0>(ws, ho, ho + 16, tag, ha, hb);
      STAMP(5);
      *(LAS v4u*)xw = v4u{ha[0], ha[2], hb[0], hb[2]};
    } else VMWAIT(0);
    barrier();
    v4u xb[4];
#pragma unroll
    for (int ks = 0; ks < 4; ++ks) xb[ks] = *(LAS v4u*)(xr + 64 * ks);
#pragma unroll
    for (int i = 0; i < 8; ++i) wd[i] = *(LAS v4u*)(wda + i * 1024);
    __builtin_amdgcn_sched_barrier(0);
    const float pv = bflo(__builtin_amdgcn_readfirstlane(ha[0]));
    const float d0 = bflo(ha[0]) - pv, d1 = bfhi(ha[0]) - pv, d2 = bflo(ha[2]) - pv, d3 = bfhi(ha[2]) - pv, d4 = bflo(hb[0]) - pv, d5 = bfhi(hb[0]) - pv, d6 = bflo(hb[2]) - pv, d7 = bfhi(hb[2]) - pv;
    float s1 = ((d0 + d1) + (d2 + d3)) + ((d4 + d5) + (d6 + d7));
    float s2 = (fmaf(d1, d1, d0 * d0) + fmaf(d3, d3, d2 * d2)) + (fmaf(d5, d5, d4 * d4) + fmaf(d7, d7, d6 * d6));
    __builtin_amdgcn_sched_barrier(0);
    GU_MFMA(xb[ks]);
    {
      float t = sw32_sum(s1, s2);
      t = sw16_sum(t, t); t += dpp<ROR8>(t); t += dpp<ROR4>(t); t += dpp<XOR2>(t); t += dpp<XOR1>(t);
      const float md = u2f(__builtin_amdgcn_readlane(f2u(t), 0)) * (1.0f / D), q2 = u2f(__builtin_amdgcn_readlane(f2u(t), 32)) * (1.0f / D);
      const float var = vmax(fmaf(-md, md, q2), 0.f);
      const float rs = __builtin_amdgcn_rcpf(rbf(__builtin_amdgcn_sqrtf(rbf(rbf(var) + 1e-5f))));
      *(LAS f32x2*)sw_ = f32x2{rbf(pv + md), rs};
    }
    GU_STORE();
    barrier();
    const f32x2 st = *(LAS f32x2*)sr;
    const f32x4 p0 = *(LAS f32x4*)fo, p1 = *(LAS f32x4*)(fo + 256), p2 = *(LAS f32x4*)(fo + 1024), p3 = *(LAS f32x4*)(fo + 1280);
    __builtin_amdgcn_sched_barrier(0);
    const float G0 = SUM4(p0), G1 = SUM4(p1), U0 = SUM4(p2), U1 = SUM4(p3);
    const unsigned gp = cvtpk(fmaf(-st[0], cg0, G0) * st[1], fmaf(-st[0], cg1, G1) * st[1]);
    const unsigned up = cvtpk(fmaf(-st[0], cu0, U0) * st[1], fmaf(-st[0], cu1, U1) * st[1]);
    const float g0 = bflo(gp), g1 = bfhi(gp);
    const unsigned sg = cvtpk(g0 * __builtin_amdgcn_rcpf(1.0f + __builtin_amdgcn_exp2f(-g0 * LOG2E)), g1 * __builtin_amdgcn_rcpf(1.0f + __builtin_amdgcn_exp2f(-g1 * LOG2E)));
    const float av0 = bflo(sg) * bflo(up), av1 = bfhi(sg) * bfhi(up);
    diag_chain_quad(acc, wd, cvtpk(av0, av0), cvtpk(av1, av1), dm0, dm1, dm2, dm3);
    mfma_drain(acc);
    PUBLISH_ACC(acc, so_);
    STAMP(6);
    poll_done(ha, hb);
  }
  WAIT_UNTIL(5500, 16);
  if (tid == 0) __builtin_amdgcn_raw_buffer_store_b64(v2u{tstart, tval}, rw, 0x2000 + wg * 8, 0, AUX_SC1);
  if (role == 3 && rj == 0 && w == 0) {
    WAIT_UNTIL(6200, 16);
    int mn = 0x7fffffff, sm = 0;
    for (int k = 0; k < 2; ++k) { const v4u ts = LD128(0x2000 + k * 1024 + lane * 16);
      for (int i = 0; i < 4; i += 2) { const int d = (int)(ts[i] - tstart), e = (int)(ts[i + 1] - ts[i]); mn = d < mn ? d : mn; sm += e; } }
    for (int o = 32; o >= 1; o >>= 1) { const int a = __shfl_xor(mn, o); mn = a < mn ? a : mn; sm += __shfl_xor(sm, o); }
    mn = __builtin_amdgcn_readfirstlane(mn); sm = __builtin_amdgcn_readfirstlane(sm);
    int dl = sm * 8 / 256; dl = dl < 0 ? 0 : dl > 6000 ? 6000 : dl;
    WAIT_UNTIL(7000 + mn + dl, 1);
  }
}

extern "C" void kernel_launch(void* const* d_in, const int* in_sizes, int n_in, void* d_out, int out_size, void* d_ws, size_t ws_size, hipStream_t stream) {
  fused_block<<<NWG, NTHR, 0, stream>>>((const u16*)d_in[0], (const u16*)d_in[1], (const u16*)d_in[2], (const u16*)d_in[3], (unsigned char*)d_ws,
                                        (const u16*)d_in[4], (const u16*)d_in[6], (const u16*)d_in[7], (const u16*)d_in[8], (u16*)d_out, make_plan() TUNE_ARG);
}
```

```cpp
#pragma clang diagnostic ignored "-Wint-to-pointer-cast"
#include <hip/hip_runtime.h>
#include <hip/hip_bf16.h>
#include <cstdint>

typedef unsigned short u16;
typedef unsigned v4u __attribute__((ext_vector_type(4)));
typedef unsigned v2u __attribute__((ext_vector_type(2)));
typedef float f32x4 __attribute__((ext_vector_type(4)));
typedef float f32x2 __attribute__((ext_vector_type(2)));
typedef short s16x4 __attribute__((ext_vector_type(4)));
typedef __bf16 bf16x8_t __attribute__((ext_vector_type(8)));
typedef __bf16 bf16x2_t __attribute__((ext_vector_type(2)));
#define LAS __attribute__((address_space(3)))
#define GAS __attribute__((address_space(1)))

constexpr int D = 512, HQ = 32, HKV = 8, SK = 32768, DH = 128, F = 1792;
constexpr int NWG = 256, NTHR = 320, NSPLIT = 16;
struct Plan { unsigned e[32]; };
#define TUNE_PARAM
#define TUNE_ARG
#define ABL 0
#define TL_C 130
#define TL_CS 129
#define TL_B 128
#define TL_Q 124
#define TL_QS 120
#define T_WC 0
#define T_WB 0
#define T_L 3500
#define T_PD 150
#define T_P 680
static Plan make_plan() {
  Plan p;
  for (int b = 0; b < 2; ++b) {
    const int nc = 4 - b;
    int n[16], role[16], ix[16], used = 0, nn = 0;
    for (int sp = 0; sp < 16; ++sp) {
      if (sp < nc) { role[sp] = 1; ix[sp] = 2 * sp + b; n[sp] = sp == 0 ? TL_CS : TL_C; }
      else if (sp >= 4 && sp < 8) { role[sp] = 2; ix[sp] = 2 * (sp - 4) + b; n[sp] = TL_B; }
      else if (sp >= 12) { role[sp] = 4; ix[sp] = 2 * (sp - 12) + b; n[sp] = ix[sp] == 3 ? TL_QS : TL_Q; }
      else { role[sp] = (sp == 8 || sp == 9) ? 3 : 0; ix[sp] = role[sp] ? 2 * (sp - 8) + b : 0; n[sp] = 0; ++nn; }
      used += n[sp];
    }
    int rest = 2048 - used, t0 = 0;
    for (int sp = 0; sp < 16; ++sp) {
      if (n[sp] == 0) { n[sp] = (rest + nn - 1) / nn; rest -= n[sp]; --nn; }
      p.e[2 * sp + b] = (unsigned)t0 | ((unsigned)n[sp] << 11) | ((unsigned)role[sp] << 19) | ((unsigned)ix[sp] << 22);
      t0 += n[sp];
    }
  }
  return p;
}
#define HMASK 0x0F0F
#define HEAVY(d, s) ((HMASK >> (4 * ((d) & 3) + (s))) & 1)
#define NINST(d) (4 + __builtin_popcount((HMASK >> (4 * ((d) & 3))) & 15))
#define NPRE 12
#define TOUCH_EARLY 1
#define NLR 2
#define PD1 2
#define PD2 3
#define PD3 1
#define PD4 3
#define PSL 1
#define PACE_N 4
#define CODE_BYTES 25600
constexpr int NB = 64, NC = 56, NR = 64, NREP = 8;
constexpr int OFF_EPOCH = 0;
constexpr int OFF_HB = 0x190000;
constexpr int OFF_PART = 0x10000;
constexpr int OFF_P1 = 0x90000;
constexpr int OFF_P2 = 0xD0000;
constexpr int OFF_Q = 0x180000;
constexpr int WS_BYTES = 0x1A0000, OFF_DBG = 0x200000;
constexpr int AUX_NT = 2, AUX_SC1 = 16;
constexpr unsigned SPIN_CAP = 1u << 16;
constexpr int L_W = 0;
constexpr int WREG = 24576;
constexpr int L_QF = 4 * WREG;
constexpr int L_SC = L_QF + 1024;
constexpr int L_TAG = L_SC + 4 * 40 * 128;
constexpr int L_S = L_TAG + 16;
constexpr int L_XN = 0;
constexpr int L_RED = L_XN + 2048;
constexpr int L_MRG = L_RED + 2048;
constexpr int L_OB = L_MRG + 4 * 4 * 132 * 4;
constexpr int L_RR = L_OB + 512;
constexpr int L_ST = L_RR + 1024;
constexpr int S_BYTES = L_ST + 32;
constexpr int L_PF = L_S + S_BYTES;
constexpr int L_DBG = L_PF + 256;
constexpr int L_BYTES = L_DBG + 128;

__device__ __forceinline__ float bflo(unsigned v) { return __uint_as_float(v << 16); }
__device__ __forceinline__ float bfhi(unsigned v) { return __uint_as_float(v & 0xffff0000u); }
__device__ __forceinline__ unsigned cvtpk(float lo, float hi) { return __builtin_bit_cast(unsigned, __builtin_convertvector(f32x2{lo, hi}, bf16x2_t)); }
__device__ __forceinline__ float rbf(float x) { return bflo(cvtpk(x, x)); }
__device__ __forceinline__ bool tagged(v4u v, unsigned tag) { return (v[1] == tag) & (v[3] == tag); }
__device__ __forceinline__ float u2f(unsigned v) { return __uint_as_float(v); }
__device__ __forceinline__ unsigned f2u(float v) { return __float_as_uint(v); }

__device__ __forceinline__ float vmax(float a, float b) { return __builtin_elementwise_maximum(a, b); }
template <int CTRL> __device__ __forceinline__ float dpp(float x) { return __builtin_bit_cast(float, __builtin_amdgcn_mov_dpp(__builtin_bit_cast(int, x), CTRL, 0xf, 0xf, true)); }
template <int CTRL> __device__ __forceinline__ unsigned dppu(unsigned x) { return (unsigned)__builtin_amdgcn_mov_dpp((int)x, CTRL, 0xf, 0xf, true); }
constexpr int XOR1 = 0xB1, XOR2 = 0x4E, ROR8 = 0x128, ROR4 = 0x124, ROR12 = 0x12C;
__device__ __forceinline__ float sw32_sum(float a, float b) { auto r = __builtin_amdgcn_permlane32_swap(f2u(a), f2u(b), false, false); return u2f(r[0]) + u2f(r[1]); }
__device__ __forceinline__ float sw16_sum(float a, float b) { auto r = __builtin_amdgcn_permlane16_swap(f2u(a), f2u(b), false, false); return u2f(r[0]) + u2f(r[1]); }
__device__ __forceinline__ float sw32_max(float a, float b) { auto r = __builtin_amdgcn_permlane32_swap(f2u(a), f2u(b), false, false); return vmax(u2f(r[0]), u2f(r[1])); }
__device__ __forceinline__ float sw16_max(float a, float b) { auto r = __builtin_amdgcn_permlane16_swap(f2u(a), f2u(b), false, false); return vmax(u2f(r[0]), u2f(r[1])); }
__device__ __forceinline__ float wave_sum(float x) {
  x += dpp<XOR1>(x); x += dpp<XOR2>(x); x += dpp<ROR4>(x); x += dpp<ROR8>(x);
  x = sw16_sum(x, x); return sw32_sum(x, x);
}
__device__ __forceinline__ f32x4 mfma32(v4u a, v4u b, f32x4 c) { return __builtin_amdgcn_mfma_f32_16x16x32_bf16(__builtin_bit_cast(bf16x8_t, a), __builtin_bit_cast(bf16x8_t, b), c, 0, 0, 0); }
__device__ __forceinline__ f32x4 mfma16(v2u a, v2u b, f32x4 c) { return __builtin_amdgcn_mfma_f32_16x16x16bf16_1k(__builtin_bit_cast(s16x4, a), __builtin_bit_cast(s16x4, b), c, 0, 0, 0); }
__device__ __forceinline__ void barrier() { asm volatile("s_waitcnt lgkmcnt(0)\n\ts_barrier" ::: "memory"); }
__device__ __forceinline__ v4u tr_frag(LAS unsigned char* lo, LAS unsigned char* hi) {
  const v2u a = __builtin_bit_cast(v2u, __builtin_amdgcn_ds_read_tr16_b64_v4i16((LAS s16x4*)lo));
  const v2u b = __builtin_bit_cast(v2u, __builtin_amdgcn_ds_read_tr16_b64_v4i16((LAS s16x4*)hi));
  return v4u{a[0], a[1], b[0], b[1]};
}

#define AND4(T0, T1, T2, T3, SRC, MOD, SFX) "v_and_b32" SFX " " T0 ", " SRC ", %[m0]" MOD "\n\tv_and_b32" SFX " " T1 ", " SRC ", %[m1]" MOD "\n\tv_and_b32" SFX " " T2 ", " SRC ", %[m2]" MOD "\n\tv_and_b32" SFX " " T3 ", " SRC ", %[m3]" MOD "\n\t"
#define ANDA(SRC, MOD, SFX) AND4("v240", "v241", "v242", "v243", SRC, MOD, SFX)
#define ANDB(SRC, MOD, SFX) AND4("v244", "v245", "v246", "v247", SRC, MOD, SFX)
#define MFA(W) "v_mfma_f32_16x16x32_bf16 %[acc], " W ", v[240:243], %[acc]\n\t"
#define MFB(W) "v_mfma_f32_16x16x32_bf16 %[acc], " W ", v[244:247], %[acc]\n\t"
#define CHAIN_CLOB "v240", "v241", "v242", "v243", "v244", "v245", "v246", "v247"
#define CHAIN_W(W) [w0] "v"(W[0]), [w1] "v"(W[1]), [w2] "v"(W[2]), [w3] "v"(W[3]), [w4] "v"(W[4]), [w5] "v"(W[5]), [w6] "v"(W[6]), [w7] "v"(W[7])
__device__ __forceinline__ void diag_chain_regs(f32x4& acc, const v4u* W, v4u pa, v4u pb, unsigned m0, unsigned m1, unsigned m2, unsigned m3) {
  asm volatile(ANDA("%[p0]", "", "") ANDB("%[p1]", "", "") MFA("%[w0]") ANDA("%[p2]", "", "") MFB("%[w1]") ANDB("%[p3]", "", "") MFA("%[w2]") ANDA("%[p4]", "", "") MFB("%[w3]")
               ANDB("%[p5]", "", "") MFA("%[w4]") ANDA("%[p6]", "", "") MFB("%[w5]") ANDB("%[p7]", "", "") MFA("%[w6]") "s_nop 0\n\t" MFB("%[w7]")
               : [acc] "+v"(acc)
               : CHAIN_W(W), [p0] "v"(pa[0]), [p1] "v"(pa[1]), [p2] "v"(pa[2]), [p3] "v"(pa[3]), [p4] "v"(pb[0]), [p5] "v"(pb[1]), [p6] "v"(pb[2]), [p7] "v"(pb[3]),
                 [m0] "v"(m0), [m1] "v"(m1), [m2] "v"(m2), [m3] "v"(m3)
               : CHAIN_CLOB);
}
#define QP(t) " quad_perm:[" #t "," #t "," #t "," #t "] row_mask:0xf bank_mask:0xf"
__device__ __forceinline__ void diag_chain_quad(f32x4& acc, const v4u* W, unsigned pa, unsigned pb, unsigned m0, unsigned m1, unsigned m2, unsigned m3) {
  asm volatile("s_nop 1\n\t" ANDA("%[pa]", QP(0), "_dpp") ANDB("%[pa]", QP(1), "_dpp") MFA("%[w0]") ANDA("%[pa]", QP(2), "_dpp") MFB("%[w1]") ANDB("%[pa]", QP(3), "_dpp") MFA("%[w2]")
               ANDA("%[pb]", QP(0), "_dpp") MFB("%[w3]") ANDB("%[pb]", QP(1), "_dpp") MFA("%[w4]") ANDA("%[pb]", QP(2), "_dpp") MFB("%[w5]") ANDB("%[pb]", QP(3), "_dpp") MFA("%[w6]")
               "s_nop 0\n\t" MFB("%[w7]")
               : [acc] "+v"(acc)
               : CHAIN_W(W), [pa] "v"(pa), [pb] "v"(pb), [m0] "v"(m0), [m1] "v"(m1), [m2] "v"(m2), [m3] "v"(m3)
               : CHAIN_CLOB);
}
__device__ __forceinline__ void mfma_drain(f32x4& acc) { asm volatile("s_nop 7" : "+v"(acc)); }

__device__ __forceinline__ v4u ln_row(v4u xv) {
  float x[8];
#pragma unroll
  for (int i = 0; i < 4; ++i) { x[2 * i] = bflo(xv[i]); x[2 * i + 1] = bfhi(xv[i]); }
  const float mean = wave_sum(((x[0] + x[1]) + (x[2] + x[3])) + ((x[4] + x[5]) + (x[6] + x[7]))) * (1.0f / D);
  float vs = 0.f;
#pragma unroll
  for (int i = 0; i < 8; ++i) { const float d = x[i] - mean; vs = fmaf(d, d, vs); }
  const float var = wave_sum(vs) * (1.0f / D);
  const float mb = rbf(mean);
  const float rs = __builtin_amdgcn_rcpf(rbf(__builtin_amdgcn_sqrtf(rbf(rbf(var) + 1e-5f))));
  v4u r;
#pragma unroll
  for (int i = 0; i < 4; ++i) { const unsigned d = cvtpk(x[2 * i] - mb, x[2 * i + 1] - mb); r[i] = cvtpk(bflo(d) * rs, bfhi(d) * rs); }
  return r;
}
__device__ __forceinline__ void glds16(const u16* gsrc, unsigned lds_dst) {
  unsigned keep;
  asm volatile("s_mov_b32 %0, m0\n\ts_mov_b32 m0, %2\n\ts_nop 0\n\tglobal_load_lds_dwordx4 %1, off nt\n\ts_mov_b32 m0, %0" : "=&s"(keep) : "v"(gsrc), "s"(lds_dst) : "memory");
}

__device__ __forceinline__ unsigned mixtag(unsigned t) { t *= 0x9E3779B1u; return t ^ (t >> 15); }
#define PK24(x, tb) (((f2u(x) + 0x80u) & 0xffffff00u) | (tb))
#define UP24(x) u2f((x) & 0xffffff00u)
__device__ __forceinline__ unsigned lowbytes(v4u v) { return __builtin_amdgcn_perm(v[1], v[0], 0x0c0c0400u) | __builtin_amdgcn_perm(v[3], v[2], 0x04000c0cu); }
template <int PD_, int TSH>
__device__ __forceinline__ void poll2(const unsigned char* ws, int o0, int o1, unsigned tag, v4u& r0, v4u& r1) {
  r0 = v4u{~tag, ~tag, ~tag, ~tag}; r1 = r0;
#define PASS() asm volatile("global_load_dwordx4 %0, %2, %4 sc1\n\tglobal_load_dwordx4 %1, %3, %4 sc1" : "+v"(r0), "+v"(r1) : "v"(o0), "v"(o1), "s"(ws) : "memory")
#pragma unroll
  for (int k = 0; k < PD_ - 1; ++k) { PASS(); __builtin_amdgcn_s_sleep(PSL); }
#pragma unroll 1
  for (unsigned k = 0; k < SPIN_CAP; ++k) {
    PASS();
    asm volatile("s_waitcnt vmcnt(%2)" : "+v"(r0), "+v"(r1) : "i"((PD_ - 1) * 2) : "memory");
    if (TSH == 16) { if (__all((__builtin_amdgcn_perm(r0[3], r0[1], 0x07060302u) == tag) & (__builtin_amdgcn_perm(r1[3], r1[1], 0x07060302u) == tag))) break; }
    else if (TSH < 0) { if (__all((lowbytes(r0) == tag) & (lowbytes(r1) == tag))) break; }
    else if (__all((((r0[1] ^ tag) | (r0[3] ^ tag) | (r1[1] ^ tag) | (r1[3] ^ tag)) >> (TSH < 0 ? 0 : TSH)) == 0u)) break;
    if (PD_ > 1) __builtin_amdgcn_s_sleep(PSL);
  }
#undef PASS
  asm volatile("s_nop 3 ; POLL_END" : "+v"(r0), "+v"(r1));
}
__device__ __forceinline__ void poll_done(v4u& r0, v4u& r1) { asm volatile("s_waitcnt vmcnt(0)" : "+v"(r0), "+v"(r1) :: "memory"); }

#define STAMP(i) do { } while (0)
#define DUMP_STAMPS() do { } while (0)
#define LD128(off) __builtin_amdgcn_raw_buffer_load_b128(rw, (off), 0, AUX_SC1)
#define ST128(v, off) __builtin_amdgcn_raw_buffer_store_b128((v), rw, (off), 0, AUX_SC1)
#define SPIN_LOOP _Pragma("unroll 1") for (unsigned spins = 0; spins < SPIN_CAP; ++spins)
#define NOHOIST() asm volatile("" ::: "memory")

__global__ void __launch_bounds__(NTHR, 2) fused_block(const u16* x_, const u16* kc_, const u16* vc_, const u16* wq_, unsigned char* ws_,
                                                       const u16* wo_, const u16* wgate_, const u16* wup_, const u16* wdown_, u16* out_, Plan plan_ TUNE_PARAM) {
  typedef unsigned long long u64x8 __attribute__((ext_vector_type(8)));
  typedef unsigned long long u64x2 __attribute__((ext_vector_type(2)));
  u64x8 ka; u64x2 kb2; unsigned pe;
  asm volatile("s_load_dwordx16 %0, %3, 0x0\n\ts_load_dwordx4 %1, %3, 0x40\n\ts_load_dword %2, %3, %4 offset:0x50"
               : "=&s"(ka), "=&s"(kb2), "=&s"(pe) : "s"(__builtin_amdgcn_kernarg_segment_ptr()), "s"((blockIdx.x >> 3) * 4));
  __shared__ __attribute__((aligned(16))) unsigned char smem[L_BYTES];
  LAS unsigned char* L = (LAS unsigned char*)smem;
  STAMP(0);
  const unsigned tstart = (unsigned)__builtin_amdgcn_s_memrealtime();
  const int wg = blockIdx.x, xcd = wg & 7, idx = wg >> 3;
  const int kvh = xcd, b = idx & 1, sp = idx >> 1;
  int lane = threadIdx.x & 63;
  const int w = __builtin_amdgcn_readfirstlane(threadIdx.x >> 6);
  int c = lane & 15, g = lane >> 4;
  if (w == 4 && !(ABL & 32)) {
    unsigned long long pc;
    asm volatile("s_getpc_b64 %0" : "=s"(pc));
    if ((lane * 32 + idx) * 128 < CODE_BYTES) {
      unsigned keep;
      asm volatile("s_mov_b32 %0, m0\n\ts_mov_b32 m0, %3\n\ts_nop 0\n\tglobal_load_lds_dword %1, %2\n\ts_mov_b32 m0, %0"
                   : "=&s"(keep) : "v"((lane * 32 + idx) * 128), "s"(pc & ~255ull), "s"((unsigned)(uintptr_t)L + L_PF) : "memory");
    }
  }
  int koff = (c >> 2) * 1024 + g * 64 + (c & 3) * 16;
  unsigned pbase = (unsigned)(b * HKV + kvh) * (SK * DH * 2);
  asm volatile("s_waitcnt lgkmcnt(0)" : "+s"(ka), "+s"(kb2), "+s"(pe), "+v"(koff), "+s"(pbase));
  const u16 *x = (const u16*)ka[0], *wq = (const u16*)ka[3], *wo = (const u16*)ka[5], *wgate = (const u16*)ka[6], *wup = (const u16*)ka[7], *wdown = (const u16*)kb2[0];
  unsigned char* ws = (unsigned char*)ka[4];
  u16* out = (u16*)kb2[1];
  const int tile0 = pe & 2047, ntile = (pe >> 11) & 255;
  const __amdgpu_buffer_rsrc_t rk = __builtin_amdgcn_make_buffer_rsrc((void*)(ka[1] + pbase), 0, SK * DH * 2, 0x00020000);
  const unsigned sbase = (unsigned)(tile0 + w) * 4096u;
  v4u kb[NPRE + 1][4];
#define SB() __builtin_amdgcn_sched_barrier(0)
#define ISSUE(d, rs, off, so) do { _Pragma("unroll") for (int s = 0; s < 4; ++s) { \
      if (!HEAVY(d, s)) kb[d][s] = __builtin_amdgcn_raw_buffer_load_b128(rs, (off) + s * 256, (so), AUX_NT); \
      else { const v2u lo_ = __builtin_amdgcn_raw_buffer_load_b64(rs, (off) + s * 256, (so), AUX_NT); NOHOIST(); SB(); \
             const v2u hi_ = __builtin_amdgcn_raw_buffer_load_b64(rs, (off) + s * 256 + 8, (so), AUX_NT); NOHOIST(); \
             kb[d][s] = v4u{lo_[0], lo_[1], hi_[0], hi_[1]}; } } SB(); } while (0)
#define ISSUEK(d, j) ISSUE(d, rk, koff, sbase + (unsigned)(j) * 16384u)
  const int role = (pe >> 19) & 7, rix = pe >> 22;
  const bool qrole = role == 4;
#define CI() ({ int r_ = rix; asm volatile("" : "+s"(r_)); const int cn_ = xcd * 5 + r_ - (xcd < 7 ? 2 : 0); (xcd < 7 && r_ < 2) ? 8 * xcd + 6 + r_ : cn_ + 2 * ((cn_ * 43) >> 8); })
  const int bi = xcd * 8 + rix, rj = xcd * 4 + rix, qi = bi;
  const __amdgpu_buffer_rsrc_t rw = __builtin_amdgcn_make_buffer_rsrc((void*)ws, 0, WS_BYTES, 0x00020000);
  const unsigned lds0 = (unsigned)(uintptr_t)L;
  LAS unsigned char* S = L + L_S;
#define WAIT_UNTIL(t, slp) do { while ((int)((unsigned)__builtin_amdgcn_s_memrealtime() - (tstart + (unsigned)(t))) < 0) __builtin_amdgcn_s_sleep(slp); } while (0)
  int q4 = (lane >> 2) & 3, p4 = lane & 3;
  constexpr float SCALE = 0.088388347648318440f, LOG2E = 1.4426950408889634f;
  unsigned tag;

  if (w == 4) {
    __builtin_amdgcn_s_setprio(3);
#define TOUCH() \
    {     \
      const int ci = CI(); \
      const unsigned long long vb_ = ka[2] + pbase + (unsigned)tile0 * 4096u, ke_ = ka[1] + pbase + (unsigned)(tile0 + ntile) * 4096u - 128u; \
      const unsigned long long wa_ = role == 1 ? ka[6] + ci * 64 : role == 2 ? ka[5] + bi * 65536 : kb2[1]; \
      const unsigned long long ad = lane == 0 ? vb_ : lane == 1 ? vb_ + (unsigned)ntile * 4096u - 128u : lane == 2 ? ke_ : lane == 3 ? wa_ \
                                  : lane == 4 ? wa_ + 511 * F * 2 : lane == 5 ? ka[7] + ci * 64 : lane == 6 ? ka[7] + ci * 64 + 511 * F * 2 : kb2[0] + ci * 32768; \
      if (lane < (role == 1 ? 8 : 4)) { \
        unsigned keep; \
        asm volatile("s_mov_b32 %0, m0\n\ts_mov_b32 m0, %2\n\ts_nop 0\n\tglobal_load_lds_dword %1, off nt\n\ts_mov_b32 m0, %0" : "=&s"(keep) : "v"(ad), "s"(lds0 + L_PF) : "memory"); \
      } \
    }
    if (qrole) {
      const v4u x0 = *(const GAS v4u*)(x + lane * 8), x1 = *(const GAS v4u*)(x + D + lane * 8);
      tag = __builtin_amdgcn_raw_buffer_load_b32(rw, OFF_EPOCH, 0, AUX_SC1) + 1u;
      const v4u n0 = ln_row(x0), n1 = ln_row(x1);
      *(LAS v4u*)(S + L_XN + lane * 16) = n0; *(LAS v4u*)(S + L_XN + 1024 + lane * 16) = n1;
      STAMP(9);
      barrier();
      if (TOUCH_EARLY) TOUCH();
      barrier();
      const f32x2 s = (*(LAS f32x2*)(S + L_RED + lane * 8) + *(LAS f32x2*)(S + L_RED + 512 + lane * 8)) + (*(LAS f32x2*)(S + L_RED + 1024 + lane * 8) + *(LAS f32x2*)(S + L_RED + 1536 + lane * 8));
      __builtin_amdgcn_raw_buffer_store_b64(v2u{cvtpk(s[0], s[1]), tag}, rw, OFF_Q + ((lane >> 5) * 2048 + qi * 32 + (lane & 31)) * 8, 0, AUX_SC1);
    } else {
      if (TOUCH_EARLY) TOUCH();
      tag = __builtin_amdgcn_raw_buffer_load_b32(rw, OFF_EPOCH, 0, AUX_SC1) + 1u;
    }
    STAMP(1);
    if (lane == 0) *(LAS unsigned*)(L + L_TAG) = tag;
    {
      const int qo = OFF_Q + (b * 2048 + kvh * 256) * 8 + lane * 16;
      v4u q0, q1;
      bool ok0 = false, ok1 = false;
      WAIT_UNTIL(T_P, 2);
      SPIN_LOOP {
        NOHOIST();
        if (!ok0) q0 = LD128(qo);
        if (!ok1) q1 = LD128(qo + 1024);
        ok0 = tagged(q0, tag); ok1 = tagged(q1, tag);
        if (__all(ok0 & ok1)) break;
      }
      *(LAS v2u*)(L + L_QF + lane * 8) = v2u{q0[0], q0[2]};
      *(LAS v2u*)(L + L_QF + 512 + lane * 8) = v2u{q1[0], q1[2]};
      STAMP(2);
      barrier();
    }
    if (!TOUCH_EARLY) TOUCH();
    asm volatile("s_waitcnt vmcnt(0)" ::: "memory");
    return;
  }
  const int nstep = (ntile - w + 3) >> 2;
  const __amdgpu_buffer_rsrc_t rv = __builtin_amdgcn_make_buffer_rsrc((void*)(ka[2] + pbase), 0, SK * DH * 2, 0x00020000);
#define VMWAIT(n) do { asm volatile("s_waitcnt vmcnt(%0)" :: "i"(n) : "memory"); SB(); } while (0)
#define RELANE() do { asm volatile("v_mbcnt_lo_u32_b32 %0, -1, 0\n\tv_mbcnt_hi_u32_b32 %0, -1, %0" : "=v"(lane)); c = lane & 15; g = lane >> 4; } while (0)
  int wp = 0;
#define WUPTO(n, pace) do { _Pragma("unroll 1") while (wp < (n)) { \
      const u16* src_ = role == 2 ? wo + (size_t)(bi * 64 + 4 * wp + g) * D + 128 * w + 8 * c \
                      : wp < 16 ? (wp < 8 ? wgate : wup) + (size_t)(128 * w + 16 * (wp & 7) + (lane >> 2)) * F + ci * 32 + (((lane & 3) ^ ((lane >> 4) & 2)) * 8) \
                                : wdown + (size_t)(ci * 32 + 4 * (wp & 7) + g) * D + 128 * w + 8 * c; \
      VMWAIT(pace); glds16(src_, lds0 + L_W + w * WREG + wp * 1024); ++wp; } } while (0)
  if (qrole) {
    const int r8 = lane >> 3;
    const u16* p = wq + (size_t)(128 * w + r8) * (HQ * DH) + qi * 64;
    const int ch0 = ((lane & 7) ^ (r8 & 2)) * 8;
#pragma unroll 1
    for (int t = 0; t < 16; t += 2) {
      glds16(p + ch0, lds0 + L_W + w * WREG + t * 1024);
      glds16(p + 8 * (HQ * DH) + (ch0 ^ 32), lds0 + L_W + w * WREG + t * 1024 + 1024);
      p += 16 * (HQ * DH);
    }
    STAMP(7);
  }
  STAMP(11);
  if (!qrole) { ISSUEK(4, 0); ISSUEK(5, 1); }
  else {
    const int sw = (q4 & 2) | ((g & 1) << 2);
    v4u fr[16];
#define QFRAGS(ks) do { _Pragma("unroll") for (int t = 0; t < 4; ++t) { \
        LAS unsigned char* a = L + L_W + w * WREG + (ks) * 4096 + g * 1024 + q4 * 128 + (p4 & 1) * 8 + (((2 * t + (p4 >> 1)) ^ sw) * 16); fr[(ks) * 4 + t] = tr_frag(a, a + 512); } } while (0)
    VMWAIT(8); QFRAGS(0); QFRAGS(1); ISSUEK(4, 0);
    VMWAIT(4 + NINST(0)); QFRAGS(2); ISSUEK(5, 1);
    barrier();
    f32x4 qa[4];
    v4u xb[4];
#pragma unroll
    for (int ks = 0; ks < 4; ++ks) xb[ks] = *(LAS v4u*)(S + L_XN + (c & 1) * 1024 + (128 * w + 32 * ks + 8 * g) * 2);
#pragma unroll
    for (int t = 0; t < 4; ++t) qa[t] = f32x4{0.f, 0.f, 0.f, 0.f};
#pragma unroll
    for (int ks = 0; ks < 3; ++ks)
#pragma unroll
      for (int t = 0; t < 4; ++t) qa[t] = mfma32(fr[ks * 4 + t], xb[ks], qa[t]);
    VMWAIT(NINST(0) + NINST(1)); STAMP(8); QFRAGS(3);
#pragma unroll
    for (int t = 0; t < 4; ++t) qa[t] = mfma32(fr[12 + t], xb[3], qa[t]);
    if (c < 2) {
#pragma unroll
      for (int t = 0; t < 4; ++t) *(LAS f32x4*)(S + L_RED + w * 512 + c * 256 + t * 64 + g * 16) = qa[t];
    }
    STAMP(1);
    barrier();
  }
  const int rem = nstep & 3, nlt = 3 + ((nstep - 3) & 3);
#pragma unroll
  for (int d = 6; d < NPRE; ++d) { VMWAIT(NINST(d - 1)); ISSUEK(d, d - 4); }
  {
    const unsigned char* kt = (const unsigned char*)(ka[1] + pbase + sbase + (NPRE - 4) * 16384u);
    const int kh = c * 256 + g * 16;
#pragma unroll 1
    for (int t = 0; t < nlt; ++t) {
      const unsigned char* src = kt + ((t & 1) ? koff : kh);
      const int st = (t & 1) ? 256 : 64;
      VMWAIT(4);
#pragma unroll
      for (int s = 0; s < 4; ++s) glds16((const u16*)(src + s * st), lds0 + L_W + w * WREG + t * 4096 + s * 1024);
      kt += 16384;
    }
  }
  int j = NPRE - 4 + nlt;
#pragma unroll
  for (int d = 0; d < 4; ++d) { VMWAIT(d ? NINST(d - 1) : 4); ISSUEK(d, j + d); }
  STAMP(10);
  barrier();
  STAMP(2);
  const v4u qf = *(LAS v4u*)(L + L_QF + (c >> 2) * 256 + g * 64 + (c & 3) * 16);
  tag = __builtin_amdgcn_readfirstlane(*(LAS unsigned*)(L + L_TAG));

  LAS unsigned char* scp = L + L_SC + w * 5120 + g * 32 + (c >> 2) * 8;
  float m = -INFINITY;
  f32x4 sc;
#define QK(d) do { \
    _Pragma("unroll") for (int s = 0; s < 4; ++s) { const f32x4 dd = mfma32(kb[d][s], qf, f32x4{0.f, 0.f, 0.f, 0.f}); sc[s] = (c & 2) ? ((c & 1) ? dd[3] : dd[2]) : ((c & 1) ? dd[1] : dd[0]); } \
    asm volatile("" : "+v"(sc) :: "memory"); SB(); } while (0)
#define SCORE(d, j) do { \
    _Pragma("unroll") for (int s = 0; s < 4; ++s) { float t_ = sc[s]; t_ += dpp<XOR1>(t_); t_ += dpp<XOR2>(t_); sc[s] = t_; } \
    const unsigned r01 = cvtpk(sc[0], sc[1]), r23 = cvtpk(sc[2], sc[3]); \
    const unsigned t01 = cvtpk(bflo(r01) * SCALE, bfhi(r01) * SCALE), t23 = cvtpk(bflo(r23) * SCALE, bfhi(r23) * SCALE); \
    m = vmax(vmax(m, vmax(bflo(t01), bfhi(t01))), vmax(bflo(t23), bfhi(t23))); \
    *(LAS v2u*)(scp + (j) * 128) = v2u{t01, t23}; asm volatile("" : "+v"(m)); SB(); } while (0)
  const int voff = g * 1024 + c * 16;
  int lt = 0;
#pragma unroll 1
  do {
    const bool tov = j + 4 >= nstep;
    const __amdgpu_buffer_rsrc_t rn = __builtin_amdgcn_make_buffer_rsrc((void*)((tov ? ka[2] : ka[1]) + pbase), 0, SK * DH * 2, 0x00020000);
    const int on = tov ? voff : koff;
    const unsigned sn = sbase + (unsigned)(tov ? rem : j + 4) * 16384u;
#pragma unroll
    for (int d = 0; d < 4; ++d) { QK(d); ISSUE(d, rn, on, sn + d * 16384u); SCORE(d, j + d); }
    j += 4;
#pragma unroll 1
    for (const int le = lt + 2 < nlt ? lt + 2 : nlt; lt < le; ++lt) {
      LAS unsigned char* a = L + L_W + w * WREG + lt * 4096 + ((lt & 1) ? lane * 16 : (g * 64 + (c & 3) * 16 + (c >> 2) * 4) * 16);
      const int st = (lt & 1) ? 1024 : 16;
#pragma unroll
      for (int s = 0; s < 4; ++s) kb[NPRE][s] = *(LAS v4u*)(a + s * st);
      QK(NPRE); SCORE(NPRE, NPRE - 4 + lt);
    }
  } while (j < nstep);
  {
    const unsigned char* vt = (const unsigned char*)(ka[2] + pbase + sbase) + voff;
#pragma unroll 1
    for (int t = 0; t < rem; ++t) {
#pragma unroll
      for (int s = 0; s < 4; ++s) glds16((const u16*)(vt + s * 256), lds0 + L_W + w * WREG + t * 4096 + s * 1024);
      vt += 16384;
    }
  }
#pragma unroll
  for (int d = 4; d < NPRE; ++d) { QK(d); SCORE(d, d - 4); }
  STAMP(9);
  m = sw16_max(m, m); m = sw32_max(m, m);
  const float mlog = m * LOG2E;
  const unsigned m0 = (c & 3) == 0 ? 0xffffu : (c & 3) == 1 ? 0xffff0000u : 0u;
  const unsigned m1 = (c & 3) == 2 ? 0xffffu : (c & 3) == 3 ? 0xffff0000u : 0u;
  f32x4 acc0 = {0.f, 0.f, 0.f, 0.f}, acc1 = {0.f, 0.f, 0.f, 0.f};
  float l = 0.f;
#define PCALC(bb, j) do { \
    const v2u t_ = *(LAS v2u*)(scp + (j) * 128); \
    const float sv[4] = {bflo(t_[0]), bfhi(t_[0]), bflo(t_[1]), bfhi(t_[1])}; \
    _Pragma("unroll") for (int i = 0; i < 4; ++i) { const float p_ = __builtin_amdgcn_exp2f(fmaf(sv[i], LOG2E, -mlog)); const unsigned pp = cvtpk(p_, p_); l += bflo(pp); bb[i] = v2u{pp & m0, pp & m1}; } } while (0)
#define PMFMA(d, bb) do { SB(); \
    _Pragma("unroll") for (int i = 0; i < 4; ++i) { acc0 = mfma16(v2u{kb[d][i][0], kb[d][i][1]}, bb[i], acc0); acc1 = mfma16(v2u{kb[d][i][2], kb[d][i][3]}, bb[i], acc1); } \
    asm volatile("" : "+v"(acc0), "+v"(acc1) :: "memory"); SB(); } while (0)
#define PV(d, j) do { v2u bb_[4]; PCALC(bb_, j); PMFMA(d, bb_); } while (0)
  j = rem;
#pragma unroll 1
  do {
#pragma unroll
    for (int d = 0; d < 4; ++d) { PV(d, j + d); ISSUE(d, rv, voff, sbase + (unsigned)(j + 4 + d) * 16384u); }
    j += 4;
  } while (j + 4 + 4 * NLR < nstep);
#pragma unroll 1
  for (int t = 0; t < rem; ++t) {
#pragma unroll
    for (int s = 0; s < 4; ++s) kb[NPRE][s] = *(LAS v4u*)(L + L_W + w * WREG + t * 4096 + s * 1024 + lane * 16);
    PV(NPRE, t);
  }
  unsigned bal[2] = {0u, 0u};
  const int blo = (lane & 31) * 128;
#pragma unroll 1
  do {
#pragma unroll
    for (int d = 0; d < 4; ++d) {
      PV(d, j + d);
      if (!(d & 1)) asm volatile("" :: "v"(bal[d >> 1]));
#pragma unroll
      for (int s = 0; s < 4; ++s) kb[d][s] = __builtin_amdgcn_raw_buffer_load_b128(rv, voff + s * 256, sbase + (unsigned)(j + 4 + d) * 16384u, AUX_NT);
      SB();
      if (!(d & 1)) {
        const bool late = (int)((unsigned)__builtin_amdgcn_s_memrealtime() - (tstart + T_L)) > 0;
        bal[d >> 1] = __builtin_amdgcn_raw_buffer_load_b32(rv, late ? 0 : blo, sbase + (unsigned)(j + 4 + d) * 16384u, AUX_NT); SB();
      }
    }
    j += 4;
  } while (j + 4 < nstep);
  asm volatile("v_mbcnt_lo_u32_b32 %0, -1, 0\n\tv_mbcnt_hi_u32_b32 %0, -1, %0" : "=v"(lane));
  c = lane & 15; g = lane >> 4; q4 = (lane >> 2) & 3; p4 = lane & 3;
  const int tid = w * 64 + lane;
#define PIN(x) asm volatile("" : "+v"(x))
  const unsigned tagh = mixtag(tag);
  v2u lb[4][4];
#pragma unroll
  for (int d = 0; d < 4; ++d) PCALC(lb[d], j + d);
  l = sw16_sum(l, l); l = sw32_sum(l, l);
  *(LAS f32x2*)(S + L_RED + (c >> 2) * 32 + w * 8) = f32x2{mlog, l};
  barrier();
  const int hd = 2 * (w & 1) + (lane >> 5);
  float fw; unsigned t1w, t3w;
  {
#define MERGE_ML(h, M_, L_) do { const f32x4 u0_ = *(LAS f32x4*)(S + L_RED + (h) * 32), u1_ = *(LAS f32x4*)(S + L_RED + (h) * 32 + 16); \
      M_ = vmax(vmax(u0_[0], u0_[2]), vmax(u1_[0], u1_[2])); \
      L_ = fmaf(__builtin_amdgcn_exp2f(u0_[2] - M_), u0_[3], __builtin_amdgcn_exp2f(u0_[0] - M_) * u0_[1]) + fmaf(__builtin_amdgcn_exp2f(u1_[2] - M_), u1_[3], __builtin_amdgcn_exp2f(u1_[0] - M_) * u1_[1]); } while (0)
    float M1, L1, M2, L2;
    MERGE_ML(c >> 2, M1, L1); MERGE_ML(hd, M2, L2);
    fw = __builtin_amdgcn_exp2f(mlog - M1) * __builtin_amdgcn_rcpf(L1);
    const unsigned lse = f2u(M2 + __builtin_amdgcn_logf(L2));
    t1w = __builtin_amdgcn_perm(tagh, lse, 0x05040100u); t3w = __builtin_amdgcn_perm(tagh, lse, 0x07060302u);
  }
  unsigned mw = lds0 + L_S + L_MRG + w * 2048 + lane * 16, mr = lds0 + L_S + L_MRG + ((lane >> 2) & 1) * 1024 + (((lane >> 3) & 3) * 16 + hd * 4 + (lane & 3)) * 16;
  int pao = OFF_PART + ((b * HQ + kvh * 4 + hd) * NSPLIT + sp) * 512 + (lane & 31) * 16;
  PIN(mw); PIN(mr); PIN(pao); PIN(fw); PIN(t1w); PIN(t3w);
#pragma unroll
  for (int d = 0; d < 4; ++d) { PMFMA(d, lb[d]); if (d == 2) asm volatile("" :: "v"(bal[0]), "v"(bal[1])); }
  STAMP(3);
  *(LAS f32x4*)mw = acc0 * fw; *(LAS f32x4*)(mw + 1024) = acc1 * fw;
  barrier();
  if (w < 2) {
    const f32x4 o = (*(LAS f32x4*)mr + *(LAS f32x4*)(mr + 2048)) + (*(LAS f32x4*)(mr + 4096) + *(LAS f32x4*)(mr + 6144));
    ST128((v4u{cvtpk(o[0], o[1]), t1w, cvtpk(o[2], o[3]), t3w}), pao);
  }
  STAMP(4);
  const int ci = CI();
  WUPTO(role == 1 ? 24 : role == 2 ? 16 : 0, 40);
  unsigned dm0 = (c & 7) == 0 ? 0xffffu : (c & 7) == 1 ? 0xffff0000u : 0u, dm1 = (c & 7) == 2 ? 0xffffu : (c & 7) == 3 ? 0xffff0000u : 0u,
           dm2 = (c & 7) == 4 ? 0xffffu : (c & 7) == 5 ? 0xffff0000u : 0u, dm3 = (c & 7) == 6 ? 0xffffu : (c & 7) == 7 ? 0xffff0000u : 0u;
  unsigned tb0 = tagh & 0xffu, tb1 = (tagh >> 8) & 0xffu, tb2 = (tagh >> 16) & 0xffu, tb3 = tagh >> 24;
#define PUBLISH_ACC(acc, off) ST128((v4u{PK24(acc[0], tb0), PK24(acc[1], tb1), PK24(acc[2], tb2), PK24(acc[3], tb3)}), (off))

  if (role == 2) {
    unsigned wra = lds0 + L_W + w * WREG + lane * 16;
    PIN(wra);
    const int head = bi >> 1;
    const int po = OFF_PART + (((lane >> 5) * HQ + head) * NSPLIT + ((lane >> 2) & 7)) * 512 + (bi & 1) * 256 + w * 64 + (lane & 3) * 16;
    unsigned ow = lds0 + L_S + L_OB + ((g >> 1) * 4 + (lane & 3)) * 64 + (8 * (w >> 1) + 2 * ((g & 1) + ((lane & 4) >> 1)) + (w & 1)) * 4, orr = lds0 + L_S + L_OB + ((c >> 3) * 4 + g) * 64;
    int so_ = OFF_P1 + bi * 4096 + tid * 16;
    f32x4 acc = {0.f, 0.f, 0.f, 0.f};
    PIN(ow); PIN(orr); PIN(so_); PIN(dm0); PIN(dm1); PIN(dm2); PIN(dm3); PIN(tb0); PIN(tb1); PIN(tb2); PIN(tb3); PIN(acc);
    v4u a0, a1;
    poll2<PD1, 16>(ws, po, po + 8 * 512, tagh, a0, a1);
    STAMP(5);
    v4u wr[16];
#pragma unroll
    for (int i = 0; i < 16; ++i) wr[i] = *(LAS v4u*)(wra + i * 1024);
    SB();
    const float l0 = u2f(__builtin_amdgcn_perm(a0[3], a0[1], 0x05040100u)), l1 = u2f(__builtin_amdgcn_perm(a1[3], a1[1], 0x05040100u));
    float xm = vmax(l0, l1);
    xm = vmax(vmax(xm, dpp<ROR4>(xm)), vmax(dpp<ROR8>(xm), dpp<ROR12>(xm)));
    xm = sw16_max(xm, xm);
    const float w0 = __builtin_amdgcn_exp2f(l0 - xm), w1 = __builtin_amdgcn_exp2f(l1 - xm);
    float u0 = sw16_sum(fmaf(w1, bflo(a1[0]), w0 * bflo(a0[0])), fmaf(w1, bfhi(a1[0]), w0 * bfhi(a0[0])));
    float u1 = sw16_sum(fmaf(w1, bflo(a1[2]), w0 * bflo(a0[2])), fmaf(w1, bfhi(a1[2]), w0 * bfhi(a0[2])));
    float ws_ = w0 + w1;
    ws_ = sw16_sum(ws_, ws_);
    u0 += dpp<ROR8>(u0); u1 += dpp<ROR8>(u1); ws_ += dpp<ROR8>(ws_);
    u0 += dpp<ROR4>(u0); u1 += dpp<ROR4>(u1); ws_ += dpp<ROR4>(ws_);
    const float uo = ((lane & 4) ? u1 : u0) * __builtin_amdgcn_rcpf(ws_);
    *(LAS unsigned*)ow = cvtpk(uo, uo);
    barrier();
    const v4u pv0 = *(LAS v4u*)orr, pv1 = *(LAS v4u*)(orr + 16), pv2 = *(LAS v4u*)(orr + 32), pv3 = *(LAS v4u*)(orr + 48);
    diag_chain_regs(acc, wr, pv0, pv1, dm0, dm1, dm2, dm3);
    diag_chain_regs(acc, wr + 8, pv2, pv3, dm0, dm1, dm2, dm3);
    mfma_drain(acc);
    PUBLISH_ACC(acc, so_);
    STAMP(6);
    poll_done(a0, a1);
  } else if (role == 3) {
    const int rb = rj & 1, col0 = (rj >> 1) * 32;
    unsigned xp = *(const GAS unsigned*)(x + rb * D + col0 + 2 * c);
    asm volatile("" : "+v"(xp));
    const int pp = lane >> 3;
    float h0, h1;
    unsigned rw_ = lds0 + L_S + L_RR + w * 128 + (8 * g + (lane & 7)) * 4, rr_ = lds0 + L_S + L_RR + c * 8;
    int hbo = OFF_HB + ((w & 1) * 4 + g) * 4096 + rb * 2048 + col0 * 4 + c * 8;
    GAS unsigned* op = (GAS unsigned*)(out + rb * D + col0 + 2 * c);
    PIN(rw_); PIN(rr_); PIN(hbo); PIN(op);
    {
      const int po = OFF_P1 + (16 * w + pp) * 4096 + rj * 128 + (lane & 7) * 16;
      v4u a0, a1;
      poll2<PD2, -1>(ws, po, po + 8 * 4096, tagh, a0, a1);
      STAMP(5);
      float u = sw16_sum(sw32_sum(UP24(a0[0]) + UP24(a1[0]), UP24(a0[2]) + UP24(a1[2])), sw32_sum(UP24(a0[1]) + UP24(a1[1]), UP24(a0[3]) + UP24(a1[3])));
      u += dpp<ROR8>(u);
      *(LAS float*)rw_ = u;
      barrier();
      const f32x2 s = (*(LAS f32x2*)rr_ + *(LAS f32x2*)(rr_ + 128)) + (*(LAS f32x2*)(rr_ + 256) + *(LAS f32x2*)(rr_ + 384));
      const unsigned at = cvtpk(s[0], s[1]);
      const unsigned hp = cvtpk(bflo(xp) + bflo(at), bfhi(xp) + bfhi(at));
      h0 = bflo(hp); h1 = bfhi(hp);
      if (w < 2) __builtin_amdgcn_raw_buffer_store_b64(v2u{hp, tag}, rw, hbo, 0, AUX_SC1);
      STAMP(6);
      poll_done(a0, a1);
    }
    {
      const int po = OFF_P2 + (14 * w + pp) * 4096 + rj * 128 + (lane & 7) * 16;
      v4u a0, b1;
      poll2<PD4, -1>(ws, po, po + (pp < 6 ? 8 * 4096 : 0), tagh, a0, b1);
      STAMP(7);
      const unsigned km = pp < 6 ? 0xffffff00u : 0u;
      float u = sw16_sum(sw32_sum(UP24(a0[0]) + u2f(b1[0] & km), UP24(a0[2]) + u2f(b1[2] & km)), sw32_sum(UP24(a0[1]) + u2f(b1[1] & km), UP24(a0[3]) + u2f(b1[3] & km)));
      u += dpp<ROR8>(u);
      *(LAS float*)(rw_ + 512) = u;
      barrier();
      if (tid < 16) {
        const f32x2 s = (*(LAS f32x2*)(rr_ + 512) + *(LAS f32x2*)(rr_ + 640)) + (*(LAS f32x2*)(rr_ + 768) + *(LAS f32x2*)(rr_ + 896));
        const unsigned dn = cvtpk(s[0], s[1]);
        *op = cvtpk(h0 + bflo(dn), h1 + bfhi(dn));
        if (rj == 0 && tid == 0) __builtin_amdgcn_raw_buffer_store_b32(tag, rw, OFF_EPOCH, 0, AUX_SC1);
      }
      STAMP(8);
      poll_done(a0, b1);
    }
  } else if (role == 1) {
    v4u fg[8], fu[8], wd[8];
    LAS unsigned char* fa = L + L_W + w * WREG + (8 * g + q4) * 64 + (p4 & 1) * 8;
    VMWAIT(16);
#pragma unroll
    for (int ks = 0; ks < 4; ++ks)
#pragma unroll
      for (int t = 0; t < 2; ++t) { LAS unsigned char* a = fa + 2048 * ks + (((2 * t + (p4 >> 1)) ^ ((g & 1) << 1)) * 16); fg[ks * 2 + t] = tr_frag(a, a + 256); }
    VMWAIT(8);
    STAMP(10);
#pragma unroll
    for (int ks = 0; ks < 4; ++ks)
#pragma unroll
      for (int t = 0; t < 2; ++t) { LAS unsigned char* a = fa + 8192 + 2048 * ks + (((2 * t + (p4 >> 1)) ^ ((g & 1) << 1)) * 16); fu[ks * 2 + t] = tr_frag(a, a + 256); }
    f32x4 ag[2], au[2];
    unsigned ro = lds0 + L_S + L_RED + ((c >> 2) & 1) * 1024 + ((c & 1) * 32 + (c & 2) * 8 + 4 * g) * 16 + w * 4;
    unsigned fo = lds0 + L_S + L_RED + ((c >> 3) * 32 + 4 * (c & 3) + g) * 16;
#define GU_MFMA(XB) do { _Pragma("unroll") for (int t = 0; t < 2; ++t) { ag[t] = f32x4{0.f, 0.f, 0.f, 0.f}; au[t] = f32x4{0.f, 0.f, 0.f, 0.f}; } \
      _Pragma("unroll") for (int ks = 0; ks < 4; ++ks) { \
        _Pragma("unroll") for (int t = 0; t < 2; ++t) { ag[t] = mfma32(fg[ks * 2 + t], XB, ag[t]); au[t] = mfma32(fu[ks * 2 + t], XB, au[t]); } } } while (0)
#define GU_STORE() do { const f32x4 sel = (c & 4) ? ((c & 2) ? au[1] : au[0]) : ((c & 2) ? ag[1] : ag[0]); \
      _Pragma("unroll") for (int i = 0; i < 4; ++i) *(LAS float*)(ro + i * 16) = sel[i]; } while (0)
#define SUM4(p_) ({ const f32x2 t_ = f32x2{p_[0], p_[1]} + f32x2{p_[2], p_[3]}; float r_ = t_[0] + t_[1]; PIN(r_); r_; })
    GU_MFMA((v4u{0x3f803f80u, 0x3f803f80u, 0x3f803f80u, 0x3f803f80u}));
    GU_STORE();
    barrier();
    float cg0, cg1, cu0, cu1;
    { const f32x4 p0 = *(LAS f32x4*)fo, p1 = *(LAS f32x4*)(fo + 256), p2 = *(LAS f32x4*)(fo + 1024), p3 = *(LAS f32x4*)(fo + 1280); cg0 = SUM4(p0); cg1 = SUM4(p1); cu0 = SUM4(p2); cu1 = SUM4(p3); }
    unsigned xw = lds0 + L_S + L_XN + (w & 1) * 1024 + lane * 16, xr = lds0 + L_S + L_XN + (c & 1) * 1024 + (128 * w + 8 * g) * 2, sw_ = lds0 + L_S + L_ST + w * 8, sr = lds0 + L_S + L_ST + (c >> 3) * 8;
    int so_ = OFF_P2 + ci * 4096 + tid * 16;
    f32x4 acc = {0.f, 0.f, 0.f, 0.f};
    unsigned wda = lds0 + L_W + w * WREG + 16384 + lane * 16;
    PIN(wda); PIN(ro); PIN(fo); PIN(xw); PIN(xr); PIN(sw_); PIN(sr); PIN(so_); PIN(dm0); PIN(dm1); PIN(dm2); PIN(dm3); PIN(tb0); PIN(tb1); PIN(tb2); PIN(tb3); PIN(acc);
    v4u ha = {0u, 0u, 0u, 0u}, hb = ha;
    if (w < 2) {
      const int ho = OFF_HB + xcd * 4096 + w * 2048 + lane * 32;
      poll2<PD3, 0>(ws, ho, ho + 16, tag, ha, hb);
      STAMP(5);
      *(LAS v4u*)xw = v4u{ha[0], ha[2], hb[0], hb[2]};
    } else VMWAIT(0);
    barrier();
    v4u xb[4];
#pragma unroll
    for (int ks = 0; ks < 4; ++ks) xb[ks] = *(LAS v4u*)(xr + 64 * ks);
#pragma unroll
    for (int i = 0; i < 8; ++i) wd[i] = *(LAS v4u*)(wda + i * 1024);
    __builtin_amdgcn_sched_barrier(0);
    const float pv = bflo(__builtin_amdgcn_readfirstlane(ha[0]));
    const float d0 = bflo(ha[0]) - pv, d1 = bfhi(ha[0]) - pv, d2 = bflo(ha[2]) - pv, d3 = bfhi(ha[2]) - pv, d4 = bflo(hb[0]) - pv, d5 = bfhi(hb[0]) - pv, d6 = bflo(hb[2]) - pv, d7 = bfhi(hb[2]) - pv;
    float s1 = ((d0 + d1) + (d2 + d3)) + ((d4 + d5) + (d6 + d7));
    float s2 = (fmaf(d1, d1, d0 * d0) + fmaf(d3, d3, d2 * d2)) + (fmaf(d5, d5, d4 * d4) + fmaf(d7, d7, d6 * d6));
    __builtin_amdgcn_sched_barrier(0);
    GU_MFMA(xb[ks]);
    {
      float t = sw32_sum(s1, s2);
      t = sw16_sum(t, t); t += dpp<ROR8>(t); t += dpp<ROR4>(t); t += dpp<XOR2>(t); t += dpp<XOR1>(t);
      const float md = u2f(__builtin_amdgcn_readlane(f2u(t), 0)) * (1.0f / D), q2 = u2f(__builtin_amdgcn_readlane(f2u(t), 32)) * (1.0f / D);
      const float var = vmax(fmaf(-md, md, q2), 0.f);
      const float rs = __builtin_amdgcn_rcpf(rbf(__builtin_amdgcn_sqrtf(rbf(rbf(var) + 1e-5f))));
      *(LAS f32x2*)sw_ = f32x2{rbf(pv + md), rs};
    }
    GU_STORE();
    barrier();
    const f32x2 st = *(LAS f32x2*)sr;
    const f32x4 p0 = *(LAS f32x4*)fo, p1 = *(LAS f32x4*)(fo + 256), p2 = *(LAS f32x4*)(fo + 1024), p3 = *(LAS f32x4*)(fo + 1280);
    __builtin_amdgcn_sched_barrier(0);
    const float G0 = SUM4(p0), G1 = SUM4(p1), U0 = SUM4(p2), U1 = SUM4(p3);
    const unsigned gp = cvtpk(fmaf(-st[0], cg0, G0) * st[1], fmaf(-st[0], cg1, G1) * st[1]);
    const unsigned up = cvtpk(fmaf(-st[0], cu0, U0) * st[1], fmaf(-st[0], cu1, U1) * st[1]);
    const float g0 = bflo(gp), g1 = bfhi(gp);
    const unsigned sg = cvtpk(g0 * __builtin_amdgcn_rcpf(1.0f + __builtin_amdgcn_exp2f(-g0 * LOG2E)), g1 * __builtin_amdgcn_rcpf(1.0f + __builtin_amdgcn_exp2f(-g1 * LOG2E)));
    const float av0 = bflo(sg) * bflo(up), av1 = bfhi(sg) * bfhi(up);
    diag_chain_quad(acc, wd, cvtpk(av0, av0), cvtpk(av1, av1), dm0, dm1, dm2, dm3);
    mfma_drain(acc);
    PUBLISH_ACC(acc, so_);
    STAMP(6);
    poll_done(ha, hb);
  }
  DUMP_STAMPS();
}

extern "C" void kernel_launch(void* const* d_in, const int* in_sizes, int n_in, void* d_out, int out_size, void* d_ws, size_t ws_size, hipStream_t stream) {
  fused_block<<<NWG, NTHR, 0, stream>>>((const u16*)d_in[0], (const u16*)d_in[1], (const u16*)d_in[2], (const u16*)d_in[3], (unsigned char*)d_ws,
                                        (const u16*)d_in[4], (const u16*)d_in[6], (const u16*)d_in[7], (const u16*)d_in[8], (u16*)d_out, make_plan() TUNE_ARG);
}
```

```cpp
#pragma clang diagnostic ignored "-Wint-to-pointer-cast"
#include <hip/hip_runtime.h>
#include <hip/hip_bf16.h>
#include <cstdint>

typedef unsigned short u16;
typedef unsigned v4u __attribute__((ext_vector_type(4)));
typedef unsigned v2u __attribute__((ext_vector_type(2)));
typedef float f32x4 __attribute__((ext_vector_type(4)));
typedef float f32x2 __attribute__((ext_vector_type(2)));
typedef short s16x4 __attribute__((ext_vector_type(4)));
typedef __bf16 bf16x8_t __attribute__((ext_vector_type(8)));
typedef __bf16 bf16x2_t __attribute__((ext_vector_type(2)));
#define LAS __attribute__((address_space(3)))
#define GAS __attribute__((address_space(1)))

constexpr int D = 512, HQ = 32, HKV = 8, SK = 32768, DH = 128, F = 1792;
constexpr int NWG = 256, NTHR = 320, NSPLIT = 16;
struct Plan { unsigned e[32]; };
#define TUNE_PARAM
#define TUNE_ARG
#define ABL 0
#define TL_C 130
#define TL_CS 129
#define TL_B 128
#define TL_Q 124
#define TL_QS 120
#define T_WC 0
#define T_WB 0
#define T_L 3500
#define T_PD 150
#define T_P 680
static Plan make_plan() {
  Plan p;
  for (int b = 0; b < 2; ++b) {
    const int nc = 4 - b;
    int n[16], role[16], ix[16], used = 0, nn = 0;
    for (int sp = 0; sp < 16; ++sp) {
      if (sp < nc) { role[sp] = 1; ix[sp] = 2 * sp + b; n[sp] = sp == 0 ? TL_CS : TL_C; }
      else if (sp >= 4 && sp < 8) { role[sp] = 2; ix[sp] = 2 * (sp - 4) + b; n[sp] = TL_B; }
      else if (sp >= 12) { role[sp] = 4; ix[sp] = 2 * (sp - 12) + b; n[sp] = ix[sp] == 3 ? TL_QS : TL_Q; }
      else { role[sp] = (sp == 8 || sp == 9) ? 3 : 0; ix[sp] = role[sp] ? 2 * (sp - 8) + b : 0; n[sp] = 0; ++nn; }
      used += n[sp];
    }
    int rest = 2048 - used, t0 = 0;
    for (int sp = 0; sp < 16; ++sp) {
      if (n[sp] == 0) { n[sp] = (rest + nn - 1) / nn; rest -= n[sp]; --nn; }
      p.e[2 * sp + b] = (unsigned)t0 | ((unsigned)n[sp] << 11) | ((unsigned)role[sp] << 19) | ((unsigned)ix[sp] << 22);
      t0 += n[sp];
    }
  }
  return p;
}
#define HMASK 0x0F0F
#define HEAVY(d, s) ((HMASK >> (4 * ((d) & 3) + (s))) & 1)
#define NINST(d) (4 + __builtin_popcount((HMASK >> (4 * ((d) & 3))) & 15))
#define NPRE 10
#define NLT 6
#define TOUCH_EARLY 1
#define NLR 2
#define PD1 2
#define PD2 3
#define PD3 1
#define PD4 3
#define PSL 1
#define PACE_N 4
#define CODE_BYTES 32768
constexpr int NB = 64, NC = 56, NR = 64, NREP = 8;
constexpr int OFF_EPOCH = 0;
constexpr int OFF_HB = 0x190000;
constexpr int OFF_PART = 0x10000;
constexpr int OFF_P1 = 0x90000;
constexpr int OFF_P2 = 0xD0000;
constexpr int OFF_Q = 0x180000;
constexpr int WS_BYTES = 0x1A0000, OFF_DBG = 0x200000;
constexpr int AUX_NT = 2, AUX_SC1 = 16;
constexpr unsigned SPIN_CAP = 1u << 16;
constexpr int L_W = 0;
constexpr int WREG = 24576;
constexpr int L_QF = 4 * WREG;
constexpr int L_SC = L_QF + 1024;
constexpr int L_TAG = L_SC + 4 * 40 * 128;
constexpr int L_S = L_TAG + 16;
constexpr int L_XN = 0;
constexpr int L_RED = L_XN + 2048;
constexpr int L_MRG = L_RED + 2048;
constexpr int L_OB = L_MRG + 4 * 4 * 132 * 4;
constexpr int L_RR = L_OB + 512;
constexpr int L_ST = L_RR + 1024;
constexpr int S_BYTES = L_ST + 32;
constexpr int L_PF = L_S + S_BYTES;
constexpr int L_DBG = L_PF + 256;
constexpr int L_BYTES = L_DBG + 128;

__device__ __forceinline__ float bflo(unsigned v) { return __uint_as_float(v << 16); }
__device__ __forceinline__ float bfhi(unsigned v) { return __uint_as_float(v & 0xffff0000u); }
__device__ __forceinline__ unsigned cvtpk(float lo, float hi) { return __builtin_bit_cast(unsigned, __builtin_convertvector(f32x2{lo, hi}, bf16x2_t)); }
__device__ __forceinline__ float rbf(float x) { return bflo(cvtpk(x, x)); }
__device__ __forceinline__ bool tagged(v4u v, unsigned tag) { return (v[1] == tag) & (v[3] == tag); }
__device__ __forceinline__ float u2f(unsigned v) { return __uint_as_float(v); }
__device__ __forceinline__ unsigned f2u(float v) { return __float_as_uint(v); }

__device__ __forceinline__ float vmax(float a, float b) { return __builtin_elementwise_maximum(a, b); }
template <int CTRL> __device__ __forceinline__ float dpp(float x) { return __builtin_bit_cast(float, __builtin_amdgcn_mov_dpp(__builtin_bit_cast(int, x), CTRL, 0xf, 0xf, true)); }
template <int CTRL> __device__ __forceinline__ unsigned dppu(unsigned x) { return (unsigned)__builtin_amdgcn_mov_dpp((int)x, CTRL, 0xf, 0xf, true); }
constexpr int XOR1 = 0xB1, XOR2 = 0x4E, ROR8 = 0x128, ROR4 = 0x124, ROR12 = 0x12C;
__device__ __forceinline__ float sw32_sum(float a, float b) { auto r = __builtin_amdgcn_permlane32_swap(f2u(a), f2u(b), false, false); return u2f(r[0]) + u2f(r[1]); }
__device__ __forceinline__ float sw16_sum(float a, float b) { auto r = __builtin_amdgcn_permlane16_swap(f2u(a), f2u(b), false, false); return u2f(r[0]) + u2f(r[1]); }
__device__ __forceinline__ float sw32_max(float a, float b) { auto r = __builtin_amdgcn_permlane32_swap(f2u(a), f2u(b), false, false); return vmax(u2f(r[0]), u2f(r[1])); }
__device__ __forceinline__ float sw16_max(float a, float b) { auto r = __builtin_amdgcn_permlane16_swap(f2u(a), f2u(b), false, false); return vmax(u2f(r[0]), u2f(r[1])); }
__device__ __forceinline__ float wave_sum(float x) {
  x += dpp<XOR1>(x); x += dpp<XOR2>(x); x += dpp<ROR4>(x); x += dpp<ROR8>(x);
  x = sw16_sum(x, x); return sw32_sum(x, x);
}
__device__ __forceinline__ f32x4 mfma32(v4u a, v4u b, f32x4 c) { return __builtin_amdgcn_mfma_f32_16x16x32_bf16(__builtin_bit_cast(bf16x8_t, a), __builtin_bit_cast(bf16x8_t, b), c, 0, 0, 0); }
__device__ __forceinline__ f32x4 mfma16(v2u a, v2u b, f32x4 c) { return __builtin_amdgcn_mfma_f32_16x16x16bf16_1k(__builtin_bit_cast(s16x4, a), __builtin_bit_cast(s16x4, b), c, 0, 0, 0); }
__device__ __forceinline__ void barrier() { asm volatile("s_waitcnt lgkmcnt(0)\n\ts_barrier" ::: "memory"); }
__device__ __forceinline__ v4u tr_frag(LAS unsigned char* lo, LAS unsigned char* hi) {
  const v2u a = __builtin_bit_cast(v2u, __builtin_amdgcn_ds_read_tr16_b64_v4i16((LAS s16x4*)lo));
  const v2u b = __builtin_bit_cast(v2u, __builtin_amdgcn_ds_read_tr16_b64_v4i16((LAS s16x4*)hi));
  return v4u{a[0], a[1], b[0], b[1]};
}

#define AND4(T0, T1, T2, T3, SRC, MOD, SFX) "v_and_b32" SFX " " T0 ", " SRC ", %[m0]" MOD "\n\tv_and_b32" SFX " " T1 ", " SRC ", %[m1]" MOD "\n\tv_and_b32" SFX " " T2 ", " SRC ", %[m2]" MOD "\n\tv_and_b32" SFX " " T3 ", " SRC ", %[m3]" MOD "\n\t"
#define ANDA(SRC, MOD, SFX) AND4("v240", "v241", "v242", "v243", SRC, MOD, SFX)
#define ANDB(SRC, MOD, SFX) AND4("v244", "v245", "v246", "v247", SRC, MOD, SFX)
#define MFA(W) "v_mfma_f32_16x16x32_bf16 %[acc], " W ", v[240:243], %[acc]\n\t"
#define MFB(W) "v_mfma_f32_16x16x32_bf16 %[acc], " W ", v[244:247], %[acc]\n\t"
#define CHAIN_CLOB "v240", "v241", "v242", "v243", "v244", "v245", "v246", "v247"
#define CHAIN_W(W) [w0] "v"(W[0]), [w1] "v"(W[1]), [w2] "v"(W[2]), [w3] "v"(W[3]), [w4] "v"(W[4]), [w5] "v"(W[5]), [w6] "v"(W[6]), [w7] "v"(W[7])
__device__ __forceinline__ void diag_chain_regs(f32x4& acc, const v4u* W, v4u pa, v4u pb, unsigned m0, unsigned m1, unsigned m2, unsigned m3) {
  asm volatile(ANDA("%[p0]", "", "") ANDB("%[p1]", "", "") MFA("%[w0]") ANDA("%[p2]", "", "") MFB("%[w1]") ANDB("%[p3]", "", "") MFA("%[w2]") ANDA("%[p4]", "", "") MFB("%[w3]")
               ANDB("%[p5]", "", "") MFA("%[w4]") ANDA("%[p6]", "", "") MFB("%[w5]") ANDB("%[p7]", "", "") MFA("%[w6]") "s_nop 0\n\t" MFB("%[w7]")
               : [acc] "+v"(acc)
               : CHAIN_W(W), [p0] "v"(pa[0]), [p1] "v"(pa[1]), [p2] "v"(pa[2]), [p3] "v"(pa[3]), [p4] "v"(pb[0]), [p5] "v"(pb[1]), [p6] "v"(pb[2]), [p7] "v"(pb[3]),
                 [m0] "v"(m0), [m1] "v"(m1), [m2] "v"(m2), [m3] "v"(m3)
               : CHAIN_CLOB);
}
#define QP(t) " quad_perm:[" #t "," #t "," #t "," #t "] row_mask:0xf bank_mask:0xf"
__device__ __forceinline__ void diag_chain_quad(f32x4& acc, const v4u* W, unsigned pa, unsigned pb, unsigned m0, unsigned m1, unsigned m2, unsigned m3) {
  asm volatile("s_nop 1\n\t" ANDA("%[pa]", QP(0), "_dpp") ANDB("%[pa]", QP(1), "_dpp") MFA("%[w0]") ANDA("%[pa]", QP(2), "_dpp") MFB("%[w1]") ANDB("%[pa]", QP(3), "_dpp") MFA("%[w2]")
               ANDA("%[pb]", QP(0), "_dpp") MFB("%[w3]") ANDB("%[pb]", QP(1), "_dpp") MFA("%[w4]") ANDA("%[pb]", QP(2), "_dpp") MFB("%[w5]") ANDB("%[pb]", QP(3), "_dpp") MFA("%[w6]")
               "s_nop 0\n\t" MFB("%[w7]")
               : [acc] "+v"(acc)
               : CHAIN_W(W), [pa] "v"(pa), [pb] "v"(pb), [m0] "v"(m0), [m1] "v"(m1), [m2] "v"(m2), [m3] "v"(m3)
               : CHAIN_CLOB);
}
__device__ __forceinline__ void mfma_drain(f32x4& acc) { asm volatile("s_nop 7" : "+v"(acc)); }

__device__ __forceinline__ v4u ln_row(v4u xv) {
  float x[8];
#pragma unroll
  for (int i = 0; i < 4; ++i) { x[2 * i] = bflo(xv[i]); x[2 * i + 1] = bfhi(xv[i]); }
  const float mean = wave_sum(((x[0] + x[1]) + (x[2] + x[3])) + ((x[4] + x[5]) + (x[6] + x[7]))) * (1.0f / D);
  float vs = 0.f;
#pragma unroll
  for (int i = 0; i < 8; ++i) { const float d = x[i] - mean; vs = fmaf(d, d, vs); }
  const float var = wave_sum(vs) * (1.0f / D);
  const float mb = rbf(mean);
  const float rs = __builtin_amdgcn_rcpf(rbf(__builtin_amdgcn_sqrtf(rbf(rbf(var) + 1e-5f))));
  v4u r;
#pragma unroll
  for (int i = 0; i < 4; ++i) { const unsigned d = cvtpk(x[2 * i] - mb, x[2 * i + 1] - mb); r[i] = cvtpk(bflo(d) * rs, bfhi(d) * rs); }
  return r;
}
__device__ __forceinline__ void glds16(const u16* gsrc, unsigned lds_dst) {
  unsigned keep;
  asm volatile("s_mov_b32 %0, m0\n\ts_mov_b32 m0, %2\n\ts_nop 0\n\tglobal_load_lds_dwordx4 %1, off nt\n\ts_mov_b32 m0, %0" : "=&s"(keep) : "v"(gsrc), "s"(lds_dst) : "memory");
}

__device__ __forceinline__ unsigned mixtag(unsigned t) { t *= 0x9E3779B1u; return t ^ (t >> 15); }
#define PK24(x, tb) (((f2u(x) + 0x80u) & 0xffffff00u) | (tb))
#define UP24(x) u2f((x) & 0xffffff00u)
__device__ __forceinline__ unsigned lowbytes(v4u v) { return __builtin_amdgcn_perm(v[1], v[0], 0x0c0c0400u) | __builtin_amdgcn_perm(v[3], v[2], 0x04000c0cu); }
template <int PD_, int TSH>
__device__ __forceinline__ void poll2(const unsigned char* ws, int o0, int o1, unsigned tag, v4u& r0, v4u& r1) {
  r0 = v4u{~tag, ~tag, ~tag, ~tag}; r1 = r0;
#define PASS() asm volatile("global_load_dwordx4 %0, %2, %4 sc1\n\tglobal_load_dwordx4 %1, %3, %4 sc1" : "+v"(r0), "+v"(r1) : "v"(o0), "v"(o1), "s"(ws) : "memory")
#pragma unroll
  for (int k = 0; k < PD_ - 1; ++k) { PASS(); __builtin_amdgcn_s_sleep(PSL); }
#pragma unroll 1
  for (unsigned k = 0; k < SPIN_CAP; ++k) {
    PASS();
    asm volatile("s_waitcnt vmcnt(%2)" : "+v"(r0), "+v"(r1) : "i"((PD_ - 1) * 2) : "memory");
    if (TSH == 16) { if (__all((__builtin_amdgcn_perm(r0[3], r0[1], 0x07060302u) == tag) & (__builtin_amdgcn_perm(r1[3], r1[1], 0x07060302u) == tag))) break; }
    else if (TSH < 0) { if (__all((lowbytes(r0) == tag) & (lowbytes(r1) == tag))) break; }
    else if (__all((((r0[1] ^ tag) | (r0[3] ^ tag) | (r1[1] ^ tag) | (r1[3] ^ tag)) >> (TSH < 0 ? 0 : TSH)) == 0u)) break;
    if (PD_ > 1) __builtin_amdgcn_s_sleep(PSL);
  }
#undef PASS
  asm volatile("s_nop 3 ; POLL_END" : "+v"(r0), "+v"(r1));
}
__device__ __forceinline__ void poll_done(v4u& r0, v4u& r1) { asm volatile("s_waitcnt vmcnt(0)" : "+v"(r0), "+v"(r1) :: "memory"); }

#define STAMP(i) do { } while (0)
#define DUMP_STAMPS() do { } while (0)
#define LD128(off) __builtin_amdgcn_raw_buffer_load_b128(rw, (off), 0, AUX_SC1)
#define ST128(v, off) __builtin_amdgcn_raw_buffer_store_b128((v), rw, (off), 0, AUX_SC1)
#define SPIN_LOOP _Pragma("unroll 1") for (unsigned spins = 0; spins < SPIN_CAP; ++spins)
#define NOHOIST() asm volatile("" ::: "memory")

__global__ void __launch_bounds__(NTHR, 2) fused_block(const u16* x_, const u16* kc_, const u16* vc_, const u16* wq_, unsigned char* ws_,
                                                       const u16* wo_, const u16* wgate_, const u16* wup_, const u16* wdown_, u16* out_, Plan plan_ TUNE_PARAM) {
  typedef unsigned long long u64x8 __attribute__((ext_vector_type(8)));
  typedef unsigned long long u64x2 __attribute__((ext_vector_type(2)));
  u64x8 ka; u64x2 kb2; unsigned pe;
  asm volatile("s_load_dwordx16 %0, %3, 0x0\n\ts_load_dwordx4 %1, %3, 0x40\n\ts_load_dword %2, %3, %4 offset:0x50"
               : "=&s"(ka), "=&s"(kb2), "=&s"(pe) : "s"(__builtin_amdgcn_kernarg_segment_ptr()), "s"((blockIdx.x >> 3) * 4));
  __shared__ __attribute__((aligned(16))) unsigned char smem[L_BYTES];
  LAS unsigned char* L = (LAS unsigned char*)smem;
  STAMP(0);
  const unsigned tstart = (unsigned)__builtin_amdgcn_s_memrealtime();
  const int wg = blockIdx.x, xcd = wg & 7, idx = wg >> 3;
  const int kvh = xcd, b = idx & 1, sp = idx >> 1;
  int lane = threadIdx.x & 63;
  const int w = __builtin_amdgcn_readfirstlane(threadIdx.x >> 6);
  int c = lane & 15, g = lane >> 4;
  if (w == 4 && !(ABL & 32)) {
    unsigned long long pc;
    asm volatile("s_getpc_b64 %0" : "=s"(pc));
    if ((lane * 32 + idx) * 128 < CODE_BYTES) {
      unsigned keep;
      asm volatile("s_mov_b32 %0, m0\n\ts_mov_b32 m0, %3\n\ts_nop 0\n\tglobal_load_lds_dword %1, %2\n\ts_mov_b32 m0, %0"
                   : "=&s"(keep) : "v"((lane * 32 + idx) * 128), "s"(pc & ~255ull), "s"((unsigned)(uintptr_t)L + L_PF) : "memory");
    }
  }
  int koff = (c >> 2) * 1024 + g * 64 + (c & 3) * 16;
  unsigned pbase = (unsigned)(b * HKV + kvh) * (SK * DH * 2);
  asm volatile("s_waitcnt lgkmcnt(0)" : "+s"(ka), "+s"(kb2), "+s"(pe), "+v"(koff), "+s"(pbase));
  const u16 *x = (const u16*)ka[0], *wq = (const u16*)ka[3], *wo = (const u16*)ka[5], *wgate = (const u16*)ka[6], *wup = (const u16*)ka[7], *wdown = (const u16*)kb2[0];
  unsigned char* ws = (unsigned char*)ka[4];
  u16* out = (u16*)kb2[1];
  const int tile0 = pe & 2047, ntile = (pe >> 11) & 255;
  const __amdgpu_buffer_rsrc_t rk = __builtin_amdgcn_make_buffer_rsrc((void*)(ka[1] + pbase), 0, SK * DH * 2, 0x00020000);
  const unsigned sbase = (unsigned)(tile0 + w) * 4096u;
  v4u kb[NPRE + 3][4];
#define SB() __builtin_amdgcn_sched_barrier(0)
#define ISSUEK(d, j) do { _Pragma("unroll") for (int s = 0; s < 4; ++s) { \
      if (!HEAVY(d, s)) kb[d][s] = __builtin_amdgcn_raw_buffer_load_b128(rk, koff + s * 256, sbase + (unsigned)(j) * 16384u, AUX_NT); \
      else { const v2u lo_ = __builtin_amdgcn_raw_buffer_load_b64(rk, koff + s * 256, sbase + (unsigned)(j) * 16384u, AUX_NT); NOHOIST(); SB(); \
             const v2u hi_ = __builtin_amdgcn_raw_buffer_load_b64(rk, koff + s * 256 + 8, sbase + (unsigned)(j) * 16384u, AUX_NT); NOHOIST(); \
             kb[d][s] = v4u{lo_[0], lo_[1], hi_[0], hi_[1]}; } } SB(); } while (0)
  const int role = (pe >> 19) & 7, rix = pe >> 22;
  const bool qrole = role == 4;
#define CI() ({ int r_ = rix; asm volatile("" : "+s"(r_)); const int cn_ = xcd * 5 + r_ - (xcd < 7 ? 2 : 0); (xcd < 7 && r_ < 2) ? 8 * xcd + 6 + r_ : cn_ + 2 * ((cn_ * 43) >> 8); })
  const int bi = xcd * 8 + rix, rj = xcd * 4 + rix, qi = bi;
  const __amdgpu_buffer_rsrc_t rw = __builtin_amdgcn_make_buffer_rsrc((void*)ws, 0, WS_BYTES, 0x00020000);
  const unsigned lds0 = (unsigned)(uintptr_t)L;
  LAS unsigned char* S = L + L_S;
#define WAIT_UNTIL(t, slp) do { while ((int)((unsigned)__builtin_amdgcn_s_memrealtime() - (tstart + (unsigned)(t))) < 0) __builtin_amdgcn_s_sleep(slp); } while (0)
  int q4 = (lane >> 2) & 3, p4 = lane & 3;
  constexpr float SCALE = 0.088388347648318440f, LOG2E = 1.4426950408889634f;
  unsigned tag;

  if (w == 4) {
    __builtin_amdgcn_s_setprio(3);
#define TOUCH() \
    {     \
      const int ci = CI(); \
      const unsigned long long vb_ = ka[2] + pbase + (unsigned)tile0 * 4096u, ke_ = ka[1] + pbase + (unsigned)(tile0 + ntile) * 4096u - 128u; \
      const unsigned long long wa_ = role == 1 ? ka[6] + ci * 64 : role == 2 ? ka[5] + bi * 65536 : kb2[1]; \
      const unsigned long long ad = lane == 0 ? vb_ : lane == 1 ? vb_ + (unsigned)ntile * 4096u - 128u : lane == 2 ? ke_ : lane == 3 ? wa_ \
                                  : lane == 4 ? wa_ + 511 * F * 2 : lane == 5 ? ka[7] + ci * 64 : lane == 6 ? ka[7] + ci * 64 + 511 * F * 2 : kb2[0] + ci * 32768; \
      if (lane < (role == 1 ? 8 : 4)) { \
        unsigned keep; \
        asm volatile("s_mov_b32 %0, m0\n\ts_mov_b32 m0, %2\n\ts_nop 0\n\tglobal_load_lds_dword %1, off nt\n\ts_mov_b32 m0, %0" : "=&s"(keep) : "v"(ad), "s"(lds0 + L_PF) : "memory"); \
      } \
    }
    if (qrole) {
      const v4u x0 = *(const GAS v4u*)(x + lane * 8), x1 = *(const GAS v4u*)(x + D + lane * 8);
      tag = __builtin_amdgcn_raw_buffer_load_b32(rw, OFF_EPOCH, 0, AUX_SC1) + 1u;
      const v4u n0 = ln_row(x0), n1 = ln_row(x1);
      *(LAS v4u*)(S + L_XN + lane * 16) = n0; *(LAS v4u*)(S + L_XN + 1024 + lane * 16) = n1;
      STAMP(9);
      barrier();
      if (TOUCH_EARLY) TOUCH();
      barrier();
      const f32x2 s = (*(LAS f32x2*)(S + L_RED + lane * 8) + *(LAS f32x2*)(S + L_RED + 512 + lane * 8)) + (*(LAS f32x2*)(S + L_RED + 1024 + lane * 8) + *(LAS f32x2*)(S + L_RED + 1536 + lane * 8));
      __builtin_amdgcn_raw_buffer_store_b64(v2u{cvtpk(s[0], s[1]), tag}, rw, OFF_Q + ((lane >> 5) * 2048 + qi * 32 + (lane & 31)) * 8, 0, AUX_SC1);
    } else {
      if (TOUCH_EARLY) TOUCH();
      tag = __builtin_amdgcn_raw_buffer_load_b32(rw, OFF_EPOCH, 0, AUX_SC1) + 1u;
    }
    STAMP(1);
    if (lane == 0) *(LAS unsigned*)(L + L_TAG) = tag;
    {
      const int qo = OFF_Q + (b * 2048 + kvh * 256) * 8 + lane * 16;
      v4u q0, q1;
      bool ok0 = false, ok1 = false;
      WAIT_UNTIL(T_P, 2);
      SPIN_LOOP {
        NOHOIST();
        if (!ok0) q0 = LD128(qo);
        if (!ok1) q1 = LD128(qo + 1024);
        ok0 = tagged(q0, tag); ok1 = tagged(q1, tag);
        if (__all(ok0 & ok1)) break;
      }
      *(LAS v2u*)(L + L_QF + lane * 8) = v2u{q0[0], q0[2]};
      *(LAS v2u*)(L + L_QF + 512 + lane * 8) = v2u{q1[0], q1[2]};
      STAMP(2);
      barrier();
    }
    if (!TOUCH_EARLY) TOUCH();
    asm volatile("s_waitcnt vmcnt(0)" ::: "memory");
    return;
  }
  const int nstep = (ntile - w + 3) >> 2;
  const __amdgpu_buffer_rsrc_t rv = __builtin_amdgcn_make_buffer_rsrc((void*)(ka[2] + pbase), 0, SK * DH * 2, 0x00020000);
#define VMWAIT(n) do { asm volatile("s_waitcnt vmcnt(%0)" :: "i"(n) : "memory"); SB(); } while (0)
#define RELANE() do { asm volatile("v_mbcnt_lo_u32_b32 %0, -1, 0\n\tv_mbcnt_hi_u32_b32 %0, -1, %0" : "=v"(lane)); c = lane & 15; g = lane >> 4; } while (0)
  int wp = 0;
#define WUPTO(n, pace) do { _Pragma("unroll 1") while (wp < (n)) { \
      const u16* src_ = role == 2 ? wo + (size_t)(bi * 64 + 4 * wp + g) * D + 128 * w + 8 * c \
                      : wp < 16 ? (wp < 8 ? wgate : wup) + (size_t)(128 * w + 16 * (wp & 7) + (lane >> 2)) * F + ci * 32 + (((lane & 3) ^ ((lane >> 4) & 2)) * 8) \
                                : wdown + (size_t)(ci * 32 + 4 * (wp & 7) + g) * D + 128 * w + 8 * c; \
      VMWAIT(pace); glds16(src_, lds0 + L_W + w * WREG + wp * 1024); ++wp; } } while (0)
  if (qrole) {
    const int r8 = lane >> 3;
    const u16* p = wq + (size_t)(128 * w + r8) * (HQ * DH) + qi * 64;
    const int ch0 = ((lane & 7) ^ (r8 & 2)) * 8;
#pragma unroll 1
    for (int t = 0; t < 16; t += 2) {
      glds16(p + ch0, lds0 + L_W + w * WREG + t * 1024);
      glds16(p + 8 * (HQ * DH) + (ch0 ^ 32), lds0 + L_W + w * WREG + t * 1024 + 1024);
      p += 16 * (HQ * DH);
    }
    STAMP(7);
  }
  STAMP(11);
  if (!qrole) { ISSUEK(0, 0); ISSUEK(1, 1); }
  else {
    const int sw = (q4 & 2) | ((g & 1) << 2);
    v4u fr[16];
#define QFRAGS(ks) do { _Pragma("unroll") for (int t = 0; t < 4; ++t) { \
        LAS unsigned char* a = L + L_W + w * WREG + (ks) * 4096 + g * 1024 + q4 * 128 + (p4 & 1) * 8 + (((2 * t + (p4 >> 1)) ^ sw) * 16); fr[(ks) * 4 + t] = tr_frag(a, a + 512); } } while (0)
    VMWAIT(8); QFRAGS(0); QFRAGS(1); ISSUEK(0, 0);
    VMWAIT(4 + NINST(0)); QFRAGS(2); ISSUEK(1, 1);
    barrier();
    f32x4 qa[4];
    v4u xb[4];
#pragma unroll
    for (int ks = 0; ks < 4; ++ks) xb[ks] = *(LAS v4u*)(S + L_XN + (c & 1) * 1024 + (128 * w + 32 * ks + 8 * g) * 2);
#pragma unroll
    for (int t = 0; t < 4; ++t) qa[t] = f32x4{0.f, 0.f, 0.f, 0.f};
#pragma unroll
    for (int ks = 0; ks < 3; ++ks)
#pragma unroll
      for (int t = 0; t < 4; ++t) qa[t] = mfma32(fr[ks * 4 + t], xb[ks], qa[t]);
    VMWAIT(NINST(0) + NINST(1)); STAMP(8); QFRAGS(3);
#pragma unroll
    for (int t = 0; t < 4; ++t) qa[t] = mfma32(fr[12 + t], xb[3], qa[t]);
    if (c < 2) {
#pragma unroll
      for (int t = 0; t < 4; ++t) *(LAS f32x4*)(S + L_RED + w * 512 + c * 256 + t * 64 + g * 16) = qa[t];
    }
    STAMP(1);
    barrier();
  }
  const int rem = nstep & 3, remk = (nstep - NPRE - NLT) & 3;
#pragma unroll
  for (int d = 2; d < NPRE; ++d) { VMWAIT(NINST(d - 1)); ISSUEK(d, d); }
  {
    const u16* kt = (const u16*)(ka[1] + pbase + sbase + NPRE * 16384u);
    VMWAIT(NINST(NPRE - 1));
#pragma unroll 1
    for (int t = 0; t < NLT; t += 2) {
#pragma unroll
      for (int s = 0; s < 4; ++s) glds16(kt + (c * 256 + g * 16 + s * 64) / 2, lds0 + L_W + w * WREG + t * 4096 + s * 1024);
      VMWAIT(4);
#pragma unroll
      for (int s = 0; s < 4; ++s) glds16(kt + (16384 + koff + s * 256) / 2, lds0 + L_W + w * WREG + t * 4096 + 4096 + s * 1024);
      if (t + 2 < NLT) VMWAIT(4);
      kt += 16384;
    }
  }
  STAMP(10);
  barrier();
  STAMP(2);
  const v4u qf = *(LAS v4u*)(L + L_QF + (c >> 2) * 256 + g * 64 + (c & 3) * 16);
  tag = __builtin_amdgcn_readfirstlane(*(LAS unsigned*)(L + L_TAG));

  LAS unsigned char* scp = L + L_SC + w * 5120 + g * 32 + (c >> 2) * 8;
  float m = -INFINITY;
  f32x4 sc;
#define QK(d) do { \
    _Pragma("unroll") for (int s = 0; s < 4; ++s) { const f32x4 dd = mfma32(kb[d][s], qf, f32x4{0.f, 0.f, 0.f, 0.f}); sc[s] = (c & 2) ? ((c & 1) ? dd[3] : dd[2]) : ((c & 1) ? dd[1] : dd[0]); } \
    asm volatile("" : "+v"(sc) :: "memory"); SB(); } while (0)
#define SCORE(d, j) do { \
    _Pragma("unroll") for (int s = 0; s < 4; ++s) { float t_ = sc[s]; t_ += dpp<XOR1>(t_); t_ += dpp<XOR2>(t_); sc[s] = t_; } \
    const unsigned r01 = cvtpk(sc[0], sc[1]), r23 = cvtpk(sc[2], sc[3]); \
    const unsigned t01 = cvtpk(bflo(r01) * SCALE, bfhi(r01) * SCALE), t23 = cvtpk(bflo(r23) * SCALE, bfhi(r23) * SCALE); \
    m = vmax(vmax(m, vmax(bflo(t01), bfhi(t01))), vmax(bflo(t23), bfhi(t23))); \
    *(LAS v2u*)(scp + (j) * 128) = v2u{t01, t23}; asm volatile("" : "+v"(m)); SB(); } while (0)
#pragma unroll
  for (int d = 0; d < 4; ++d) { QK(d); ISSUEK(d, NPRE + NLT + remk + d); SCORE(d, d); }
#pragma unroll
  for (int d = 4; d < NPRE; ++d) { QK(d); SCORE(d, d); }
#pragma unroll
  for (int d = NPRE; d < NPRE + 3; ++d) if (d - NPRE < remk) ISSUEK(d, d + NLT);
  int j = NPRE + NLT + remk;
#pragma unroll 1
  do {
#pragma unroll
    for (int d = 0; d < 4; ++d) { QK(d); ISSUEK(d, j + 4 + d); SCORE(d, j + d); }
    j += 4;
  } while (j + 4 < nstep);
#pragma unroll
  for (int d = NPRE; d < NPRE + 3; ++d) if (d - NPRE < remk) { QK(d); SCORE(d, d + NLT); }
  const int voff = g * 1024 + c * 16;
  v2u vl[7][4], vh[7][4];
#define ISSUEV(d, j) do { _Pragma("unroll") for (int s = 0; s < 4; ++s) { \
      if (!HEAVY(d, s)) { const v4u t_ = __builtin_amdgcn_raw_buffer_load_b128(rv, voff + s * 256, sbase + (unsigned)(j) * 16384u, AUX_NT); vl[d][s] = v2u{t_[0], t_[1]}; vh[d][s] = v2u{t_[2], t_[3]}; } \
      else { vl[d][s] = __builtin_amdgcn_raw_buffer_load_b64(rv, voff + s * 256, sbase + (unsigned)(j) * 16384u, AUX_NT); NOHOIST(); SB(); \
             vh[d][s] = __builtin_amdgcn_raw_buffer_load_b64(rv, voff + s * 256 + 8, sbase + (unsigned)(j) * 16384u, AUX_NT); NOHOIST(); } } SB(); } while (0)
#pragma unroll
  for (int d = 0; d < 4; ++d) { QK(d); ISSUEV(d, d); SCORE(d, j + d); }
#pragma unroll
  for (int d = 4; d < 7; ++d) if (d - 4 < rem) ISSUEV(d, d);
  {
    const int he = (g * 64 + (c & 3) * 16 + (c >> 2) * 4) * 16;
#pragma unroll 1
    for (int t = 0; t < NLT; t += 2) {
#pragma unroll
      for (int s = 0; s < 4; ++s) kb[4][s] = *(LAS v4u*)(L + L_W + w * WREG + t * 4096 + he + s * 16);
#pragma unroll
      for (int s = 0; s < 4; ++s) kb[5][s] = *(LAS v4u*)(L + L_W + w * WREG + t * 4096 + 4096 + s * 1024 + lane * 16);
      QK(4); SCORE(4, NPRE + t); QK(5); SCORE(5, NPRE + t + 1);
    }
  }
  STAMP(9);
  m = sw16_max(m, m); m = sw32_max(m, m);
  const float mlog = m * LOG2E;
  const unsigned m0 = (c & 3) == 0 ? 0xffffu : (c & 3) == 1 ? 0xffff0000u : 0u;
  const unsigned m1 = (c & 3) == 2 ? 0xffffu : (c & 3) == 3 ? 0xffff0000u : 0u;
  f32x4 acc0 = {0.f, 0.f, 0.f, 0.f}, acc1 = {0.f, 0.f, 0.f, 0.f};
  float l = 0.f;
#define PCALC(bb, j) do { \
    const v2u t_ = *(LAS v2u*)(scp + (j) * 128); \
    const float sv[4] = {bflo(t_[0]), bfhi(t_[0]), bflo(t_[1]), bfhi(t_[1])}; \
    _Pragma("unroll") for (int i = 0; i < 4; ++i) { const float p_ = __builtin_amdgcn_exp2f(fmaf(sv[i], LOG2E, -mlog)); const unsigned pp = cvtpk(p_, p_); l += bflo(pp); bb[i] = v2u{pp & m0, pp & m1}; } } while (0)
#define PMFMA(d, bb) do { SB(); \
    _Pragma("unroll") for (int i = 0; i < 4; ++i) { acc0 = mfma16(vl[d][i], bb[i], acc0); acc1 = mfma16(vh[d][i], bb[i], acc1); } \
    asm volatile("" : "+v"(acc0), "+v"(acc1) :: "memory"); SB(); } while (0)
#define PV(d, j) do { v2u bb_[4]; PCALC(bb_, j); PMFMA(d, bb_); } while (0)
#pragma unroll
  for (int d = 0; d < 4; ++d) { PV(d, d); ISSUEV(d, 4 + rem + d); }
#pragma unroll
  for (int d = 4; d < 7; ++d) if (d - 4 < rem) PV(d, d);
  j = 4 + rem;
#pragma unroll 1
  do {
#pragma unroll
    for (int d = 0; d < 4; ++d) { PV(d, j + d); ISSUEV(d, j + 4 + d); }
    j += 4;
  } while (j + 4 + 4 * NLR < nstep);
  unsigned bal[2] = {0u, 0u};
  const int blo = (lane & 31) * 128;
#pragma unroll
  for (int r = 0; r < NLR; ++r) {
#pragma unroll
    for (int d = 0; d < 4; ++d) {
      if (r > 0 && !(d & 1)) asm volatile("" :: "v"(bal[d >> 1]));
      PV(d, j + d);
#pragma unroll
      for (int s = 0; s < 4; ++s) { const v4u t_ = __builtin_amdgcn_raw_buffer_load_b128(rv, voff + s * 256, sbase + (unsigned)(j + 4 + d) * 16384u, AUX_NT); vl[d][s] = v2u{t_[0], t_[1]}; vh[d][s] = v2u{t_[2], t_[3]}; }
      SB();
      if (!(d & 1)) {
        const bool late = (int)((unsigned)__builtin_amdgcn_s_memrealtime() - (tstart + T_L)) > 0;
        bal[d >> 1] = __builtin_amdgcn_raw_buffer_load_b32(rv, late ? 0 : blo, sbase + (unsigned)(j + 4 + d) * 16384u, AUX_NT); SB();
      }
    }
    j += 4;
  }
  asm volatile("v_mbcnt_lo_u32_b32 %0, -1, 0\n\tv_mbcnt_hi_u32_b32 %0, -1, %0" : "=v"(lane));
  c = lane & 15; g = lane >> 4; q4 = (lane >> 2) & 3; p4 = lane & 3;
  const int tid = w * 64 + lane;
#define PIN(x) asm volatile("" : "+v"(x))
  const unsigned tagh = mixtag(tag);
  v2u lb[4][4];
#pragma unroll
  for (int d = 0; d < 4; ++d) PCALC(lb[d], j + d);
  l = sw16_sum(l, l); l = sw32_sum(l, l);
  *(LAS f32x2*)(S + L_RED + (c >> 2) * 32 + w * 8) = f32x2{mlog, l};
  barrier();
  const int hd = 2 * (w & 1) + (lane >> 5);
  float fw; unsigned t1w, t3w;
  {
#define MERGE_ML(h, M_, L_) do { const f32x4 u0_ = *(LAS f32x4*)(S + L_RED + (h) * 32), u1_ = *(LAS f32x4*)(S + L_RED + (h) * 32 + 16); \
      M_ = vmax(vmax(u0_[0], u0_[2]), vmax(u1_[0], u1_[2])); \
      L_ = fmaf(__builtin_amdgcn_exp2f(u0_[2] - M_), u0_[3], __builtin_amdgcn_exp2f(u0_[0] - M_) * u0_[1]) + fmaf(__builtin_amdgcn_exp2f(u1_[2] - M_), u1_[3], __builtin_amdgcn_exp2f(u1_[0] - M_) * u1_[1]); } while (0)
    float M1, L1, M2, L2;
    MERGE_ML(c >> 2, M1, L1); MERGE_ML(hd, M2, L2);
    fw = __builtin_amdgcn_exp2f(mlog - M1) * __builtin_amdgcn_rcpf(L1);
    const unsigned lse = f2u(M2 + __builtin_amdgcn_logf(L2));
    t1w = __builtin_amdgcn_perm(tagh, lse, 0x05040100u); t3w = __builtin_amdgcn_perm(tagh, lse, 0x07060302u);
  }
  unsigned mw = lds0 + L_S + L_MRG + w * 2048 + lane * 16, mr = lds0 + L_S + L_MRG + ((lane >> 2) & 1) * 1024 + (((lane >> 3) & 3) * 16 + hd * 4 + (lane & 3)) * 16;
  int pao = OFF_PART + ((b * HQ + kvh * 4 + hd) * NSPLIT + sp) * 512 + (lane & 31) * 16;
  PIN(mw); PIN(mr); PIN(pao); PIN(fw); PIN(t1w); PIN(t3w);
#pragma unroll
  for (int d = 0; d < 4; ++d) { PMFMA(d, lb[d]); if (d == 2) asm volatile("" :: "v"(bal[0]), "v"(bal[1])); }
  STAMP(3);
  *(LAS f32x4*)mw = acc0 * fw; *(LAS f32x4*)(mw + 1024) = acc1 * fw;
  barrier();
  if (w < 2) {
    const f32x4 o = (*(LAS f32x4*)mr + *(LAS f32x4*)(mr + 2048)) + (*(LAS f32x4*)(mr + 4096) + *(LAS f32x4*)(mr + 6144));
    ST128((v4u{cvtpk(o[0], o[1]), t1w, cvtpk(o[2], o[3]), t3w}), pao);
  }
  STAMP(4);
  const int ci = CI();
  WUPTO(role == 1 ? 24 : role == 2 ? 16 : 0, 40);
  unsigned dm0 = (c & 7) == 0 ? 0xffffu : (c & 7) == 1 ? 0xffff0000u : 0u, dm1 = (c & 7) == 2 ? 0xffffu : (c & 7) == 3 ? 0xffff0000u : 0u,
           dm2 = (c & 7) == 4 ? 0xffffu : (c & 7) == 5 ? 0xffff0000u : 0u, dm3 = (c & 7) == 6 ? 0xffffu : (c & 7) == 7 ? 0xffff0000u : 0u;
  unsigned tb0 = tagh & 0xffu, tb1 = (tagh >> 8) & 0xffu, tb2 = (tagh >> 16) & 0xffu, tb3 = tagh >> 24;
#define PUBLISH_ACC(acc, off) ST128((v4u{PK24(acc[0], tb0), PK24(acc[1], tb1), PK24(acc[2], tb2), PK24(acc[3], tb3)}), (off))

  if (role == 2) {
    unsigned wra = lds0 + L_W + w * WREG + lane * 16;
    PIN(wra);
    const int head = bi >> 1;
    const int po = OFF_PART + (((lane >> 5) * HQ + head) * NSPLIT + ((lane >> 2) & 7)) * 512 + (bi & 1) * 256 + w * 64 + (lane & 3) * 16;
    unsigned ow = lds0 + L_S + L_OB + ((g >> 1) * 4 + (lane & 3)) * 64 + (8 * (w >> 1) + 2 * ((g & 1) + ((lane & 4) >> 1)) + (w & 1)) * 4, orr = lds0 + L_S + L_OB + ((c >> 3) * 4 + g) * 64;
    int so_ = OFF_P1 + bi * 4096 + tid * 16;
    f32x4 acc = {0.f, 0.f, 0.f, 0.f};
    PIN(ow); PIN(orr); PIN(so_); PIN(dm0); PIN(dm1); PIN(dm2); PIN(dm3); PIN(tb0); PIN(tb1); PIN(tb2); PIN(tb3); PIN(acc);
    v4u a0, a1;
    poll2<PD1, 16>(ws, po, po + 8 * 512, tagh, a0, a1);
    STAMP(5);
    v4u wr[16];
#pragma unroll
    for (int i = 0; i < 16; ++i) wr[i] = *(LAS v4u*)(wra + i * 1024);
    SB();
    const float l0 = u2f(__builtin_amdgcn_perm(a0[3], a0[1], 0x05040100u)), l1 = u2f(__builtin_amdgcn_perm(a1[3], a1[1], 0x05040100u));
    float xm = vmax(l0, l1);
    xm = vmax(vmax(xm, dpp<ROR4>(xm)), vmax(dpp<ROR8>(xm), dpp<ROR12>(xm)));
    xm = sw16_max(xm, xm);
    const float w0 = __builtin_amdgcn_exp2f(l0 - xm), w1 = __builtin_amdgcn_exp2f(l1 - xm);
    float u0 = sw16_sum(fmaf(w1, bflo(a1[0]), w0 * bflo(a0[0])), fmaf(w1, bfhi(a1[0]), w0 * bfhi(a0[0])));
    float u1 = sw16_sum(fmaf(w1, bflo(a1[2]), w0 * bflo(a0[2])), fmaf(w1, bfhi(a1[2]), w0 * bfhi(a0[2])));
    float ws_ = w0 + w1;
    ws_ = sw16_sum(ws_, ws_);
    u0 += dpp<ROR8>(u0); u1 += dpp<ROR8>(u1); ws_ += dpp<ROR8>(ws_);
    u0 += dpp<ROR4>(u0); u1 += dpp<ROR4>(u1); ws_ += dpp<ROR4>(ws_);
    const float uo = ((lane & 4) ? u1 : u0) * __builtin_amdgcn_rcpf(ws_);
    *(LAS unsigned*)ow = cvtpk(uo, uo);
    barrier();
    const v4u pv0 = *(LAS v4u*)orr, pv1 = *(LAS v4u*)(orr + 16), pv2 = *(LAS v4u*)(orr + 32), pv3 = *(LAS v4u*)(orr + 48);
    diag_chain_regs(acc, wr, pv0, pv1, dm0, dm1, dm2, dm3);
    diag_chain_regs(acc, wr + 8, pv2, pv3, dm0, dm1, dm2, dm3);
    mfma_drain(acc);
    PUBLISH_ACC(acc, so_);
    STAMP(6);
    poll_done(a0, a1);
  } else if (role == 3) {
    const int rb = rj & 1, col0 = (rj >> 1) * 32;
    unsigned xp = *(const GAS unsigned*)(x + rb * D + col0 + 2 * c);
    asm volatile("" : "+v"(xp));
    const int pp = lane >> 3;
    float h0, h1;
    unsigned rw_ = lds0 + L_S + L_RR + w * 128 + (8 * g + (lane & 7)) * 4, rr_ = lds0 + L_S + L_RR + c * 8;
    int hbo = OFF_HB + ((w & 1) * 4 + g) * 4096 + rb * 2048 + col0 * 4 + c * 8;
    GAS unsigned* op = (GAS unsigned*)(out + rb * D + col0 + 2 * c);
    PIN(rw_); PIN(rr_); PIN(hbo); PIN(op);
    {
      const int po = OFF_P1 + (16 * w + pp) * 4096 + rj * 128 + (lane & 7) * 16;
      v4u a0, a1;
      poll2<PD2, -1>(ws, po, po + 8 * 4096, tagh, a0, a1);
      STAMP(5);
      float u = sw16_sum(sw32_sum(UP24(a0[0]) + UP24(a1[0]), UP24(a0[2]) + UP24(a1[2])), sw32_sum(UP24(a0[1]) + UP24(a1[1]), UP24(a0[3]) + UP24(a1[3])));
      u += dpp<ROR8>(u);
      *(LAS float*)rw_ = u;
      barrier();
      const f32x2 s = (*(LAS f32x2*)rr_ + *(LAS f32x2*)(rr_ + 128)) + (*(LAS f32x2*)(rr_ + 256) + *(LAS f32x2*)(rr_ + 384));
      const unsigned at = cvtpk(s[0], s[1]);
      const unsigned hp = cvtpk(bflo(xp) + bflo(at), bfhi(xp) + bfhi(at));
      h0 = bflo(hp); h1 = bfhi(hp);
      if (w < 2) __builtin_amdgcn_raw_buffer_store_b64(v2u{hp, tag}, rw, hbo, 0, AUX_SC1);
      STAMP(6);
      poll_done(a0, a1);
    }
    {
      const int po = OFF_P2 + (14 * w + pp) * 4096 + rj * 128 + (lane & 7) * 16;
      v4u a0, b1;
      poll2<PD4, -1>(ws, po, po + (pp < 6 ? 8 * 4096 : 0), tagh, a0, b1);
      STAMP(7);
      const unsigned km = pp < 6 ? 0xffffff00u : 0u;
      float u = sw16_sum(sw32_sum(UP24(a0[0]) + u2f(b1[0] & km), UP24(a0[2]) + u2f(b1[2] & km)), sw32_sum(UP24(a0[1]) + u2f(b1[1] & km), UP24(a0[3]) + u2f(b1[3] & km)));
      u += dpp<ROR8>(u);
      *(LAS float*)(rw_ + 512) = u;
      barrier();
      if (tid < 16) {
        const f32x2 s = (*(LAS f32x2*)(rr_ + 512) + *(LAS f32x2*)(rr_ + 640)) + (*(LAS f32x2*)(rr_ + 768) + *(LAS f32x2*)(rr_ + 896));
        const unsigned dn = cvtpk(s[0], s[1]);
        *op = cvtpk(h0 + bflo(dn), h1 + bfhi(dn));
        if (rj == 0 && tid == 0) __builtin_amdgcn_raw_buffer_store_b32(tag, rw, OFF_EPOCH, 0, AUX_SC1);
      }
      STAMP(8);
      poll_done(a0, b1);
    }
  } else if (role == 1) {
    v4u fg[8], fu[8], wd[8];
    LAS unsigned char* fa = L + L_W + w * WREG + (8 * g + q4) * 64 + (p4 & 1) * 8;
    VMWAIT(16);
#pragma unroll
    for (int ks = 0; ks < 4; ++ks)
#pragma unroll
      for (int t = 0; t < 2; ++t) { LAS unsigned char* a = fa + 2048 * ks + (((2 * t + (p4 >> 1)) ^ ((g & 1) << 1)) * 16); fg[ks * 2 + t] = tr_frag(a, a + 256); }
    VMWAIT(8);
    STAMP(10);
#pragma unroll
    for (int ks = 0; ks < 4; ++ks)
#pragma unroll
      for (int t = 0; t < 2; ++t) { LAS unsigned char* a = fa + 8192 + 2048 * ks + (((2 * t + (p4 >> 1)) ^ ((g & 1) << 1)) * 16); fu[ks * 2 + t] = tr_frag(a, a + 256); }
    f32x4 ag[2], au[2];
    unsigned ro = lds0 + L_S + L_RED + ((c >> 2) & 1) * 1024 + ((c & 1) * 32 + (c & 2) * 8 + 4 * g) * 16 + w * 4;
    unsigned fo = lds0 + L_S + L_RED + ((c >> 3) * 32 + 4 * (c & 3) + g) * 16;
#define GU_MFMA(XB) do { _Pragma("unroll") for (int t = 0; t < 2; ++t) { ag[t] = f32x4{0.f, 0.f, 0.f, 0.f}; au[t] = f32x4{0.f, 0.f, 0.f, 0.f}; } \
      _Pragma("unroll") for (int ks = 0; ks < 4; ++ks) { \
        _Pragma("unroll") for (int t = 0; t < 2; ++t) { ag[t] = mfma32(fg[ks * 2 + t], XB, ag[t]); au[t] = mfma32(fu[ks * 2 + t], XB, au[t]); } } } while (0)
#define GU_STORE() do { const f32x4 sel = (c & 4) ? ((c & 2) ? au[1] : au[0]) : ((c & 2) ? ag[1] : ag[0]); \
      _Pragma("unroll") for (int i = 0; i < 4; ++i) *(LAS float*)(ro + i * 16) = sel[i]; } while (0)
#define SUM4(p_) ({ const f32x2 t_ = f32x2{p_[0], p_[1]} + f32x2{p_[2], p_[3]}; float r_ = t_[0] + t_[1]; PIN(r_); r_; })
    GU_MFMA((v4u{0x3f803f80u, 0x3f803f80u, 0x3f803f80u, 0x3f803f80u}));
    GU_STORE();
    barrier();
    float cg0, cg1, cu0, cu1;
    { const f32x4 p0 = *(LAS f32x4*)fo, p1 = *(LAS f32x4*)(fo + 256), p2 = *(LAS f32x4*)(fo + 1024), p3 = *(LAS f32x4*)(fo + 1280); cg0 = SUM4(p0); cg1 = SUM4(p1); cu0 = SUM4(p2); cu1 = SUM4(p3); }
    unsigned xw = lds0 + L_S + L_XN + (w & 1) * 1024 + lane * 16, xr = lds0 + L_S + L_XN + (c & 1) * 1024 + (128 * w + 8 * g) * 2, sw_ = lds0 + L_S + L_ST + w * 8, sr = lds0 + L_S + L_ST + (c >> 3) * 8;
    int so_ = OFF_P2 + ci * 4096 + tid * 16;
    f32x4 acc = {0.f, 0.f, 0.f, 0.f};
    unsigned wda = lds0 + L_W + w * WREG + 16384 + lane * 16;
    PIN(wda); PIN(ro); PIN(fo); PIN(xw); PIN(xr); PIN(sw_); PIN(sr); PIN(so_); PIN(dm0); PIN(dm1); PIN(dm2); PIN(dm3); PIN(tb0); PIN(tb1); PIN(tb2); PIN(tb3); PIN(acc);
    v4u ha = {0u, 0u, 0u, 0u}, hb = ha;
    if (w < 2) {
      const int ho = OFF_HB + xcd * 4096 + w * 2048 + lane * 32;
      poll2<PD3, 0>(ws, ho, ho + 16, tag, ha, hb);
      STAMP(5);
      *(LAS v4u*)xw = v4u{ha[0], ha[2], hb[0], hb[2]};
    } else VMWAIT(0);
    barrier();
    v4u xb[4];
#pragma unroll
    for (int ks = 0; ks < 4; ++ks) xb[ks] = *(LAS v4u*)(xr + 64 * ks);
#pragma unroll
    for (int i = 0; i < 8; ++i) wd[i] = *(LAS v4u*)(wda + i * 1024);
    __builtin_amdgcn_sched_barrier(0);
    const float pv = bflo(__builtin_amdgcn_readfirstlane(ha[0]));
    const float d0 = bflo(ha[0]) - pv, d1 = bfhi(ha[0]) - pv, d2 = bflo(ha[2]) - pv, d3 = bfhi(ha[2]) - pv, d4 = bflo(hb[0]) - pv, d5 = bfhi(hb[0]) - pv, d6 = bflo(hb[2]) - pv, d7 = bfhi(hb[2]) - pv;
    float s1 = ((d0 + d1) + (d2 + d3)) + ((d4 + d5) + (d6 + d7));
    float s2 = (fmaf(d1, d1, d0 * d0) + fmaf(d3, d3, d2 * d2)) + (fmaf(d5, d5, d4 * d4) + fmaf(d7, d7, d6 * d6));
    __builtin_amdgcn_sched_barrier(0);
    GU_MFMA(xb[ks]);
    {
      float t = sw32_sum(s1, s2);
      t = sw16_sum(t, t); t += dpp<ROR8>(t); t += dpp<ROR4>(t); t += dpp<XOR2>(t); t += dpp<XOR1>(t);
      const float md = u2f(__builtin_amdgcn_readlane(f2u(t), 0)) * (1.0f / D), q2 = u2f(__builtin_amdgcn_readlane(f2u(t), 32)) * (1.0f / D);
      const float var = vmax(fmaf(-md, md, q2), 0.f);
      const float rs = __builtin_amdgcn_rcpf(rbf(__builtin_amdgcn_sqrtf(rbf(rbf(var) + 1e-5f))));
      *(LAS f32x2*)sw_ = f32x2{rbf(pv + md), rs};
    }
    GU_STORE();
    barrier();
    const f32x2 st = *(LAS f32x2*)sr;
    const f32x4 p0 = *(LAS f32x4*)fo, p1 = *(LAS f32x4*)(fo + 256), p2 = *(LAS f32x4*)(fo + 1024), p3 = *(LAS f32x4*)(fo + 1280);
    __builtin_amdgcn_sched_barrier(0);
    const float G0 = SUM4(p0), G1 = SUM4(p1), U0 = SUM4(p2), U1 = SUM4(p3);
    const unsigned gp = cvtpk(fmaf(-st[0], cg0, G0) * st[1], fmaf(-st[0], cg1, G1) * st[1]);
    const unsigned up = cvtpk(fmaf(-st[0], cu0, U0) * st[1], fmaf(-st[0], cu1, U1) * st[1]);
    const float g0 = bflo(gp), g1 = bfhi(gp);
    const unsigned sg = cvtpk(g0 * __builtin_amdgcn_rcpf(1.0f + __builtin_amdgcn_exp2f(-g0 * LOG2E)), g1 * __builtin_amdgcn_rcpf(1.0f + __builtin_amdgcn_exp2f(-g1 * LOG2E)));
    const float av0 = bflo(sg) * bflo(up), av1 = bfhi(sg) * bfhi(up);
    diag_chain_quad(acc, wd, cvtpk(av0, av0), cvtpk(av1, av1), dm0, dm1, dm2, dm3);
    mfma_drain(acc);
    PUBLISH_ACC(acc, so_);
    STAMP(6);
    poll_done(ha, hb);
  }
  DUMP_STAMPS();
}

extern "C" void kernel_launch(void* const* d_in, const int* in_sizes, int n_in, void* d_out, int out_size, void* d_ws, size_t ws_size, hipStream_t stream) {
  fused_block<<<NWG, NTHR, 0, stream>>>((const u16*)d_in[0], (const u16*)d_in[1], (const u16*)d_in[2], (const u16*)d_in[3], (unsigned char*)d_ws,
                                        (const u16*)d_in[4], (const u16*)d_in[6], (const u16*)d_in[7], (const u16*)d_in[8], (u16*)d_out, make_plan() TUNE_ARG);
}
```
